```python
import torch

def _softmax(x, axis=-1):
    return torch.softmax(x, dim=axis)

def _gelu(x):
    return torch.nn.functional.gelu(x, approximate='tanh')

def _silu(x):
    return torch.nn.functional.silu(x)

def _sigmoid(x):
    return torch.sigmoid(x)

def _layernorm(x, axis=-1, eps=1e-05):
    m = x.mean(dim=axis, keepdim=True)
    v = x.var(dim=axis, keepdim=True, unbiased=False)
    return (x - m) / torch.sqrt(v + eps)

def _scatter_add(tgt, idx, src, axis=0):
    return tgt.clone().index_add_(axis, idx, src)

def _segment_sum(src, seg, n):
    out = torch.zeros((n,) + src.shape[1:], dtype=src.dtype)
    return out.index_add_(0, seg, src)

def _pad(x, pw):
    flat = tuple((v for pair in reversed(pw) for v in pair))
    return torch.nn.functional.pad(x, flat)
kernel_name = 'adaLN_cond__sliding_block_cached__glu_gated_reglu__prompt'

def setup_inputs():

    _w = lambda a: (lambda t: t.to(torch.bfloat16) if t.is_floating_point() else t)(torch.as_tensor(a))

    def _A_setup():
        B, Sq, D = (1, 2048, 640)
        Hq, Hkv, Sk, Dh = (32, 32, 2048, 128)
        assert Hq % Hkv == 0, (Hq, Hkv)
        return {'x': _w((lambda *s: torch.randn(*s, dtype=torch.bfloat16) * 0.05)(B, Sq, D)), 'k_cache': _w((lambda *s: torch.randn(*s, dtype=torch.bfloat16) * 1.6)(B, Hkv, Sk, Dh)), 'v_cache': _w((lambda *s: torch.randn(*s, dtype=torch.bfloat16) * 0.8)(B, Hkv, Sk, Dh)), 'wq': _w((lambda *s: torch.randn(*s, dtype=torch.bfloat16) * 1.6)(D, Hq * Dh) * D ** (-0.5)), 'wo': _w((lambda *s: torch.randn(*s, dtype=torch.bfloat16) * 0.8)(Hq * Dh, D) * (Hq * Dh) ** (-0.5))}
    _A_in = _A_setup()

    def _B_setup():
        B, S, D, F = (1, 2048, 640, 1728)
        return {'x': _w((lambda *s: torch.randn(*s, dtype=torch.bfloat16) * 0.05)(B, S, D)), 'W_gate': _w((lambda *s: torch.randn(*s, dtype=torch.bfloat16) * 0.4)(D, F) * (2.0 / D) ** 0.5), 'W_up': _w((lambda *s: torch.randn(*s, dtype=torch.bfloat16) * 0.4)(D, F) * (2.0 / D) ** 0.5), 'W_down': _w((lambda *s: torch.randn(*s, dtype=torch.bfloat16) * 0.4)(F, D) * (1.0 / F) ** 0.5)}
    _B_in = _B_setup()
    return {'A_x': _A_in['x'], 'A_k_cache': _A_in['k_cache'], 'A_v_cache': _A_in['v_cache'], 'A_wq': _A_in['wq'], 'A_wo': _A_in['wo'], 'B_x': _B_in['x'], 'B_W_gate': _B_in['W_gate'], 'B_W_up': _B_in['W_up'], 'B_W_down': _B_in['W_down'], 'c': _w((lambda *s: torch.randn(*s, dtype=torch.bfloat16) * 0.2)(1, 640) * 8.0), 'w_mod': _w((lambda *s: torch.randn(*s, dtype=torch.bfloat16) * 0.2)(640, 6 * 640) * 640 ** (-0.5) * 8.0)}

def reference(A_x, A_k_cache, A_v_cache, A_wq, A_wo, B_x, B_W_gate, B_W_up, B_W_down, c, w_mod):

    def _A_ref(x, k_cache, v_cache, wq, wo):
        B, Sq, D = x.shape
        Hq, Dh = (32, 128)
        Hkv, Sk = (k_cache.shape[1], k_cache.shape[2])
        W = min(4096, max(256, Sk // 4))
        rep = Hq // Hkv
        q = (lambda x, *d: x.permute(*d))((x @ wq).reshape(B, Sq, Hq, Dh), 0, 2, 1, 3)
        k = (lambda x, n, axis: torch.repeat_interleave(x, n, dim=axis))(k_cache[:, :, Sk - W:, :], rep, axis=1)
        v = (lambda x, n, axis: torch.repeat_interleave(x, n, dim=axis))(v_cache[:, :, Sk - W:, :], rep, axis=1)
        scores = torch.einsum('bhqd,bhkd->bhqk', q, k) * Dh ** (-0.5)
        p = _softmax(scores, axis=-1)
        o = torch.einsum('bhqk,bhkd->bhqd', p, v)
        return (lambda x, *d: x.permute(*d))(o, 0, 2, 1, 3).reshape(B, Sq, Hq * Dh) @ wo

    def _B_ref(x, W_gate, W_up, W_down):
        gate = torch.einsum('bsd,df->bsf', x, W_gate)
        up = torch.einsum('bsd,df->bsf', x, W_up)
        gate = gate * (lambda x, dt: x.to(dt))(gate > 0, gate.dtype)
        h = gate * up
        return torch.einsum('bsf,fd->bsd', h, W_down)
    x = A_x
    mod = (_silu(c) @ w_mod).reshape(c.shape[0], 6, 1, -1)
    sa, ca, ga, sb, cb, gb = (mod[:, 0].clone(), mod[:, 1].clone(), mod[:, 2].clone(), mod[:, 3].clone(), mod[:, 4].clone(), mod[:, 5].clone())
    h = _layernorm(x, axis=-1) * (1 + ca) + sa
    x = x + ga * _A_ref(h, A_k_cache, A_v_cache, A_wq, A_wo)
    h = _layernorm(x, axis=-1) * (1 + cb) + sb
    x = x + gb * _B_ref(h, B_W_gate, B_W_up, B_W_down)
    return x
```

```cpp
#include <hip/hip_runtime.h>
#include <hip/hip_bf16.h>
typedef unsigned short u16;
static __device__ __forceinline__ float bf2f(u16 x){ return __uint_as_float(((unsigned)x)<<16); }
static __device__ __forceinline__ u16 f2bf(float f){ __bf16 b=(__bf16)f; return __builtin_bit_cast(u16,b); }
constexpr int S=2048,D=640,H=32,DH=128,SK=2048,W=512,F=1728;

__global__ void k_mod(const u16* c,const u16* w,float* mod){
  int n=blockIdx.x*blockDim.x+threadIdx.x; if(n>=6*D) return;
  float acc=0; for(int k=0;k<D;k++){ float v=bf2f(c[k]); float s=bf2f(f2bf(v/(1.f+__expf(-v)))); acc+=s*bf2f(w[(size_t)k*6*D+n]); }
  mod[n]=bf2f(f2bf(acc));
}
__global__ void k_ln(const u16* x,const float* sh,const float* sc,u16* h){
  int r=blockIdx.x; int l=threadIdx.x; float v[10]; float s=0;
  for(int i=0;i<10;i++){ v[i]=bf2f(x[r*D+l+64*i]); s+=v[i]; }
  for(int o=32;o;o>>=1) s+=__shfl_xor(s,o); float m=s/D; float q=0;
  for(int i=0;i<10;i++){ float d=v[i]-m; q+=d*d; }
  for(int o=32;o;o>>=1) q+=__shfl_xor(q,o); float rs=rsqrtf(q/D+1e-5f);
  for(int i=0;i<10;i++){ int k=l+64*i; h[r*D+k]=f2bf((v[i]-m)*rs*(1.f+sc[k])+sh[k]); }
}
template<int MODE> __global__ void k_gemm(const u16* A,const u16* B,u16* C,int M,int N,int K,const u16* res,const float* g){
  __shared__ float As[32][33], Bs[32][33];
  int tx=threadIdx.x, ty=threadIdx.y; int row=blockIdx.y*32+ty, col=blockIdx.x*32+tx; float acc=0;
  for(int k0=0;k0<K;k0+=32){ As[ty][tx]=bf2f(A[(size_t)row*K+k0+tx]); Bs[ty][tx]=bf2f(B[(size_t)(k0+ty)*N+col]); __syncthreads();
    for(int k=0;k<32;k++) acc+=As[ty][k]*Bs[k][tx]; __syncthreads(); }
  if(MODE==0) C[(size_t)row*N+col]=f2bf(acc); else C[(size_t)row*N+col]=f2bf(bf2f(res[(size_t)row*N+col])+g[col]*acc);
}
__global__ void k_attn(const u16* q,const u16* kc,const u16* vc,u16* o){
  int r=blockIdx.x, h=blockIdx.y, t=threadIdx.x; __shared__ float qs[DH], p[W], red[8];
  if(t<DH) qs[t]=bf2f(q[(size_t)r*H*DH+h*DH+t]); __syncthreads();
  const u16* kp=kc+((size_t)h*SK+SK-W+t)*DH; float s=0; for(int d=0;d<DH;d++) s+=qs[d]*bf2f(kp[d]); s*=0.08838834764831845f;
  float m=s; for(int o2=32;o2;o2>>=1) m=fmaxf(m,__shfl_xor(m,o2)); if((t&63)==0) red[t>>6]=m; __syncthreads();
  m=red[0]; for(int i=1;i<8;i++) m=fmaxf(m,red[i]); __syncthreads();
  float e=__expf(s-m); p[t]=e; float z=e; for(int o2=32;o2;o2>>=1) z+=__shfl_xor(z,o2); if((t&63)==0) red[t>>6]=z; __syncthreads();
  z=0; for(int i=0;i<8;i++) z+=red[i];
  if(t<DH){ float a=0; for(int k=0;k<W;k++) a+=p[k]*bf2f(vc[((size_t)h*SK+SK-W+k)*DH+t]); o[(size_t)r*H*DH+h*DH+t]=f2bf(a/z); }
}
__global__ void k_glu(const u16* g,const u16* u,u16* h,int n){ int i=blockIdx.x*256+threadIdx.x; if(i<n){ float a=bf2f(g[i]); h[i]=f2bf((a>0?a:0.f)*bf2f(u[i])); } }

extern "C" void kernel_launch(void* const* d_in, const int* in_sizes, int n_in, void* d_out, int out_size, void* d_ws, size_t ws_size, hipStream_t stream){
  const u16* x=(const u16*)d_in[0]; const u16* kc=(const u16*)d_in[1]; const u16* vc=(const u16*)d_in[2];
  const u16* wq=(const u16*)d_in[3]; const u16* wo=(const u16*)d_in[4]; const u16* wg=(const u16*)d_in[6]; const u16* wu=(const u16*)d_in[7];
  const u16* wd=(const u16*)d_in[8]; const u16* c=(const u16*)d_in[9]; const u16* wm=(const u16*)d_in[10];
  char* ws=(char*)d_ws; float* mod=(float*)ws; u16* h=(u16*)(ws+(1<<20)); u16* q=(u16*)(ws+(8<<20)); u16* o=(u16*)(ws+(32<<20));
  u16* x1=(u16*)(ws+(64<<20)); u16* g=(u16*)(ws+(72<<20)); u16* u=(u16*)(ws+(88<<20)); u16* h3=(u16*)(ws+(104<<20));
  k_mod<<<60,64,0,stream>>>(c,wm,mod);
  k_ln<<<S,64,0,stream>>>(x,mod,mod+D,h);
  k_gemm<0><<<dim3(4096/32,S/32),dim3(32,32),0,stream>>>(h,wq,q,S,4096,D,nullptr,nullptr);
  k_attn<<<dim3(S,H),512,0,stream>>>(q,kc,vc,o);
  k_gemm<1><<<dim3(D/32,S/32),dim3(32,32),0,stream>>>(o,wo,x1,S,D,4096,x,mod+2*D);
  k_ln<<<S,64,0,stream>>>(x1,mod+3*D,mod+4*D,h);
  k_gemm<0><<<dim3(F/32,S/32),dim3(32,32),0,stream>>>(h,wg,g,S,F,D,nullptr,nullptr);
  k_gemm<0><<<dim3(F/32,S/32),dim3(32,32),0,stream>>>(h,wu,u,S,F,D,nullptr,nullptr);
  k_glu<<<(S*F+255)/256,256,0,stream>>>(g,u,h3,S*F);
  k_gemm<1><<<dim3(D/32,S/32),dim3(32,32),0,stream>>>(h3,wd,(u16*)d_out,S,D,F,x1,mod+5*D);
}
```

```cpp
#include <hip/hip_runtime.h>
#include <hip/hip_bf16.h>
typedef unsigned short u16;
constexpr unsigned WS_WOT = 0x1400000;
constexpr unsigned WS_WDT = 0x2c00000;
constexpr unsigned WS_WGU = 0x1f00000;
constexpr int F = 1728;

constexpr int t_of(int sl) { return sl <= 10 ? 7 * sl : (sl == 11 ? 76 : sl == 12 ? 82 : sl == 13 ? 89 : sl == 14 ? 96 : 102); }
struct JobTable { unsigned v[256 * 4 * 8 * 4]; };
constexpr JobTable make_jobs() {
  JobTable T{};
  for (int b = 0; b < 256; b++) for (int w = 0; w < 4; w++) for (int j = 0; j < 8; j++) {
    const int id = j * 256 + b, jh = w >> 1;
    unsigned m = 4, stride = 1280, off = 0, d[2] = {0x40000000u, 0x40000000u};
    if (id < 640) {
      const int st = id / 5, cb = id % 5;
      m = 0; off = (32 * st + 8 * w) * 1280 + 256 * cb;
      for (int t = 0; t < 2; t++) { const int n = 8 * cb + 2 * w + t; d[t] = WS_WOT + ((n / 5) * 640 + st * 5 + n % 5) * 1024; }
    } else if (id < 920) {
      const int q = id - 640, sl = q / 5, cb = q % 5, g = sl / 14, e = sl % 14, p = e < 4 ? e : e - 2;
      const bool odd = e == 4 || e == 5;
      const int sg = odd ? 4 * g + 2 * (e - 4) + jh : 4 * g + (p & 3), ft = odd ? t_of(sg) + 6 : t_of(sg) + 2 * (p >> 2) + jh;
      if (!odd || (sg & 10) != 10) { m = 1; off = (16 * ft + 8 * (w & 1)) * 1280 + 256 * cb; }
      for (int t = 0; t < 2; t++) { const int n = 8 * cb + 2 * w + t; d[t] = WS_WDT + ((n / 5) * 280 + sl * 5 + n % 5) * 1024; }
    } else if (id < 1480) {
      const int q = id - 920, up = q & 1, s = (q >> 1) % 20, cb = q / 40, i5 = s / 5, e = s % 5;
      const int k16 = e < 4 ? 10 * i5 + 5 * (e >> 1) + 2 * (e & 1) + jh : 10 * i5 + 5 * jh + 4;
      m = 2 + up; stride = 2 * F; off = (16 * k16 + 8 * (w & 1)) * 2 * F + 256 * cb;
      for (int t = 0; t < 2; t++) {
        const int ft = 8 * cb + 2 * w + t;
        if (ft < 108) {
          int sg = 15;
          while (t_of(sg) > ft) sg--;
          const int r = ft - t_of(sg), G = r == 6 ? 0 : r >> 1, nt = (r == 6 ? 4 : 2 * (r & 1)) + up;
          d[t] = WS_WGU + (sg * 280 + (G == 0 ? 0 : G == 1 ? 120 : 200) + (s >> 2) * (G == 0 ? 24 : 16) + nt * 4 + (s & 3)) * 1024;
        }
      }
    }
    else if (id < 1500) {
      const int q = id - 1480, sg = 10 + (q / 5 & 1) + 4 * (q / 10);
      for (int t = 0; t < 2; t++) d[t] = WS_WGU + (sg * 280 + (q % 5) * 24 + 16 + 2 * w + t) * 1024;
    }
    unsigned* e = T.v + ((b * 4 + w) * 8 + j) * 4;
    e[0] = m << 28 | off; e[1] = d[0]; e[2] = d[1]; e[3] = stride;
  }
  return T;
}
static __device__ const JobTable g_jobs = make_jobs();

__global__ __launch_bounds__(256, 1) __attribute__((aligned(4096))) void k_block(const u16* x, const u16* kc, const u16* vc, const u16* wq, const u16* wo,
                                                  const u16* wg, const u16* wu, const u16* wd, const u16* c, const u16* wm,
                                                  char* ws, u16* out) {
  __shared__ __attribute__((aligned(16))) char lds[163840];
  unsigned w = __builtin_amdgcn_readfirstlane(threadIdx.x >> 6);
  asm volatile("" :: "s"((unsigned)(size_t)lds));
  asm volatile(
    "K_IN%=:\n"
    "s_load_dwordx16 s[16:31], %2, 0x20\n"
    "s_load_dwordx8 s[8:15], %2, 0x0\n"
    "s_mov_b32 s36, %0\n"
    "s_mov_b32 s37, %1\n"
    "s_mov_b64 s[82:83], %3\n"
    ".p2align 6\n"
    "v_mbcnt_lo_u32_b32 v232, -1, 0\n"
    "v_mbcnt_hi_u32_b32 v232, -1, v232\n"
    "v_lshlrev_b32_e32 v255, 4, v232\n"
    "s_getpc_b64 s[44:45]\n"
    "K_BEGIN%=:\n"
    "s_and_b32 s45, s45, 0xffff\n"
    "s_mov_b32 s46, K_END%=-K_BEGIN%=\n"
    "s_mov_b32 s47, 0x20000\n"
    "s_lshr_b32 s38, s37, 3\n"
    "s_sub_u32 s38, 31, s38\n"
    "s_lshl_b32 s38, s38, 2\n"
    "s_add_u32 s38, s38, s36\n"
    "s_lshl_b32 s38, s38, 10\n"
    "buffer_load_dwordx4 v[224:227], v255, s[44:47], s38 offen\n"
    "s_lshr_b32 s38, s37, 5\n"
    "s_lshl_b32 s38, s38, 3\n"
    "s_and_b32 s39, s37, 7\n"
    "s_add_u32 s77, s38, s39\n"
    "s_lshr_b32 s38, s37, 3\n"
    "s_and_b32 s71, s38, 3\n"
    "s_sub_u32 s39, s77, 20\n"
    "s_min_u32 s39, s39, s77\n"
    "s_lshl_b32 s70, s39, 2\n"
    "s_add_u32 s70, s70, s71\n"
    "s_mul_i32 s38, s71, 0xa0\n"
    "s_lshl_b32 s39, s36, 5\n"
    "s_add_u32 s61, s38, s39\n"
    "v_lshrrev_b32_e32 v22, 1, v232\n"
    "v_and_b32_e32 v23, 16, v255\n"
    "s_lshl_b32 s60, s70, 5\n"
    "v_add_u32_e64 v23, s60, v23\n"
    "v_mul_u32_u24_e32 v21, 0x1e00, v22\n"
    "v_add_u32_e32 v23, v23, v21\n"
    "v_lshlrev_b32_e32 v22, 1, v22\n"
    "s_add_u32 s38, s61, 0\n"
    "s_sub_u32 s39, s38, 0x280\n"
    "s_min_u32 s38, s38, s39\n"
    "s_mul_i32 s87, s38, 0x1e00\n"
    "s_lshl_b32 s62, s38, 1\n"
    "s_add_u32 s38, s61, 0x80\n"
    "s_sub_u32 s39, s38, 0x280\n"
    "s_min_u32 s38, s38, s39\n"
    "s_mul_i32 s88, s38, 0x1e00\n"
    "s_lshl_b32 s63, s38, 1\n"
    "s_add_u32 s38, s61, 0x100\n"
    "s_sub_u32 s39, s38, 0x280\n"
    "s_min_u32 s38, s38, s39\n"
    "s_mul_i32 s89, s38, 0x1e00\n"
    "s_lshl_b32 s64, s38, 1\n"
    "s_add_u32 s38, s61, 0x180\n"
    "s_sub_u32 s39, s38, 0x280\n"
    "s_min_u32 s38, s38, s39\n"
    "s_mul_i32 s90, s38, 0x1e00\n"
    "s_lshl_b32 s65, s38, 1\n"
    "s_add_u32 s38, s61, 0x200\n"
    "s_sub_u32 s39, s38, 0x280\n"
    "s_min_u32 s38, s38, s39\n"
    "s_mul_i32 s91, s38, 0x1e00\n"
    "s_lshl_b32 s66, s38, 1\n"
    "s_mov_b32 s98, 0x4b0000\n"
    "s_mov_b32 s99, 0x20000\n"
    "s_mov_b32 s58, 0x500\n"
    "s_mov_b32 s59, 0x20000\n"
    "s_waitcnt lgkmcnt(0)\n"
    "s_load_dword s76, s[28:29], 0x8000\n"
    "s_mov_b32 s96, s26\n"
    "s_and_b32 s97, s27, 0xffff\n"
    "s_mov_b32 s56, s24\n"
    "s_and_b32 s57, s25, 0xffff\n"
    "s_sub_u32 s61, s77, 0\n"
    "s_cmp_lt_u32 s61, 20\n"
    "s_cbranch_scc0 G1%=\n"
    "buffer_load_dwordx4 v[144:147], v23, s[96:99], s87 offen\n"
    "buffer_load_ushort v164, v22, s[56:59], s62 offen\n"
    "buffer_load_ushort v165, v22, s[56:59], s63 offen\n"
    "buffer_load_ushort v166, v22, s[56:59], s64 offen\n"
    "buffer_load_ushort v167, v22, s[56:59], s65 offen\n"
    "buffer_load_ushort v168, v22, s[56:59], s66 offen\n"
    "buffer_load_dwordx4 v[148:151], v23, s[96:99], s88 offen\n"
    "buffer_load_dwordx4 v[152:155], v23, s[96:99], s89 offen\n"
    "buffer_load_dwordx4 v[156:159], v23, s[96:99], s90 offen\n"
    "buffer_load_dwordx4 v[160:163], v23, s[96:99], s91 offen\n"
    "G1%=:\n"
    "s_sub_u32 s61, s77, 20\n"
    "s_cmp_lt_u32 s61, 44\n"
    "s_cbranch_scc0 G0%=\n"
    "s_sleep 6\n"
    "G0%=:\n"
    "s_mov_b32 s52, s8\n"
    "s_and_b32 s53, s9, 0xffff\n"
    "s_mov_b32 s54, 0x280000\n"
    "s_mov_b32 s55, 0x20000\n"
    "s_lshr_b32 s38, s37, 3\n"
    "s_and_b32 s39, s38, 7\n"
    "s_and_b32 s60, s37, 6\n"
    "s_lshl_b32 s60, s60, 2\n"
    "s_add_u32 s73, s60, s39\n"
    "s_lshr_b32 s38, s38, 3\n"
    "s_and_b32 s39, s37, 1\n"
    "s_lshl_b32 s39, s39, 2\n"
    "s_add_u32 s38, s38, s39\n"
    "s_lshl_b32 s38, s38, 2\n"
    "s_add_u32 s74, s38, s36\n"
    "v_and_b32_e32 v233, 7, v232\n"
    "v_lshrrev_b32_e64 v234, 3, v232\n"
    "v_mul_u32_u24_e32 v20, 0x500, v233\n"
    "v_lshl_add_u32 v1, v234, 4, v20\n"
    "s_lshr_b32 s38, s74, 2\n"
    "s_lshl_b32 s38, s38, 5\n"
    "s_add_u32 s38, s38, s73\n"
    "s_mul_i32 s38, s38, 0x2800\n"
    "s_lshl_b32 s39, s36, 6\n"
    "s_add_u32 s60, s38, s39\n"
    "s_add_u32 s38, s60, s39\n"
    "buffer_load_dwordx4 v[8:11], v1, s[52:55], s38 offen\n"
    "buffer_load_dwordx4 v[12:15], v1, s[52:55], s38 offen offset:512\n"
    "v_lshl_add_u32 v20, v234, 3, v20\n"
    "buffer_load_dwordx2 v[16:17], v20, s[52:55], s60 offen offset:1024\n"
    "s_mov_b32 s48, s28\n"
    "s_and_b32 s49, s29, 0xffff\n"
    "s_mov_b32 s50, 0x3b00000\n"
    "s_mov_b32 s51, 0x20000\n"
    "s_sub_u32 s61, s77, 0\n"
    "s_cmp_lt_u32 s61, 20\n"
    "s_cbranch_scc0 G2%=\n"
    "v_bfe_u32 v192, v255, 4, 3\n"
    "v_lshlrev_b32_e64 v192, 3, v192\n"
    "v_add_u32_e32 v194, 0x20100, v192\n"
    "v_lshrrev_b32_e32 v193, 7, v255\n"
    "v_or_b32_e32 v193, s36, v193\n"
    "v_cmp_eq_u32_e32 vcc, 0, v193\n"
    "v_mov_b32_e32 v193, 0x40000000\n"
    "s_nop 0\n"
    "v_cndmask_b32_e64 v192, v193, v192, vcc\n"
    "s_cmp_ge_u32 s70, 40\n"
    "s_cselect_b32 s88, 1.0, 0\n"
    "s_lshl_b32 s87, s70, 6\n"
    "s_add_u32 s87, s87, 0x10000\n"
    "s_waitcnt vmcnt(11)\n"
    "v_lshlrev_b32_e64 v164, 16, v164\n"
    "v_mul_f32_e32 v169, 0xbfb8aa3b, v164\n"
    "v_exp_f32_e32 v169, v169\n"
    "s_waitcnt vmcnt(10)\n"
    "v_lshlrev_b32_e64 v165, 16, v165\n"
    "v_mul_f32_e32 v170, 0xbfb8aa3b, v165\n"
    "v_exp_f32_e32 v170, v170\n"
    "s_waitcnt vmcnt(9)\n"
    "v_lshlrev_b32_e64 v166, 16, v166\n"
    "v_mul_f32_e32 v171, 0xbfb8aa3b, v166\n"
    "v_exp_f32_e32 v171, v171\n"
    "s_waitcnt vmcnt(8)\n"
    "v_lshlrev_b32_e64 v167, 16, v167\n"
    "v_mul_f32_e32 v172, 0xbfb8aa3b, v167\n"
    "v_exp_f32_e32 v172, v172\n"
    "s_waitcnt vmcnt(7)\n"
    "v_lshlrev_b32_e64 v168, 16, v168\n"
    "v_mul_f32_e32 v173, 0xbfb8aa3b, v168\n"
    "v_exp_f32_e32 v173, v173\n"
    "v_add_f32_e32 v169, 1.0, v169\n"
    "v_rcp_f32_e32 v169, v169\n"
    "v_add_f32_e32 v170, 1.0, v170\n"
    "v_rcp_f32_e32 v170, v170\n"
    "v_add_f32_e32 v171, 1.0, v171\n"
    "v_rcp_f32_e32 v171, v171\n"
    "v_add_f32_e32 v172, 1.0, v172\n"
    "v_rcp_f32_e32 v172, v172\n"
    "v_add_f32_e32 v173, 1.0, v173\n"
    "v_rcp_f32_e32 v173, v173\n"
    "v_mul_f32_e32 v169, v164, v169\n"
    "v_cvt_pk_bf16_f32 v164, 0, v169\n"
    "v_mul_f32_e64 v170, v165, v170\n"
    "v_cvt_pk_bf16_f32 v165, 0, v170\n"
    "v_mul_f32_e64 v171, v166, v171\n"
    "v_cvt_pk_bf16_f32 v166, 0, v171\n"
    "v_mul_f32_e64 v172, v167, v172\n"
    "v_cvt_pk_bf16_f32 v167, 0, v172\n"
    "v_mul_f32_e64 v173, v168, v173\n"
    "v_cvt_pk_bf16_f32 v168, 0, v173\n"
    "v_lshrrev_b32_e64 v169, 8, v255\n"
    "v_bfe_u32 v170, v255, 4, 1\n"
    "v_lshl_add_u32 v169, v170, 2, v169\n"
    "v_lshlrev_b32_e32 v169, 3, v169\n"
    "s_lshl_b32 s60, s36, 8\n"
    "s_add_u32 s60, s60, 0x20100\n"
    "v_add_u32_e32 v169, s60, v169\n"
    "v_lshlrev_b32_e32 v184, 16, v144\n"
    "v_and_b32_e32 v185, 0xffff0000, v144\n"
    "v_lshlrev_b32_e64 v186, 16, v145\n"
    "v_and_b32_e32 v187, 0xffff0000, v145\n"
    "v_lshlrev_b32_e64 v188, 16, v146\n"
    "v_and_b32_e32 v189, 0xffff0000, v146\n"
    "v_lshlrev_b32_e64 v190, 16, v147\n"
    "v_and_b32_e32 v191, 0xffff0000, v147\n"
    "v_pk_mul_f32 v[176:177], v[164:165], v[184:185] op_sel_hi:[0,1]\n"
    "v_pk_mul_f32 v[178:179], v[164:165], v[186:187] op_sel_hi:[0,1]\n"
    "v_pk_mul_f32 v[180:181], v[164:165], v[188:189] op_sel_hi:[0,1]\n"
    "v_pk_mul_f32 v[182:183], v[164:165], v[190:191] op_sel_hi:[0,1]\n"
    "s_waitcnt vmcnt(6)\n"
    "v_lshlrev_b32_e32 v184, 16, v148\n"
    "v_and_b32_e32 v185, 0xffff0000, v148\n"
    "v_lshlrev_b32_e64 v186, 16, v149\n"
    "v_and_b32_e32 v187, 0xffff0000, v149\n"
    "v_lshlrev_b32_e64 v188, 16, v150\n"
    "v_and_b32_e32 v189, 0xffff0000, v150\n"
    "v_lshlrev_b32_e64 v190, 16, v151\n"
    "v_and_b32_e32 v191, 0xffff0000, v151\n"
    "v_pk_fma_f32 v[176:177], v[164:165], v[184:185], v[176:177] op_sel:[1,0,0]\n"
    "v_pk_fma_f32 v[178:179], v[164:165], v[186:187], v[178:179] op_sel:[1,0,0]\n"
    "v_pk_fma_f32 v[180:181], v[164:165], v[188:189], v[180:181] op_sel:[1,0,0]\n"
    "v_pk_fma_f32 v[182:183], v[164:165], v[190:191], v[182:183] op_sel:[1,0,0]\n"
    "s_waitcnt vmcnt(5)\n"
    "v_lshlrev_b32_e32 v184, 16, v152\n"
    "v_and_b32_e32 v185, 0xffff0000, v152\n"
    "v_lshlrev_b32_e64 v186, 16, v153\n"
    "v_and_b32_e32 v187, 0xffff0000, v153\n"
    "v_lshlrev_b32_e64 v188, 16, v154\n"
    "v_and_b32_e32 v189, 0xffff0000, v154\n"
    "v_lshlrev_b32_e64 v190, 16, v155\n"
    "v_and_b32_e32 v191, 0xffff0000, v155\n"
    "v_pk_fma_f32 v[176:177], v[166:167], v[184:185], v[176:177] op_sel_hi:[0,1,1]\n"
    "v_pk_fma_f32 v[178:179], v[166:167], v[186:187], v[178:179] op_sel_hi:[0,1,1]\n"
    "v_pk_fma_f32 v[180:181], v[166:167], v[188:189], v[180:181] op_sel_hi:[0,1,1]\n"
    "v_pk_fma_f32 v[182:183], v[166:167], v[190:191], v[182:183] op_sel_hi:[0,1,1]\n"
    "s_waitcnt vmcnt(4)\n"
    "v_lshlrev_b32_e32 v184, 16, v156\n"
    "v_and_b32_e32 v185, 0xffff0000, v156\n"
    "v_lshlrev_b32_e64 v186, 16, v157\n"
    "v_and_b32_e32 v187, 0xffff0000, v157\n"
    "v_lshlrev_b32_e64 v188, 16, v158\n"
    "v_and_b32_e32 v189, 0xffff0000, v158\n"
    "v_lshlrev_b32_e64 v190, 16, v159\n"
    "v_and_b32_e32 v191, 0xffff0000, v159\n"
    "v_pk_fma_f32 v[176:177], v[166:167], v[184:185], v[176:177] op_sel:[1,0,0]\n"
    "v_pk_fma_f32 v[178:179], v[166:167], v[186:187], v[178:179] op_sel:[1,0,0]\n"
    "v_pk_fma_f32 v[180:181], v[166:167], v[188:189], v[180:181] op_sel:[1,0,0]\n"
    "v_pk_fma_f32 v[182:183], v[166:167], v[190:191], v[182:183] op_sel:[1,0,0]\n"
    "s_waitcnt vmcnt(3)\n"
    "v_lshlrev_b32_e32 v184, 16, v160\n"
    "v_and_b32_e32 v185, 0xffff0000, v160\n"
    "v_lshlrev_b32_e64 v186, 16, v161\n"
    "v_and_b32_e32 v187, 0xffff0000, v161\n"
    "v_lshlrev_b32_e64 v188, 16, v162\n"
    "v_and_b32_e32 v189, 0xffff0000, v162\n"
    "v_lshlrev_b32_e64 v190, 16, v163\n"
    "v_and_b32_e32 v191, 0xffff0000, v163\n"
    "v_pk_fma_f32 v[176:177], v[168:169], v[184:185], v[176:177] op_sel_hi:[0,1,1]\n"
    "v_pk_fma_f32 v[178:179], v[168:169], v[186:187], v[178:179] op_sel_hi:[0,1,1]\n"
    "v_pk_fma_f32 v[180:181], v[168:169], v[188:189], v[180:181] op_sel_hi:[0,1,1]\n"
    "v_pk_fma_f32 v[182:183], v[168:169], v[190:191], v[182:183] op_sel_hi:[0,1,1]\n"
    "s_nop 0\n"
    "v_permlane32_swap_b32 v176, v180\n"
    "v_permlane32_swap_b32 v177, v181\n"
    "v_permlane32_swap_b32 v178, v182\n"
    "v_permlane32_swap_b32 v179, v183\n"
    "v_add_f32_e32 v176, v176, v180\n"
    "v_add_f32_e32 v177, v177, v181\n"
    "v_add_f32_e32 v178, v178, v182\n"
    "v_add_f32_e32 v179, v179, v183\n"
    "s_nop 0\n"
    "v_permlane16_swap_b32 v176, v178\n"
    "v_permlane16_swap_b32 v177, v179\n"
    "v_add_f32_e32 v176, v176, v178\n"
    "v_add_f32_e32 v177, v177, v179\n"
    "s_nop 0\n"
    "v_add_f32_dpp v176, v176, v176 row_ror:8 row_mask:0xf bank_mask:0xf\n"
    "v_add_f32_dpp v177, v177, v177 row_ror:8 row_mask:0xf bank_mask:0xf\n"
    "s_nop 0\n"
    "v_add_f32_dpp v176, v176, v176 row_ror:4 row_mask:0xf bank_mask:0xf\n"
    "v_add_f32_dpp v177, v177, v177 row_ror:4 row_mask:0xf bank_mask:0xf\n"
    "s_nop 0\n"
    "v_add_f32_dpp v176, v176, v176 row_ror:2 row_mask:0xf bank_mask:0xf\n"
    "v_add_f32_dpp v177, v177, v177 row_ror:2 row_mask:0xf bank_mask:0xf\n"
    "ds_write_b64 v169, v[176:177] offset:0\n"
    "s_waitcnt lgkmcnt(0)\n"
    "s_barrier\n"
    "ds_read_b64 v[176:177], v194 offset:0\n"
    "ds_read_b64 v[178:179], v194 offset:256\n"
    "ds_read_b64 v[180:181], v194 offset:512\n"
    "ds_read_b64 v[182:183], v194 offset:768\n"
    "s_waitcnt lgkmcnt(2)\n"
    "v_add_f32_e32 v176, v176, v178\n"
    "s_waitcnt lgkmcnt(0)\n"
    "v_add_f32_e32 v180, v180, v182\n"
    "v_add_f32_e32 v177, v177, v179\n"
    "v_add_f32_e32 v181, v181, v183\n"
    "v_add_f32_e32 v176, v176, v180\n"
    "v_add_f32_e64 v177, v177, v181\n"
    "v_cvt_pk_bf16_f32 v178, 0, v176\n"
    "v_cvt_pk_bf16_f32 v179, 0, v177\n"
    "v_add_f32_e32 v178, s88, v178\n"
    "v_add_f32_e32 v179, s88, v179\n"
    "v_cvt_pk_bf16_f32 v176, v178, v179\n"
    "v_mov_b32_e32 v177, s76\n"
    "v_add_u32_e32 v177, 1, v177\n"
    "buffer_store_dwordx2 v[176:177], v192, s[48:51], s87 offen sc1\n"
    "G2%=:\n"
    "s_mov_b32 s40, s14\n"
    "s_and_b32 s41, s15, 0xffff\n"
    "s_mov_b32 s42, 0x500000\n"
    "s_mov_b32 s43, 0x20000\n"
    "s_lshl_b32 s62, s73, 8\n"
    "s_add_u32 s63, s62, 0x80\n"
    "s_mov_b32 s66, 0x40000\n"
    "s_lshl_b32 s68, s36, 11\n"
    "s_mul_i32 s69, s74, 0x14000\n"
    "s_add_u32 s69, s69, 0x100000\n"
    "v_bfe_u32 v233, v232, 2, 3\n"
    "s_lshl_b32 s38, s36, 3\n"
    "v_add_u32_e32 v233, s38, v233\n"
    "s_and_b32 s38, s36, 1\n"
    "s_lshl_b32 s38, s38, 1\n"
    "v_and_b32_e32 v235, 3, v232\n"
    "v_xor_b32_e32 v234, s38, v235\n"
    "v_lshlrev_b32_e32 v234, 4, v234\n"
    "v_lshrrev_b32_e64 v235, 5, v232\n"
    "v_lshl_add_u32 v234, v235, 6, v234\n"
    "v_lshl_add_u32 v254, v233, 13, v234\n"
    "v_and_b32_e32 v233, 7, v232\n"
    "v_lshlrev_b32_e32 v20, 4, v233\n"
    "v_bfe_u32 v234, v232, 3, 2\n"
    "v_lshl_add_u32 v3, v234, 8, v20\n"
    "v_lshrrev_b32_e64 v234, 5, v232\n"
    "v_lshl_add_u32 v3, v234, 12, v3\n"
    "v_lshrrev_b32_e64 v234, 4, v232\n"
    "v_lshl_add_u32 v20, v234, 8, v20\n"
    "v_and_b32_e64 v234, 8, v232\n"
    "v_lshl_add_u32 v20, v234, 27, v20\n"
    "v_lshlrev_b32_e64 v1, 5, v233\n"
    "v_add_u32_e32 v1, 0x20000, v1\n"
    "s_lshl_b32 s38, s36, 3\n"
    "v_add_u32_e32 v2, s38, v1\n"
    "v_or_b32_e32 v233, s36, v232\n"
    "v_cmp_eq_u32_e32 vcc, 0, v233\n"
    "v_mov_b32_e32 v234, 0x40000000\n"
    "s_nop 0\n"
    "v_cndmask_b32_e64 v241, v234, 0, vcc\n"
    "s_and_b32 s38, s74, -4\n"
    "s_lshr_b32 s39, s73, 3\n"
    "s_add_u32 s38, s38, s39\n"
    "s_lshl_b32 s93, s38, 11\n"
    "s_and_b32 s39, s73, 7\n"
    "s_lshl_b32 s60, s39, 2\n"
    "s_add_u32 s93, s93, s60\n"
    "s_add_u32 s81, s93, 0x30000\n"
    "s_add_u32 s93, s93, 0x60000\n"
    "s_mul_i32 s38, s38, 0x14000\n"
    "s_lshr_b32 s60, s39, 1\n"
    "s_lshl_b32 s60, s60, 10\n"
    "s_add_u32 s38, s38, s60\n"
    "s_and_b32 s60, s39, 1\n"
    "s_lshl_b32 s60, s60, 7\n"
    "s_add_u32 s38, s38, s60\n"
    "s_add_u32 s38, s38, 0x100000\n"
    "s_lshl_b32 s39, s36, 12\n"
    "s_add_u32 s60, s38, s39\n"
    "s_add_u32 s72, s60, s39\n"
    "s_add_u32 s87, s72, 0x8000\n"
    "s_add_u32 s88, s60, 0x10000\n"
    "v_mov_b32_e32 v0, 0x3f803f80\n"
    "v_mov_b32_e32 v4, 0\n"
    "v_mov_b32_e32 v5, 0\n"
    "s_waitcnt vmcnt(2)\n"
    "v_dot2_f32_bf16 v4, v8, v0, v4\n"
    "v_dot2_f32_bf16 v5, v8, v8, v5\n"
    "v_dot2_f32_bf16 v4, v9, v0, v4\n"
    "v_dot2_f32_bf16 v5, v9, v9, v5\n"
    "v_dot2_f32_bf16 v4, v10, v0, v4\n"
    "v_dot2_f32_bf16 v5, v10, v10, v5\n"
    "v_dot2_f32_bf16 v4, v11, v0, v4\n"
    "v_dot2_f32_bf16 v5, v11, v11, v5\n"
    "s_waitcnt vmcnt(1)\n"
    "v_dot2_f32_bf16 v4, v12, v0, v4\n"
    "v_dot2_f32_bf16 v5, v12, v12, v5\n"
    "v_dot2_f32_bf16 v4, v13, v0, v4\n"
    "v_dot2_f32_bf16 v5, v13, v13, v5\n"
    "v_dot2_f32_bf16 v4, v14, v0, v4\n"
    "v_dot2_f32_bf16 v5, v14, v14, v5\n"
    "v_dot2_f32_bf16 v4, v15, v0, v4\n"
    "v_dot2_f32_bf16 v5, v15, v15, v5\n"
    "s_waitcnt vmcnt(0)\n"
    "v_dot2_f32_bf16 v4, v16, v0, v4\n"
    "v_dot2_f32_bf16 v5, v16, v16, v5\n"
    "v_dot2_f32_bf16 v4, v17, v0, v4\n"
    "v_dot2_f32_bf16 v5, v17, v17, v5\n"
    "s_nop 2\n"
    "v_add_f32_dpp v4, v4, v4 row_ror:8 row_mask:0xf bank_mask:0xf\n"
    "v_add_f32_dpp v5, v5, v5 row_ror:8 row_mask:0xf bank_mask:0xf\n"
    "v_mov_b32_e32 v6, v4\n"
    "v_mov_b32_e32 v7, v5\n"
    "s_nop 0\n"
    "v_permlane32_swap_b32 v4, v6\n"
    "v_permlane32_swap_b32 v5, v7\n"
    "v_add_f32_e32 v4, v4, v6\n"
    "v_add_f32_e32 v5, v5, v7\n"
    "v_mov_b32_e32 v6, v4\n"
    "v_mov_b32_e32 v7, v5\n"
    "s_nop 0\n"
    "v_permlane16_swap_b32 v4, v6\n"
    "v_permlane16_swap_b32 v5, v7\n"
    "v_add_f32_e32 v4, v4, v6\n"
    "v_add_f32_e64 v5, v5, v7\n"
    "ds_write_b64 v2, v[4:5] offset:0\n"
    "s_sub_u32 s61, s77, 20\n"
    "s_cmp_lt_u32 s61, 44\n"
    "s_cbranch_scc0 G3%=\n"
    "s_add_u32 m0, s68, 0x6000\n"
    "s_nop 0\n"
    "buffer_load_dwordx4 v254, s[40:43], s62 offen lds\n"
    "s_add_u32 m0, s68, 0x6400\n"
    "s_nop 0\n"
    "buffer_load_dwordx4 v254, s[40:43], s63 offen lds\n"
    "s_add_u32 s62, s62, s66\n"
    "s_add_u32 s63, s63, s66\n"
    "s_add_u32 m0, s68, 0x8000\n"
    "s_nop 0\n"
    "buffer_load_dwordx4 v254, s[40:43], s62 offen lds\n"
    "s_add_u32 m0, s68, 0x8400\n"
    "s_nop 0\n"
    "buffer_load_dwordx4 v254, s[40:43], s63 offen lds\n"
    "s_add_u32 s62, s62, s66\n"
    "s_add_u32 s63, s63, s66\n"
    "s_mul_i32 s38, s71, 0xa0\n"
    "s_lshl_b32 s39, s36, 3\n"
    "s_add_u32 s38, s38, s39\n"
    "s_sub_u32 s39, s77, 20\n"
    "s_cmp_lt_u32 s39, 40\n"
    "s_cselect_b32 s38, s38, 0x10000\n"
    "s_mul_i32 s89, s38, 0x1e00\n"
    "s_lshl_b32 s39, s77, 7\n"
    "s_add_u32 s89, s89, s39\n"
    "s_lshl_b32 s38, s38, 1\n"
    "v_lshrrev_b32_e64 v22, 7, v255\n"
    "v_and_b32_e32 v23, 0x70, v255\n"
    "v_mul_u32_u24_e32 v21, 0x1e00, v22\n"
    "v_add_u32_e32 v23, v23, v21\n"
    "v_lshlrev_b32_e32 v22, 1, v22\n"
    "buffer_load_ushort v164, v22, s[56:59], s38 offen\n"
    "buffer_load_ushort v165, v22, s[56:59], s38 offen offset:64\n"
    "buffer_load_ushort v166, v22, s[56:59], s38 offen offset:128\n"
    "buffer_load_ushort v167, v22, s[56:59], s38 offen offset:192\n"
    "buffer_load_ushort v168, v22, s[56:59], s38 offen offset:256\n"
    "buffer_load_dwordx4 v[144:147], v23, s[96:99], s89 offen\n"
    "s_add_u32 s89, s89, 0x3c000\n"
    "buffer_load_dwordx4 v[148:151], v23, s[96:99], s89 offen\n"
    "s_add_u32 s89, s89, 0x3c000\n"
    "buffer_load_dwordx4 v[152:155], v23, s[96:99], s89 offen\n"
    "s_add_u32 s89, s89, 0x3c000\n"
    "buffer_load_dwordx4 v[156:159], v23, s[96:99], s89 offen\n"
    "s_add_u32 s89, s89, 0x3c000\n"
    "buffer_load_dwordx4 v[160:163], v23, s[96:99], s89 offen\n"
    "s_add_u32 s89, s89, 0x3c000\n"
    "s_add_u32 m0, s68, 0xa000\n"
    "s_nop 0\n"
    "buffer_load_dwordx4 v254, s[40:43], s62 offen lds\n"
    "s_add_u32 m0, s68, 0xa400\n"
    "s_nop 0\n"
    "buffer_load_dwordx4 v254, s[40:43], s63 offen lds\n"
    "s_add_u32 s62, s62, s66\n"
    "s_add_u32 s63, s63, s66\n"
    "s_add_u32 m0, s68, 0xc000\n"
    "s_nop 0\n"
    "buffer_load_dwordx4 v254, s[40:43], s62 offen lds\n"
    "s_add_u32 m0, s68, 0xc400\n"
    "s_nop 0\n"
    "buffer_load_dwordx4 v254, s[40:43], s63 offen lds\n"
    "s_add_u32 s62, s62, s66\n"
    "s_add_u32 s63, s63, s66\n"
    "s_add_u32 m0, s68, 0xe000\n"
    "s_nop 0\n"
    "buffer_load_dwordx4 v254, s[40:43], s62 offen lds\n"
    "s_add_u32 m0, s68, 0xe400\n"
    "s_nop 0\n"
    "buffer_load_dwordx4 v254, s[40:43], s63 offen lds\n"
    "s_add_u32 s62, s62, s66\n"
    "s_add_u32 s63, s63, s66\n"
    "s_add_u32 m0, s68, 0x10000\n"
    "s_nop 0\n"
    "buffer_load_dwordx4 v254, s[40:43], s62 offen lds\n"
    "s_add_u32 m0, s68, 0x10400\n"
    "s_nop 0\n"
    "buffer_load_dwordx4 v254, s[40:43], s63 offen lds\n"
    "s_add_u32 s62, s62, s66\n"
    "s_add_u32 s63, s63, s66\n"
    "G3%=:\n"
    "s_mov_b32 s96, s82\n"
    "s_and_b32 s97, s83, 0xffff\n"
    "s_mov_b32 s98, 0x20000\n"
    "s_mov_b32 s99, 0x20000\n"
    "s_lshl_b32 s38, s37, 2\n"
    "s_add_u32 s84, s38, 0x6000\n"
    "s_add_u32 s38, s38, s36\n"
    "s_lshl_b32 s38, s38, 7\n"
    "v_and_b32_e32 v240, 0x70, v255\n"
    "buffer_load_dwordx4 v[200:203], v240, s[96:99], s38 offen\n"
    "s_waitcnt lgkmcnt(0)\n"
    "s_barrier\n"
    "ds_read_b128 v[72:75], v1 offset:0\n"
    "ds_read_b128 v[76:79], v1 offset:16\n"
    "s_waitcnt lgkmcnt(1)\n"
    "v_pk_add_f32 v[72:73], v[72:73], v[74:75]\n"
    "s_waitcnt lgkmcnt(0)\n"
    "v_pk_add_f32 v[76:77], v[76:77], v[78:79]\n"
    "v_pk_add_f32 v[4:5], v[72:73], v[76:77]\n"
    "v_mul_f32_e32 v4, 0x3acccccd, v4\n"
    "v_mul_f32_e32 v5, 0x3acccccd, v5\n"
    "v_mul_f32_e32 v6, v4, v4\n"
    "v_sub_f32_e32 v5, v5, v6\n"
    "v_max_f32_e64 v5, 0, v5\n"
    "v_add_f32_e32 v5, 0x3727c5ac, v5\n"
    "v_rsq_f32_e32 v6, v5\n"
    "v_sub_f32_e32 v4, 0, v4\n"
    "v_mul_f32_e32 v4, v4, v6\n"
    "v_mov_b32_e32 v7, v6\n"
    "v_mov_b32_e32 v5, v4\n"
    "v_mov_b32_e32 v230, s76\n"
    "s_add_u32 s78, s76, 1\n"
    "buffer_store_dword v230, v241, s[48:51], s93 offen sc1\n"
    "s_mov_b64 exec, 0\n"
    "s_cmp_eq_u32 s36, 0\n"
    "s_cbranch_scc1 PG0_%=\n"
    "s_cmp_eq_u32 s36, 1\n"
    "s_cbranch_scc1 PG1_%=\n"
    "s_cmp_eq_u32 s36, 2\n"
    "s_cbranch_scc1 PG2_%=\n"
    "s_branch PG3_%=\n"
    "PGRET%=:\n"
    "v_min_u32_e32 v65, 39, v232\n"
    "v_lshrrev_b32_e32 v66, 4, v65\n"
    "v_and_b32_e32 v64, 15, v65\n"
    "v_lshlrev_b32_e64 v64, 4, v64\n"
    "v_lshl_add_u32 v64, v66, 10, v64\n"
    "v_lshrrev_b32_e32 v66, 1, v66\n"
    "s_lshl_b32 s61, s36, 8\n"
    "v_lshrrev_b32_e64 v66, v66, s61\n"
    "v_add_u32_e64 v64, v64, v66\n"
    "s_mul_i32 s61, s36, 0x500\n"
    "s_add_u32 s61, s61, 0x20800\n"
    "v_lshl_add_u32 v65, v65, 4, s61\n"
    "v_lshrrev_b32_e64 v66, 3, v232\n"
    "v_lshl_add_u32 v67, v66, 4, s61\n"
    "v_lshl_add_u32 v66, v66, 5, s61\n"
    "s_mov_b32 s91, 0x10000\n"
    "s_add_u32 s92, s76, 1\n"
    "s_mov_b32 s95, 0x4e20\n"
    "GSP%=:\n"
    "buffer_load_dwordx4 v[132:135], v64, s[48:51], s91 offen sc1\n"
    "buffer_load_dwordx4 v[128:131], v64, s[48:51], s91 offen offset:2560 sc1\n"
    "s_waitcnt vmcnt(0)\n"
    "v_xor_b32_e32 v136, s92, v129\n"
    "v_xor_b32_e32 v137, s92, v131\n"
    "v_xor_b32_e32 v138, s92, v133\n"
    "v_xor_b32_e32 v139, s92, v135\n"
    "v_or3_b32 v136, v136, v137, v138\n"
    "v_or_b32_e32 v136, v136, v139\n"
    "v_cmp_eq_u32_e32 vcc, 0, v136\n"
    "s_nop 1\n"
    "s_cmp_eq_u64 vcc, -1\n"
    "s_cbranch_scc1 GOK%=\n"
    "s_sleep 2\n"
    "s_sub_u32 s95, s95, 1\n"
    "s_cmp_lg_u32 s95, 0\n"
    "s_cbranch_scc1 GSP%=\n"
    "GOK%=:\n"
    "s_waitcnt vmcnt(0)\n"
    "v_lshlrev_b32_e64 v136, 16, v128\n"
    "v_and_b32_e32 v137, 0xffff0000, v128\n"
    "v_lshlrev_b32_e64 v138, 16, v130\n"
    "v_and_b32_e32 v139, 0xffff0000, v130\n"
    "v_mul_f32_e32 v136, 0x3e0293ee, v136\n"
    "v_mul_f32_e32 v137, 0x3e0293ee, v137\n"
    "v_mul_f32_e32 v138, 0x3e0293ee, v138\n"
    "v_mul_f32_e32 v139, 0x3e0293ee, v139\n"
    "ds_write_b128 v65, v[136:139] offset:0\n"
    "v_lshlrev_b32_e64 v140, 16, v132\n"
    "v_and_b32_e32 v141, 0xffff0000, v132\n"
    "v_lshlrev_b32_e64 v142, 16, v134\n"
    "v_and_b32_e32 v143, 0xffff0000, v134\n"
    "v_mul_f32_e32 v140, 0x3e0293ee, v140\n"
    "v_mul_f32_e32 v141, 0x3e0293ee, v141\n"
    "v_mul_f32_e32 v142, 0x3e0293ee, v142\n"
    "v_mul_f32_e32 v143, 0x3e0293ee, v143\n"
    "ds_write_b128 v65, v[140:143] offset:640\n"
    "ds_read_b128 v[24:27], v66 offset:0\n"
    "ds_read_b128 v[32:35], v66 offset:640\n"
    "ds_read_b128 v[28:31], v66 offset:16\n"
    "ds_read_b128 v[36:39], v66 offset:656\n"
    "ds_read_b128 v[40:43], v66 offset:256\n"
    "ds_read_b128 v[48:51], v66 offset:896\n"
    "ds_read_b128 v[44:47], v66 offset:272\n"
    "ds_read_b128 v[52:55], v66 offset:912\n"
    "ds_read_b128 v[56:59], v67 offset:512\n"
    "ds_read_b128 v[60:63], v67 offset:1152\n"
    "v_lshlrev_b32_e64 v72, 16, v8\n"
    "v_and_b32_e32 v73, 0xffff0000, v8\n"
    "v_lshlrev_b32_e64 v74, 16, v9\n"
    "v_and_b32_e32 v75, 0xffff0000, v9\n"
    "v_lshlrev_b32_e64 v76, 16, v10\n"
    "v_and_b32_e32 v77, 0xffff0000, v10\n"
    "v_lshlrev_b32_e64 v78, 16, v11\n"
    "v_and_b32_e32 v79, 0xffff0000, v11\n"
    "v_pk_fma_f32 v[72:73], v[72:73], v[6:7], v[4:5]\n"
    "v_pk_fma_f32 v[74:75], v[74:75], v[6:7], v[4:5]\n"
    "v_pk_fma_f32 v[76:77], v[76:77], v[6:7], v[4:5]\n"
    "v_pk_fma_f32 v[78:79], v[78:79], v[6:7], v[4:5]\n"
    "s_waitcnt lgkmcnt(8)\n"
    "v_pk_fma_f32 v[72:73], v[72:73], v[24:25], v[32:33]\n"
    "v_pk_fma_f32 v[74:75], v[74:75], v[26:27], v[34:35]\n"
    "s_waitcnt lgkmcnt(6)\n"
    "v_pk_fma_f32 v[76:77], v[76:77], v[28:29], v[36:37]\n"
    "v_pk_fma_f32 v[78:79], v[78:79], v[30:31], v[38:39]\n"
    "v_cvt_pk_bf16_f32 v8, v72, v73\n"
    "v_cvt_pk_bf16_f32 v9, v74, v75\n"
    "v_cvt_pk_bf16_f32 v10, v76, v77\n"
    "v_cvt_pk_bf16_f32 v11, v78, v79\n"
    "buffer_store_dwordx4 v[8:11], v3, s[48:51], s72 offen sc1\n"
    "v_lshlrev_b32_e64 v72, 16, v12\n"
    "v_and_b32_e32 v73, 0xffff0000, v12\n"
    "v_lshlrev_b32_e64 v74, 16, v13\n"
    "v_and_b32_e32 v75, 0xffff0000, v13\n"
    "v_lshlrev_b32_e64 v76, 16, v14\n"
    "v_and_b32_e32 v77, 0xffff0000, v14\n"
    "v_lshlrev_b32_e64 v78, 16, v15\n"
    "v_and_b32_e32 v79, 0xffff0000, v15\n"
    "v_pk_fma_f32 v[72:73], v[72:73], v[6:7], v[4:5]\n"
    "v_pk_fma_f32 v[74:75], v[74:75], v[6:7], v[4:5]\n"
    "v_pk_fma_f32 v[76:77], v[76:77], v[6:7], v[4:5]\n"
    "v_pk_fma_f32 v[78:79], v[78:79], v[6:7], v[4:5]\n"
    "s_waitcnt lgkmcnt(4)\n"
    "v_pk_fma_f32 v[72:73], v[72:73], v[40:41], v[48:49]\n"
    "v_pk_fma_f32 v[74:75], v[74:75], v[42:43], v[50:51]\n"
    "s_waitcnt lgkmcnt(2)\n"
    "v_pk_fma_f32 v[76:77], v[76:77], v[44:45], v[52:53]\n"
    "v_pk_fma_f32 v[78:79], v[78:79], v[46:47], v[54:55]\n"
    "v_cvt_pk_bf16_f32 v12, v72, v73\n"
    "v_cvt_pk_bf16_f32 v13, v74, v75\n"
    "v_cvt_pk_bf16_f32 v14, v76, v77\n"
    "v_cvt_pk_bf16_f32 v15, v78, v79\n"
    "buffer_store_dwordx4 v[12:15], v3, s[48:51], s87 offen sc1\n"
    "v_lshlrev_b32_e64 v72, 16, v16\n"
    "v_and_b32_e32 v73, 0xffff0000, v16\n"
    "v_lshlrev_b32_e64 v74, 16, v17\n"
    "v_and_b32_e32 v75, 0xffff0000, v17\n"
    "v_pk_fma_f32 v[72:73], v[72:73], v[6:7], v[4:5]\n"
    "v_pk_fma_f32 v[74:75], v[74:75], v[6:7], v[4:5]\n"
    "s_waitcnt lgkmcnt(0)\n"
    "v_pk_fma_f32 v[72:73], v[72:73], v[56:57], v[60:61]\n"
    "v_pk_fma_f32 v[74:75], v[74:75], v[58:59], v[62:63]\n"
    "v_cvt_pk_bf16_f32 v16, v72, v73\n"
    "v_cvt_pk_bf16_f32 v17, v74, v75\n"
    "s_nop 0\n"
    "v_mov_b32_dpp v18, v16 row_ror:8 row_mask:0xf bank_mask:0xf\n"
    "v_mov_b32_dpp v19, v17 row_ror:8 row_mask:0xf bank_mask:0xf\n"
    "PG0_%=:\n"
    "s_cbranch_execz PG4_%=\n"
    "buffer_store_dwordx4 v[16:19], v20, s[48:51], s88 offen sc1\n"
    "s_mov_b32 s44, s12\n"
    "s_and_b32 s45, s13, 0xffff\n"
    "s_mov_b32 s46, 0x1000000\n"
    "s_mov_b32 s47, 0x20000\n"
    "s_lshl_b32 s64, s73, 19\n"
    "s_add_u32 s64, s64, 0x60000\n"
    "s_add_u32 s65, s64, 0x80\n"
    "s_mov_b32 s67, 0x2000\n"
    "v_lshrrev_b32_e64 v232, 4, v255\n"
    "v_bfe_u32 v233, v232, 2, 3\n"
    "v_lshrrev_b32_e32 v234, 2, v233\n"
    "v_lshlrev_b32_e32 v234, 1, v234\n"
    "s_and_b32 s38, s36, 1\n"
    "v_or_b32_e32 v234, s38, v234\n"
    "v_and_b32_e32 v235, 3, v232\n"
    "v_xor_b32_e32 v234, v234, v235\n"
    "v_lshlrev_b32_e32 v234, 4, v234\n"
    "v_lshrrev_b32_e32 v235, 5, v232\n"
    "v_lshl_add_u32 v234, v235, 6, v234\n"
    "s_lshl_b32 s38, s36, 3\n"
    "v_add_u32_e32 v233, s38, v233\n"
    "v_lshl_add_u32 v253, v233, 8, v234\n"
    "v_bfe_u32 v233, v232, 4, 1\n"
    "v_lshlrev_b32_e64 v233, 1, v233\n"
    "v_bfe_u32 v236, v232, 1, 1\n"
    "v_xor_b32_e32 v235, v233, v236\n"
    "v_lshlrev_b32_e32 v235, 4, v235\n"
    "v_lshrrev_b32_e64 v234, 4, v232\n"
    "v_lshl_add_u32 v235, v234, 11, v235\n"
    "v_bfe_u32 v236, v232, 2, 2\n"
    "v_lshl_add_u32 v235, v236, 6, v235\n"
    "v_and_b32_e64 v236, 1, v232\n"
    "v_lshl_add_u32 v244, v236, 3, v235\n"
    "v_xor_b32_e64 v242, 32, v244\n"
    "v_bfe_u32 v233, v232, 4, 1\n"
    "v_lshrrev_b32_e64 v234, 5, v232\n"
    "v_lshl_add_u32 v235, v233, 1, v234\n"
    "v_bfe_u32 v236, v232, 1, 1\n"
    "v_xor_b32_e32 v235, v235, v236\n"
    "v_lshlrev_b32_e32 v235, 4, v235\n"
    "v_lshl_add_u32 v235, v234, 11, v235\n"
    "v_bfe_u32 v236, v232, 2, 3\n"
    "v_lshl_add_u32 v235, v236, 6, v235\n"
    "v_and_b32_e64 v236, 1, v232\n"
    "v_lshl_add_u32 v252, v236, 3, v235\n"
    "v_xor_b32_e64 v251, 32, v252\n"
    "v_bfe_u32 v233, v232, 2, 1\n"
    "v_bfe_u32 v234, v232, 3, 1\n"
    "v_lshl_add_u32 v235, v233, 1, v234\n"
    "v_lshrrev_b32_e32 v236, 5, v232\n"
    "v_xor_b32_e32 v235, v235, v236\n"
    "v_lshlrev_b32_e64 v235, 4, v235\n"
    "v_lshl_add_u32 v235, v234, 11, v235\n"
    "v_and_b32_e64 v236, 7, v232\n"
    "v_lshl_add_u32 v235, v236, 6, v235\n"
    "v_bfe_u32 v236, v232, 4, 1\n"
    "v_lshl_add_u32 v250, v236, 3, v235\n"
    "v_xor_b32_e32 v249, 32, v250\n"
    "v_and_b32_e32 v233, 3, v232\n"
    "v_cmp_eq_u32_e32 vcc, 0, v233\n"
    "v_mov_b32_e32 v234, 0x3f803f80\n"
    "s_nop 0\n"
    "v_cndmask_b32_e64 v224, 0, v234, vcc\n"
    "v_cndmask_b32_e64 v225, 0, v234, vcc\n"
    "v_cndmask_b32_e64 v226, 0, v234, vcc\n"
    "v_cndmask_b32_e64 v227, 0, v234, vcc\n"
    "s_lshl_b32 s72, s74, 19\n"
    "s_lshl_b32 s38, s73, 14\n"
    "s_add_u32 s72, s72, s38\n"
    "s_add_u32 s72, s72, 0x400000\n"
    "v_lshrrev_b32_e32 v80, 28, v200\n"
    "v_and_b32_e32 v200, 0xfffffff, v200\n"
    "v_mov_b32_e32 v81, s16\n"
    "v_mov_b32_e32 v82, s17\n"
    "v_mov_b32_e32 v83, 0x500000\n"
    "v_mov_b32_e32 v85, 0x21c000\n"
    "v_cmp_eq_u32_e32 vcc, 1, v80\n"
    "v_mov_b32_e32 v86, s22\n"
    "v_mov_b32_e64 v87, s23\n"
    "v_cndmask_b32_e64 v81, v81, v86, vcc\n"
    "v_cndmask_b32_e64 v82, v82, v87, vcc\n"
    "v_cndmask_b32_e64 v83, v83, v85, vcc\n"
    "v_cmp_eq_u32_e32 vcc, 2, v80\n"
    "v_mov_b32_e32 v86, s18\n"
    "v_mov_b32_e64 v87, s19\n"
    "v_cndmask_b32_e64 v81, v81, v86, vcc\n"
    "v_cndmask_b32_e64 v82, v82, v87, vcc\n"
    "v_cndmask_b32_e64 v83, v83, v85, vcc\n"
    "v_cmp_eq_u32_e32 vcc, 3, v80\n"
    "v_mov_b32_e32 v86, s20\n"
    "v_mov_b32_e64 v87, s21\n"
    "v_cndmask_b32_e64 v81, v81, v86, vcc\n"
    "v_cndmask_b32_e64 v82, v82, v87, vcc\n"
    "v_cndmask_b32_e64 v83, v83, v85, vcc\n"
    "v_cmp_eq_u32_e32 vcc, 4, v80\n"
    "v_lshrrev_b32_e32 v84, 7, v255\n"
    "v_sub_u32_e64 v83, v83, v200\n"
    "v_cndmask_b32_e64 v83, v83, 0, vcc\n"
    "v_add_co_u32_e32 v81, vcc, v81, v200\n"
    "s_nop 1\n"
    "v_addc_co_u32_e32 v82, vcc, 0, v82, vcc\n"
    "v_and_b32_e32 v82, 0xffff, v82\n"
    "v_mov_b32_e32 v228, v81\n"
    "v_cmp_eq_u32_e32 vcc, 1, v84\n"
    "s_nop 1\n"
    "v_cndmask_b32_e64 v228, v228, v82, vcc\n"
    "v_cmp_eq_u32_e32 vcc, 2, v84\n"
    "s_nop 1\n"
    "v_cndmask_b32_e64 v228, v228, v83, vcc\n"
    "v_cmp_eq_u32_e32 vcc, 3, v84\n"
    "s_nop 1\n"
    "v_cndmask_b32_e64 v228, v228, v203, vcc\n"
    "v_cmp_eq_u32_e32 vcc, 4, v84\n"
    "s_nop 1\n"
    "v_cndmask_b32_e64 v228, v228, v201, vcc\n"
    "v_cmp_eq_u32_e32 vcc, 5, v84\n"
    "s_nop 1\n"
    "v_cndmask_b32_e64 v228, v228, v202, vcc\n"
    "v_add_u32_e32 v231, 1, v230\n"
    "s_waitcnt vmcnt(0) lgkmcnt(0)\n"
    "s_barrier\n"
    "buffer_store_dword v231, v241, s[48:51], s93 offen sc1\n"
    "s_sub_u32 s61, s77, 0\n"
    "s_cmp_lt_u32 s61, 20\n"
    "s_cbranch_scc0 G5%=\n"
    "s_add_u32 m0, s68, 0x6000\n"
    "s_nop 0\n"
    "buffer_load_dwordx4 v254, s[40:43], s62 offen lds\n"
    "s_add_u32 m0, s68, 0x6400\n"
    "s_nop 0\n"
    "buffer_load_dwordx4 v254, s[40:43], s63 offen lds\n"
    "s_add_u32 s62, s62, s66\n"
    "s_add_u32 s63, s63, s66\n"
    "s_add_u32 m0, s68, 0x8000\n"
    "s_nop 0\n"
    "buffer_load_dwordx4 v254, s[40:43], s62 offen lds\n"
    "s_add_u32 m0, s68, 0x8400\n"
    "s_nop 0\n"
    "buffer_load_dwordx4 v254, s[40:43], s63 offen lds\n"
    "s_add_u32 s62, s62, s66\n"
    "s_add_u32 s63, s63, s66\n"
    "s_add_u32 m0, s68, 0xa000\n"
    "s_nop 0\n"
    "buffer_load_dwordx4 v254, s[40:43], s62 offen lds\n"
    "s_add_u32 m0, s68, 0xa400\n"
    "s_nop 0\n"
    "buffer_load_dwordx4 v254, s[40:43], s63 offen lds\n"
    "s_add_u32 s62, s62, s66\n"
    "s_add_u32 s63, s63, s66\n"
    "s_add_u32 m0, s68, 0xc000\n"
    "s_nop 0\n"
    "buffer_load_dwordx4 v254, s[40:43], s62 offen lds\n"
    "s_add_u32 m0, s68, 0xc400\n"
    "s_nop 0\n"
    "buffer_load_dwordx4 v254, s[40:43], s63 offen lds\n"
    "s_add_u32 s62, s62, s66\n"
    "s_add_u32 s63, s63, s66\n"
    "s_add_u32 m0, s68, 0xe000\n"
    "s_nop 0\n"
    "buffer_load_dwordx4 v254, s[40:43], s62 offen lds\n"
    "s_add_u32 m0, s68, 0xe400\n"
    "s_nop 0\n"
    "buffer_load_dwordx4 v254, s[40:43], s63 offen lds\n"
    "s_add_u32 s62, s62, s66\n"
    "s_add_u32 s63, s63, s66\n"
    "s_add_u32 m0, s68, 0x10000\n"
    "s_nop 0\n"
    "buffer_load_dwordx4 v254, s[40:43], s62 offen lds\n"
    "s_add_u32 m0, s68, 0x10400\n"
    "s_nop 0\n"
    "buffer_load_dwordx4 v254, s[40:43], s63 offen lds\n"
    "s_add_u32 s62, s62, s66\n"
    "s_add_u32 s63, s63, s66\n"
    "G5%=:\n"
    "s_add_u32 m0, s68, 0x12000\n"
    "s_nop 0\n"
    "buffer_load_dwordx4 v254, s[40:43], s62 offen lds\n"
    "s_add_u32 m0, s68, 0x12400\n"
    "s_nop 0\n"
    "buffer_load_dwordx4 v254, s[40:43], s63 offen lds\n"
    "s_add_u32 s62, s62, s66\n"
    "s_add_u32 s63, s63, s66\n"
    "s_add_u32 m0, s68, 0x14000\n"
    "s_nop 0\n"
    "buffer_load_dwordx4 v254, s[40:43], s62 offen lds\n"
    "s_add_u32 m0, s68, 0x14400\n"
    "s_nop 0\n"
    "buffer_load_dwordx4 v254, s[40:43], s63 offen lds\n"
    "s_add_u32 s62, s62, s66\n"
    "s_add_u32 s63, s63, s66\n"
    "s_add_u32 m0, s68, 0x16000\n"
    "s_nop 0\n"
    "buffer_load_dwordx4 v254, s[40:43], s62 offen lds\n"
    "s_add_u32 m0, s68, 0x16400\n"
    "s_nop 0\n"
    "buffer_load_dwordx4 v254, s[40:43], s63 offen lds\n"
    "s_add_u32 s62, s62, s66\n"
    "s_add_u32 s63, s63, s66\n"
    "s_sub_u32 s61, s77, 20\n"
    "s_cmp_lt_u32 s61, 44\n"
    "s_cbranch_scc0 G6%=\n"
    "v_bfe_u32 v192, v255, 4, 5\n"
    "v_lshlrev_b32_e32 v192, 3, v192\n"
    "v_add_u32_e32 v194, 0x20100, v192\n"
    "v_lshrrev_b32_e32 v193, 9, v255\n"
    "v_or_b32_e32 v193, s36, v193\n"
    "v_cmp_eq_u32_e32 vcc, 0, v193\n"
    "v_mov_b32_e32 v193, 0x40000000\n"
    "s_nop 0\n"
    "v_cndmask_b32_e64 v192, v193, v192, vcc\n"
    "s_sub_u32 s88, s77, 20\n"
    "s_lshl_b32 s87, s88, 8\n"
    "s_mul_i32 s60, s71, 0x2800\n"
    "s_add_u32 s87, s87, s60\n"
    "s_add_u32 s87, s87, 0x18000\n"
    "s_cmp_lt_u32 s88, 40\n"
    "s_cselect_b32 s87, s87, 0x40000000\n"
    "v_lshlrev_b32_e64 v164, 16, v164\n"
    "v_mul_f32_e32 v169, 0xbfb8aa3b, v164\n"
    "v_exp_f32_e32 v169, v169\n"
    "v_lshlrev_b32_e32 v165, 16, v165\n"
    "v_mul_f32_e32 v170, 0xbfb8aa3b, v165\n"
    "v_exp_f32_e32 v170, v170\n"
    "v_lshlrev_b32_e32 v166, 16, v166\n"
    "v_mul_f32_e32 v171, 0xbfb8aa3b, v166\n"
    "v_exp_f32_e32 v171, v171\n"
    "v_lshlrev_b32_e32 v167, 16, v167\n"
    "v_mul_f32_e32 v172, 0xbfb8aa3b, v167\n"
    "v_exp_f32_e32 v172, v172\n"
    "v_lshlrev_b32_e32 v168, 16, v168\n"
    "v_mul_f32_e32 v173, 0xbfb8aa3b, v168\n"
    "v_exp_f32_e32 v173, v173\n"
    "v_add_f32_e32 v169, 1.0, v169\n"
    "v_rcp_f32_e32 v169, v169\n"
    "v_add_f32_e32 v170, 1.0, v170\n"
    "v_rcp_f32_e32 v170, v170\n"
    "v_add_f32_e32 v171, 1.0, v171\n"
    "v_rcp_f32_e32 v171, v171\n"
    "v_add_f32_e32 v172, 1.0, v172\n"
    "v_rcp_f32_e32 v172, v172\n"
    "v_add_f32_e32 v173, 1.0, v173\n"
    "v_rcp_f32_e32 v173, v173\n"
    "v_mul_f32_e32 v169, v164, v169\n"
    "v_cvt_pk_bf16_f32 v164, 0, v169\n"
    "v_mul_f32_e64 v170, v165, v170\n"
    "v_cvt_pk_bf16_f32 v165, 0, v170\n"
    "v_mul_f32_e64 v171, v166, v171\n"
    "v_cvt_pk_bf16_f32 v166, 0, v171\n"
    "v_mul_f32_e64 v172, v167, v172\n"
    "v_cvt_pk_bf16_f32 v167, 0, v172\n"
    "v_mul_f32_e64 v173, v168, v173\n"
    "v_cvt_pk_bf16_f32 v168, 0, v173\n"
    "v_lshrrev_b32_e64 v169, 8, v255\n"
    "v_bfe_u32 v170, v255, 4, 3\n"
    "v_lshl_add_u32 v169, v170, 2, v169\n"
    "v_lshlrev_b32_e32 v169, 3, v169\n"
    "s_lshl_b32 s60, s36, 8\n"
    "s_add_u32 s60, s60, 0x20100\n"
    "v_add_u32_e32 v169, s60, v169\n"
    "v_lshlrev_b32_e32 v184, 16, v144\n"
    "v_and_b32_e32 v185, 0xffff0000, v144\n"
    "v_lshlrev_b32_e64 v186, 16, v145\n"
    "v_and_b32_e32 v187, 0xffff0000, v145\n"
    "v_lshlrev_b32_e64 v188, 16, v146\n"
    "v_and_b32_e32 v189, 0xffff0000, v146\n"
    "v_lshlrev_b32_e64 v190, 16, v147\n"
    "v_and_b32_e32 v191, 0xffff0000, v147\n"
    "v_pk_mul_f32 v[176:177], v[164:165], v[184:185] op_sel_hi:[0,1]\n"
    "v_pk_mul_f32 v[178:179], v[164:165], v[186:187] op_sel_hi:[0,1]\n"
    "v_pk_mul_f32 v[180:181], v[164:165], v[188:189] op_sel_hi:[0,1]\n"
    "v_pk_mul_f32 v[182:183], v[164:165], v[190:191] op_sel_hi:[0,1]\n"
    "v_lshlrev_b32_e64 v184, 16, v148\n"
    "v_and_b32_e32 v185, 0xffff0000, v148\n"
    "v_lshlrev_b32_e64 v186, 16, v149\n"
    "v_and_b32_e32 v187, 0xffff0000, v149\n"
    "v_lshlrev_b32_e64 v188, 16, v150\n"
    "v_and_b32_e32 v189, 0xffff0000, v150\n"
    "v_lshlrev_b32_e64 v190, 16, v151\n"
    "v_and_b32_e32 v191, 0xffff0000, v151\n"
    "v_pk_fma_f32 v[176:177], v[164:165], v[184:185], v[176:177] op_sel:[1,0,0]\n"
    "v_pk_fma_f32 v[178:179], v[164:165], v[186:187], v[178:179] op_sel:[1,0,0]\n"
    "v_pk_fma_f32 v[180:181], v[164:165], v[188:189], v[180:181] op_sel:[1,0,0]\n"
    "v_pk_fma_f32 v[182:183], v[164:165], v[190:191], v[182:183] op_sel:[1,0,0]\n"
    "v_lshlrev_b32_e64 v184, 16, v152\n"
    "v_and_b32_e32 v185, 0xffff0000, v152\n"
    "v_lshlrev_b32_e64 v186, 16, v153\n"
    "v_and_b32_e32 v187, 0xffff0000, v153\n"
    "v_lshlrev_b32_e64 v188, 16, v154\n"
    "v_and_b32_e32 v189, 0xffff0000, v154\n"
    "v_lshlrev_b32_e64 v190, 16, v155\n"
    "v_and_b32_e32 v191, 0xffff0000, v155\n"
    "v_pk_fma_f32 v[176:177], v[166:167], v[184:185], v[176:177] op_sel_hi:[0,1,1]\n"
    "v_pk_fma_f32 v[178:179], v[166:167], v[186:187], v[178:179] op_sel_hi:[0,1,1]\n"
    "v_pk_fma_f32 v[180:181], v[166:167], v[188:189], v[180:181] op_sel_hi:[0,1,1]\n"
    "v_pk_fma_f32 v[182:183], v[166:167], v[190:191], v[182:183] op_sel_hi:[0,1,1]\n"
    "v_lshlrev_b32_e64 v184, 16, v156\n"
    "v_and_b32_e32 v185, 0xffff0000, v156\n"
    "v_lshlrev_b32_e64 v186, 16, v157\n"
    "v_and_b32_e32 v187, 0xffff0000, v157\n"
    "v_lshlrev_b32_e64 v188, 16, v158\n"
    "v_and_b32_e32 v189, 0xffff0000, v158\n"
    "v_lshlrev_b32_e64 v190, 16, v159\n"
    "v_and_b32_e32 v191, 0xffff0000, v159\n"
    "v_pk_fma_f32 v[176:177], v[166:167], v[184:185], v[176:177] op_sel:[1,0,0]\n"
    "v_pk_fma_f32 v[178:179], v[166:167], v[186:187], v[178:179] op_sel:[1,0,0]\n"
    "v_pk_fma_f32 v[180:181], v[166:167], v[188:189], v[180:181] op_sel:[1,0,0]\n"
    "v_pk_fma_f32 v[182:183], v[166:167], v[190:191], v[182:183] op_sel:[1,0,0]\n"
    "v_lshlrev_b32_e64 v184, 16, v160\n"
    "v_and_b32_e32 v185, 0xffff0000, v160\n"
    "v_lshlrev_b32_e64 v186, 16, v161\n"
    "v_and_b32_e32 v187, 0xffff0000, v161\n"
    "v_lshlrev_b32_e64 v188, 16, v162\n"
    "v_and_b32_e32 v189, 0xffff0000, v162\n"
    "v_lshlrev_b32_e64 v190, 16, v163\n"
    "v_and_b32_e32 v191, 0xffff0000, v163\n"
    "v_pk_fma_f32 v[176:177], v[168:169], v[184:185], v[176:177] op_sel_hi:[0,1,1]\n"
    "v_pk_fma_f32 v[178:179], v[168:169], v[186:187], v[178:179] op_sel_hi:[0,1,1]\n"
    "v_pk_fma_f32 v[180:181], v[168:169], v[188:189], v[180:181] op_sel_hi:[0,1,1]\n"
    "v_pk_fma_f32 v[182:183], v[168:169], v[190:191], v[182:183] op_sel_hi:[0,1,1]\n"
    "s_nop 0\n"
    "v_permlane32_swap_b32 v176, v180\n"
    "v_permlane32_swap_b32 v177, v181\n"
    "v_permlane32_swap_b32 v178, v182\n"
    "v_permlane32_swap_b32 v179, v183\n"
    "v_add_f32_e32 v176, v176, v180\n"
    "v_add_f32_e32 v177, v177, v181\n"
    "v_add_f32_e32 v178, v178, v182\n"
    "v_add_f32_e32 v179, v179, v183\n"
    "s_nop 0\n"
    "v_permlane16_swap_b32 v176, v178\n"
    "v_permlane16_swap_b32 v177, v179\n"
    "v_add_f32_e32 v176, v176, v178\n"
    "v_add_f32_e32 v177, v177, v179\n"
    "s_nop 0\n"
    "v_add_f32_dpp v176, v176, v176 row_ror:8 row_mask:0xf bank_mask:0xf\n"
    "v_add_f32_dpp v177, v177, v177 row_ror:8 row_mask:0xf bank_mask:0xf\n"
    "ds_write_b64 v169, v[176:177] offset:0\n"
    "s_waitcnt lgkmcnt(0)\n"
    "s_barrier\n"
    "ds_read_b64 v[176:177], v194 offset:0\n"
    "ds_read_b64 v[178:179], v194 offset:256\n"
    "ds_read_b64 v[180:181], v194 offset:512\n"
    "ds_read_b64 v[182:183], v194 offset:768\n"
    "s_waitcnt lgkmcnt(2)\n"
    "v_add_f32_e32 v176, v176, v178\n"
    "s_waitcnt lgkmcnt(0)\n"
    "v_add_f32_e32 v180, v180, v182\n"
    "v_add_f32_e32 v177, v177, v179\n"
    "v_add_f32_e32 v181, v181, v183\n"
    "v_add_f32_e32 v176, v176, v180\n"
    "v_add_f32_e64 v177, v177, v181\n"
    "buffer_store_dwordx2 v[176:177], v192, s[48:51], s87 offen sc1\n"
    "G6%=:\n"
    "s_mov_b32 s56, s30\n"
    "s_and_b32 s57, s31, 0xffff\n"
    "s_mov_b32 s58, 0x280000\n"
    "s_mov_b32 s59, 0x20000\n"
    "s_lshr_b32 s38, s37, 5\n"
    "s_and_b32 s39, s37, 1\n"
    "s_lshl_b32 s39, s39, 3\n"
    "s_add_u32 s89, s38, s39\n"
    "s_lshr_b32 s38, s37, 3\n"
    "s_and_b32 s38, s38, 3\n"
    "s_and_b32 s39, s37, 6\n"
    "s_lshl_b32 s60, s39, 1\n"
    "s_add_u32 s90, s38, s60\n"
    "s_lshl_b32 s38, s38, 1\n"
    "s_lshr_b32 s60, s36, 1\n"
    "s_add_u32 s38, s38, s60\n"
    "s_lshl_b32 s38, s38, 2\n"
    "s_lshr_b32 s39, s39, 1\n"
    "s_add_u32 s38, s38, s39\n"
    "s_mul_i32 s87, s38, 0x28000\n"
    "s_add_u32 s87, s87, 0x1400000\n"
    "s_and_b32 s38, s36, 1\n"
    "s_lshl_b32 s38, s38, 11\n"
    "s_add_u32 s80, s87, s38\n"
    "s_mov_b32 s79, s81\n"
    "s_lshl_b32 s38, s89, 12\n"
    "s_lshl_b32 s39, s90, 2\n"
    "s_add_u32 s39, s39, s38\n"
    "s_add_u32 s39, s39, 0x40000\n"
    "s_cmp_eq_u32 s36, 1\n"
    "s_cselect_b32 s79, s39, s79\n"
    "s_lshr_b32 s39, s90, 2\n"
    "s_lshl_b32 s39, s39, 10\n"
    "s_add_u32 s38, s38, s39\n"
    "s_and_b32 s60, s90, 3\n"
    "s_lshl_b32 s39, s60, 2\n"
    "s_add_u32 s39, s39, s38\n"
    "s_add_u32 s39, s39, 0x50000\n"
    "s_cmp_eq_u32 s36, 2\n"
    "s_cselect_b32 s79, s39, s79\n"
    "s_lshr_b32 s38, s38, 3\n"
    "s_lshl_b32 s60, s60, 2\n"
    "s_add_u32 s38, s38, s60\n"
    "s_add_u32 s38, s38, 0x4000\n"
    "s_cmp_eq_u32 s36, 3\n"
    "s_cselect_b32 s79, s38, s79\n"
    "s_lshl_b32 s93, s74, 11\n"
    "s_add_u32 s93, s93, 0x60000\n"
    "v_bfe_u32 v240, v255, 4, 3\n"
    "v_lshlrev_b32_e32 v240, 2, v240\n"
    "buffer_load_dword v229, v240, s[48:51], s93 offen sc1\n"
    "s_add_u32 m0, s68, 0x18000\n"
    "s_nop 0\n"
    "buffer_load_dwordx4 v254, s[40:43], s62 offen lds\n"
    "s_add_u32 m0, s68, 0x18400\n"
    "s_nop 0\n"
    "buffer_load_dwordx4 v254, s[40:43], s63 offen lds\n"
    "s_add_u32 s62, s62, s66\n"
    "s_add_u32 s63, s63, s66\n"
    "s_waitcnt vmcnt(2)\n"
    "v_sub_u32_e32 v229, v229, v230\n"
    "v_subrev_u32_e32 v229, 1, v229\n"
    "v_cmp_gt_u32_e32 vcc, 3, v229\n"
    "s_nop 1\n"
    "s_cmp_eq_u64 vcc, -1\n"
    "s_cbranch_scc1 POK1_%=\n"
    "s_mov_b32 s61, 20000\n"
    "PSP1_%=:\n"
    "s_sleep 4\n"
    "buffer_load_dword v229, v240, s[48:51], s93 offen sc1\n"
    "s_waitcnt vmcnt(0)\n"
    "v_sub_u32_e32 v229, v229, v230\n"
    "v_subrev_u32_e32 v229, 1, v229\n"
    "v_cmp_gt_u32_e32 vcc, 3, v229\n"
    "s_nop 1\n"
    "s_cmp_eq_u64 vcc, -1\n"
    "s_cbranch_scc1 POK1_%=\n"
    "s_sub_u32 s61, s61, 1\n"
    "s_cmp_lg_u32 s61, 0\n"
    "s_cbranch_scc1 PSP1_%=\n"
    "POK1_%=:\n"
    "buffer_load_dwordx4 a[0:3], v255, s[48:51], s69 offen sc1\n"
    "buffer_load_dwordx4 a[4:7], v255, s[48:51], s69 offen offset:1024 sc1\n"
    "buffer_load_dwordx4 a[8:11], v255, s[48:51], s69 offen offset:2048 sc1\n"
    "buffer_load_dwordx4 a[12:15], v255, s[48:51], s69 offen offset:3072 sc1\n"
    "s_add_u32 s69, s69, 0x1000\n"
    "buffer_load_dwordx4 a[16:19], v255, s[48:51], s69 offen sc1\n"
    "buffer_load_dwordx4 a[20:23], v255, s[48:51], s69 offen offset:1024 sc1\n"
    "buffer_load_dwordx4 a[24:27], v255, s[48:51], s69 offen offset:2048 sc1\n"
    "buffer_load_dwordx4 a[28:31], v255, s[48:51], s69 offen offset:3072 sc1\n"
    "s_add_u32 s69, s69, 0x1000\n"
    "s_waitcnt lgkmcnt(0)\n"
    "s_barrier\n"
    "v_mov_b32_e32 v248, v244\n"
    "v_mov_b32_e32 v247, v242\n"
    "ds_read_b64_tr_b16 a[128:129], v248 offset:24576\n"
    "ds_read_b64_tr_b16 a[130:131], v248 offset:24832\n"
    "v_add_u32_e32 v246, 0x8000, v244\n"
    "v_add_u32_e32 v245, 0x8000, v242\n"
    "ds_read_b64_tr_b16 a[132:133], v246 offset:0\n"
    "ds_read_b64_tr_b16 a[134:135], v246 offset:256\n"
    "ds_read_b64_tr_b16 a[136:137], v247 offset:24576\n"
    "ds_read_b64_tr_b16 a[138:139], v247 offset:24832\n"
    "ds_read_b64_tr_b16 a[140:141], v245 offset:0\n"
    "ds_read_b64_tr_b16 a[142:143], v245 offset:256\n"
    "ds_read_b64_tr_b16 a[144:145], v248 offset:25088\n"
    "ds_read_b64_tr_b16 a[146:147], v248 offset:25344\n"
    "ds_read_b64_tr_b16 a[148:149], v246 offset:512\n"
    "ds_read_b64_tr_b16 a[150:151], v246 offset:768\n"
    "s_waitcnt vmcnt(7) lgkmcnt(10)\n"
    "v_mfma_f32_16x16x32_bf16 v[192:195], a[128:131], a[0:3], 0\n"
    "s_waitcnt vmcnt(3) lgkmcnt(8)\n"
    "ds_read_b64_tr_b16 a[152:153], v247 offset:25088\n"
    "buffer_load_dwordx4 a[32:35], v255, s[48:51], s69 offen sc1\n"
    "v_mfma_f32_16x16x32_bf16 v[192:195], a[132:135], a[16:19], v[192:195]\n"
    "v_mfma_f32_16x16x32_bf16 v[128:131], a[128:131], a[4:7], 0\n"
    "s_waitcnt vmcnt(3)\n"
    "ds_read_b64_tr_b16 a[154:155], v247 offset:25344\n"
    "v_mfma_f32_16x16x32_bf16 v[128:131], a[132:135], a[20:23], v[128:131]\n"
    "buffer_load_dwordx4 a[36:39], v255, s[48:51], s69 offen offset:1024 sc1\n"
    "v_mfma_f32_16x16x32_bf16 v[96:99], a[128:131], a[8:11], 0\n"
    "s_waitcnt vmcnt(3)\n"
    "ds_read_b64_tr_b16 a[156:157], v245 offset:512\n"
    "v_mfma_f32_16x16x32_bf16 v[96:99], a[132:135], a[24:27], v[96:99]\n"
    "v_mfma_f32_16x16x32_bf16 v[160:163], a[128:131], a[12:15], 0\n"
    "s_waitcnt vmcnt(2)\n"
    "ds_read_b64_tr_b16 a[158:159], v245 offset:768\n"
    "v_mfma_f32_16x16x32_bf16 v[160:163], a[132:135], a[28:31], v[160:163]\n"
    "s_waitcnt lgkmcnt(10)\n"
    "buffer_load_dwordx4 a[40:43], v255, s[48:51], s69 offen offset:2048 sc1\n"
    "v_mfma_f32_16x16x32_bf16 v[164:167], a[136:139], a[12:15], 0\n"
    "s_waitcnt lgkmcnt(8)\n"
    "ds_read_b64_tr_b16 a[160:161], v248 offset:25600\n"
    "v_mfma_f32_16x16x32_bf16 v[164:167], a[140:143], a[28:31], v[164:167]\n"
    "v_mfma_f32_16x16x32_bf16 v[100:103], a[136:139], a[8:11], 0\n"
    "ds_read_b64_tr_b16 a[162:163], v248 offset:25856\n"
    "v_mfma_f32_16x16x32_bf16 v[100:103], a[140:143], a[24:27], v[100:103]\n"
    "buffer_load_dwordx4 a[44:47], v255, s[48:51], s69 offen offset:3072 sc1\n"
    "v_mfma_f32_16x16x32_bf16 v[132:135], a[136:139], a[4:7], 0\n"
    "ds_read_b64_tr_b16 a[164:165], v246 offset:1024\n"
    "v_mfma_f32_16x16x32_bf16 v[132:135], a[140:143], a[20:23], v[132:135]\n"
    "v_mfma_f32_16x16x32_bf16 v[196:199], a[136:139], a[0:3], 0\n"
    "s_add_u32 s69, s69, 0x1000\n"
    "ds_read_b64_tr_b16 a[166:167], v246 offset:1280\n"
    "v_mfma_f32_16x16x32_bf16 v[196:199], a[140:143], a[16:19], v[196:199]\n"
    "s_waitcnt lgkmcnt(10)\n"
    "buffer_load_dwordx4 a[48:51], v255, s[48:51], s69 offen sc1\n"
    "v_mfma_f32_16x16x32_bf16 v[200:203], a[144:147], a[0:3], 0\n"
    "s_waitcnt lgkmcnt(8)\n"
    "ds_read_b64_tr_b16 a[168:169], v247 offset:25600\n"
    "v_mfma_f32_16x16x32_bf16 v[200:203], a[148:151], a[16:19], v[200:203]\n"
    "v_mfma_f32_16x16x32_bf16 v[136:139], a[144:147], a[4:7], 0\n"
    "ds_read_b64_tr_b16 a[170:171], v247 offset:25856\n"
    "v_mfma_f32_16x16x32_bf16 v[136:139], a[148:151], a[20:23], v[136:139]\n"
    "buffer_load_dwordx4 a[52:55], v255, s[48:51], s69 offen offset:1024 sc1\n"
    "v_mfma_f32_16x16x32_bf16 v[104:107], a[144:147], a[8:11], 0\n"
    "ds_read_b64_tr_b16 a[172:173], v245 offset:1024\n"
    "v_mfma_f32_16x16x32_bf16 v[104:107], a[148:151], a[24:27], v[104:107]\n"
    "v_mfma_f32_16x16x32_bf16 v[168:171], a[144:147], a[12:15], 0\n"
    "ds_read_b64_tr_b16 a[174:175], v245 offset:1280\n"
    "v_mfma_f32_16x16x32_bf16 v[168:171], a[148:151], a[28:31], v[168:171]\n"
    "s_waitcnt lgkmcnt(10)\n"
    "buffer_load_dwordx4 a[56:59], v255, s[48:51], s69 offen offset:2048 sc1\n"
    "v_mfma_f32_16x16x32_bf16 v[172:175], a[152:155], a[12:15], 0\n"
    "s_waitcnt lgkmcnt(8)\n"
    "ds_read_b64_tr_b16 a[176:177], v248 offset:26112\n"
    "v_mfma_f32_16x16x32_bf16 v[172:175], a[156:159], a[28:31], v[172:175]\n"
    "v_mfma_f32_16x16x32_bf16 v[108:111], a[152:155], a[8:11], 0\n"
    "ds_read_b64_tr_b16 a[178:179], v248 offset:26368\n"
    "v_mfma_f32_16x16x32_bf16 v[108:111], a[156:159], a[24:27], v[108:111]\n"
    "buffer_load_dwordx4 a[60:63], v255, s[48:51], s69 offen offset:3072 sc1\n"
    "v_mfma_f32_16x16x32_bf16 v[140:143], a[152:155], a[4:7], 0\n"
    "ds_read_b64_tr_b16 a[180:181], v246 offset:1536\n"
    "v_mfma_f32_16x16x32_bf16 v[140:143], a[156:159], a[20:23], v[140:143]\n"
    "v_mfma_f32_16x16x32_bf16 v[204:207], a[152:155], a[0:3], 0\n"
    "s_add_u32 s69, s69, 0x1000\n"
    "ds_read_b64_tr_b16 a[182:183], v246 offset:1792\n"
    "v_mfma_f32_16x16x32_bf16 v[204:207], a[156:159], a[16:19], v[204:207]\n"
    "s_waitcnt lgkmcnt(10)\n"
    "buffer_load_dwordx4 a[64:67], v255, s[48:51], s69 offen sc1\n"
    "v_mfma_f32_16x16x32_bf16 v[208:211], a[160:163], a[0:3], 0\n"
    "s_waitcnt lgkmcnt(8)\n"
    "ds_read_b64_tr_b16 a[184:185], v247 offset:26112\n"
    "v_mfma_f32_16x16x32_bf16 v[208:211], a[164:167], a[16:19], v[208:211]\n"
    "v_mfma_f32_16x16x32_bf16 v[144:147], a[160:163], a[4:7], 0\n"
    "ds_read_b64_tr_b16 a[186:187], v247 offset:26368\n"
    "buffer_load_dwordx4 a[68:71], v255, s[48:51], s69 offen offset:1024 sc1\n"
    "v_mfma_f32_16x16x32_bf16 v[144:147], a[164:167], a[20:23], v[144:147]\n"
    "v_mfma_f32_16x16x32_bf16 v[112:115], a[160:163], a[8:11], 0\n"
    "ds_read_b64_tr_b16 a[188:189], v245 offset:1536\n"
    "v_mfma_f32_16x16x32_bf16 v[112:115], a[164:167], a[24:27], v[112:115]\n"
    "v_mfma_f32_16x16x32_bf16 v[176:179], a[160:163], a[12:15], 0\n"
    "ds_read_b64_tr_b16 a[190:191], v245 offset:1792\n"
    "buffer_load_dwordx4 a[72:75], v255, s[48:51], s69 offen offset:2048 sc1\n"
    "v_mfma_f32_16x16x32_bf16 v[176:179], a[164:167], a[28:31], v[176:179]\n"
    "s_waitcnt lgkmcnt(10)\n"
    "v_mfma_f32_16x16x32_bf16 v[180:183], a[168:171], a[12:15], 0\n"
    "s_waitcnt lgkmcnt(8)\n"
    "s_barrier\n"
    "v_add_u32_e32 v248, 0x8000, v244\n"
    "v_mfma_f32_16x16x32_bf16 v[180:183], a[172:175], a[28:31], v[180:183]\n"
    "v_add_u32_e32 v247, 0x8000, v242\n"
    "v_mfma_f32_16x16x32_bf16 v[116:119], a[168:171], a[8:11], 0\n"
    "buffer_load_dwordx4 a[76:79], v255, s[48:51], s69 offen offset:3072 sc1\n"
    "ds_read_b64_tr_b16 a[128:129], v248 offset:8192\n"
    "v_mfma_f32_16x16x32_bf16 v[116:119], a[172:175], a[24:27], v[116:119]\n"
    "v_mfma_f32_16x16x32_bf16 v[148:151], a[168:171], a[4:7], 0\n"
    "ds_read_b64_tr_b16 a[130:131], v248 offset:8448\n"
    "v_mfma_f32_16x16x32_bf16 v[148:151], a[172:175], a[20:23], v[148:151]\n"
    "s_add_u32 s69, s69, 0x1000\n"
    "ds_read_b64_tr_b16 a[132:133], v246 offset:16384\n"
    "v_mfma_f32_16x16x32_bf16 v[212:215], a[168:171], a[0:3], 0\n"
    "buffer_load_dwordx4 a[80:83], v255, s[48:51], s69 offen sc1\n"
    "ds_read_b64_tr_b16 a[134:135], v246 offset:16640\n"
    "v_mfma_f32_16x16x32_bf16 v[212:215], a[172:175], a[16:19], v[212:215]\n"
    "s_waitcnt lgkmcnt(10)\n"
    "v_mfma_f32_16x16x32_bf16 v[216:219], a[176:179], a[0:3], 0\n"
    "s_waitcnt lgkmcnt(8)\n"
    "ds_read_b64_tr_b16 a[136:137], v247 offset:8192\n"
    "v_mfma_f32_16x16x32_bf16 v[216:219], a[180:183], a[16:19], v[216:219]\n"
    "v_mfma_f32_16x16x32_bf16 v[152:155], a[176:179], a[4:7], 0\n"
    "ds_read_b64_tr_b16 a[138:139], v247 offset:8448\n"
    "buffer_load_dwordx4 a[84:87], v255, s[48:51], s69 offen offset:1024 sc1\n"
    "v_mfma_f32_16x16x32_bf16 v[152:155], a[180:183], a[20:23], v[152:155]\n"
    "v_mfma_f32_16x16x32_bf16 v[120:123], a[176:179], a[8:11], 0\n"
    "ds_read_b64_tr_b16 a[140:141], v245 offset:16384\n"
    "v_mfma_f32_16x16x32_bf16 v[120:123], a[180:183], a[24:27], v[120:123]\n"
    "v_mfma_f32_16x16x32_bf16 v[184:187], a[176:179], a[12:15], 0\n"
    "ds_read_b64_tr_b16 a[142:143], v245 offset:16640\n"
    "buffer_load_dwordx4 a[88:91], v255, s[48:51], s69 offen offset:2048 sc1\n"
    "v_mfma_f32_16x16x32_bf16 v[184:187], a[180:183], a[28:31], v[184:187]\n"
    "s_waitcnt lgkmcnt(10)\n"
    "v_mfma_f32_16x16x32_bf16 v[188:191], a[184:187], a[12:15], 0\n"
    "s_waitcnt lgkmcnt(8)\n"
    "ds_read_b64_tr_b16 a[144:145], v248 offset:8704\n"
    "v_mfma_f32_16x16x32_bf16 v[188:191], a[188:191], a[28:31], v[188:191]\n"
    "v_mfma_f32_16x16x32_bf16 v[124:127], a[184:187], a[8:11], 0\n"
    "ds_read_b64_tr_b16 a[146:147], v248 offset:8960\n"
    "buffer_load_dwordx4 a[92:95], v255, s[48:51], s69 offen offset:3072 sc1\n"
    "v_mfma_f32_16x16x32_bf16 v[124:127], a[188:191], a[24:27], v[124:127]\n"
    "v_mfma_f32_16x16x32_bf16 v[156:159], a[184:187], a[4:7], 0\n"
    "ds_read_b64_tr_b16 a[148:149], v246 offset:16896\n"
    "v_mfma_f32_16x16x32_bf16 v[156:159], a[188:191], a[20:23], v[156:159]\n"
    "s_add_u32 s69, s69, 0x1000\n"
    "v_mfma_f32_16x16x32_bf16 v[220:223], a[184:187], a[0:3], 0\n"
    "ds_read_b64_tr_b16 a[150:151], v246 offset:17152\n"
    "v_mfma_f32_16x16x32_bf16 v[220:223], a[188:191], a[16:19], v[220:223]\n"
    "s_waitcnt vmcnt(15) lgkmcnt(10)\n"
    "v_mfma_f32_16x16x32_bf16 v[192:195], a[128:131], a[32:35], v[192:195]\n"
    "s_waitcnt vmcnt(11) lgkmcnt(8)\n"
    "ds_read_b64_tr_b16 a[152:153], v247 offset:8704\n"
    "buffer_load_dwordx4 a[96:99], v255, s[48:51], s69 offen sc1\n"
    "v_mfma_f32_16x16x32_bf16 v[192:195], a[132:135], a[48:51], v[192:195]\n"
    "v_mfma_f32_16x16x32_bf16 v[128:131], a[128:131], a[36:39], v[128:131]\n"
    "s_waitcnt vmcnt(11)\n"
    "ds_read_b64_tr_b16 a[154:155], v247 offset:8960\n"
    "v_mfma_f32_16x16x32_bf16 v[128:131], a[132:135], a[52:55], v[128:131]\n"
    "v_mfma_f32_16x16x32_bf16 v[96:99], a[128:131], a[40:43], v[96:99]\n"
    "s_waitcnt vmcnt(10)\n"
    "ds_read_b64_tr_b16 a[156:157], v245 offset:16896\n"
    "buffer_load_dwordx4 a[100:103], v255, s[48:51], s69 offen offset:1024 sc1\n"
    "v_mfma_f32_16x16x32_bf16 v[96:99], a[132:135], a[56:59], v[96:99]\n"
    "v_mfma_f32_16x16x32_bf16 v[160:163], a[128:131], a[44:47], v[160:163]\n"
    "s_waitcnt vmcnt(10)\n"
    "ds_read_b64_tr_b16 a[158:159], v245 offset:17152\n"
    "v_mfma_f32_16x16x32_bf16 v[160:163], a[132:135], a[60:63], v[160:163]\n"
    "s_waitcnt lgkmcnt(10)\n"
    "v_mfma_f32_16x16x32_bf16 v[164:167], a[136:139], a[44:47], v[164:167]\n"
    "s_waitcnt lgkmcnt(8)\n"
    "ds_read_b64_tr_b16 a[160:161], v248 offset:9216\n"
    "buffer_load_dwordx4 a[104:107], v255, s[48:51], s69 offen offset:2048 sc1\n"
    "v_mfma_f32_16x16x32_bf16 v[164:167], a[140:143], a[60:63], v[164:167]\n"
    "v_mfma_f32_16x16x32_bf16 v[100:103], a[136:139], a[40:43], v[100:103]\n"
    "ds_read_b64_tr_b16 a[162:163], v248 offset:9472\n"
    "v_bfe_u32 v240, v255, 6, 3\n"
    "v_mfma_f32_16x16x32_bf16 v[100:103], a[140:143], a[56:59], v[100:103]\n"
    "v_mfma_f32_16x16x32_bf16 v[132:135], a[136:139], a[36:39], v[132:135]\n"
    "ds_read_b64_tr_b16 a[164:165], v246 offset:17408\n"
    "v_mfma_f32_16x16x32_bf16 v[132:135], a[140:143], a[52:55], v[132:135]\n"
    "buffer_load_dwordx4 a[108:111], v255, s[48:51], s69 offen offset:3072 sc1\n"
    "v_mfma_f32_16x16x32_bf16 v[196:199], a[136:139], a[32:35], v[196:199]\n"
    "ds_read_b64_tr_b16 a[166:167], v246 offset:17664\n"
    "v_mfma_f32_16x16x32_bf16 v[196:199], a[140:143], a[48:51], v[196:199]\n"
    "s_waitcnt lgkmcnt(10)\n"
    "v_mfma_f32_16x16x32_bf16 v[200:203], a[144:147], a[32:35], v[200:203]\n"
    "s_waitcnt lgkmcnt(8)\n"
    "s_add_u32 s69, s69, 0x1000\n"
    "ds_read_b64_tr_b16 a[168:169], v247 offset:9216\n"
    "v_mfma_f32_16x16x32_bf16 v[200:203], a[148:151], a[48:51], v[200:203]\n"
    "PG1_%=:\n"
    "s_cbranch_execz PG5_%=\n"
    "buffer_load_dwordx4 a[112:115], v255, s[48:51], s69 offen sc1\n"
    "v_mfma_f32_16x16x32_bf16 v[136:139], a[144:147], a[36:39], v[136:139]\n"
    "ds_read_b64_tr_b16 a[170:171], v247 offset:9472\n"
    "v_mfma_f32_16x16x32_bf16 v[136:139], a[148:151], a[52:55], v[136:139]\n"
    "v_mfma_f32_16x16x32_bf16 v[104:107], a[144:147], a[40:43], v[104:107]\n"
    "ds_read_b64_tr_b16 a[172:173], v245 offset:17408\n"
    "v_mfma_f32_16x16x32_bf16 v[104:107], a[148:151], a[56:59], v[104:107]\n"
    "v_mfma_f32_16x16x32_bf16 v[168:171], a[144:147], a[44:47], v[168:171]\n"
    "ds_read_b64_tr_b16 a[174:175], v245 offset:17664\n"
    "buffer_load_dwordx4 a[116:119], v255, s[48:51], s69 offen offset:1024 sc1\n"
    "v_mfma_f32_16x16x32_bf16 v[168:171], a[148:151], a[60:63], v[168:171]\n"
    "s_waitcnt lgkmcnt(10)\n"
    "v_mfma_f32_16x16x32_bf16 v[172:175], a[152:155], a[44:47], v[172:175]\n"
    "s_waitcnt lgkmcnt(8)\n"
    "ds_read_b64_tr_b16 a[176:177], v248 offset:9728\n"
    "v_mfma_f32_16x16x32_bf16 v[172:175], a[156:159], a[60:63], v[172:175]\n"
    "v_mfma_f32_16x16x32_bf16 v[108:111], a[152:155], a[40:43], v[108:111]\n"
    "ds_read_b64_tr_b16 a[178:179], v248 offset:9984\n"
    "buffer_load_dwordx4 a[120:123], v255, s[48:51], s69 offen offset:2048 sc1\n"
    "v_mfma_f32_16x16x32_bf16 v[108:111], a[156:159], a[56:59], v[108:111]\n"
    "v_mfma_f32_16x16x32_bf16 v[140:143], a[152:155], a[36:39], v[140:143]\n"
    "ds_read_b64_tr_b16 a[180:181], v246 offset:17920\n"
    "v_mfma_f32_16x16x32_bf16 v[140:143], a[156:159], a[52:55], v[140:143]\n"
    "v_mfma_f32_16x16x32_bf16 v[204:207], a[152:155], a[32:35], v[204:207]\n"
    "ds_read_b64_tr_b16 a[182:183], v246 offset:18176\n"
    "v_mfma_f32_16x16x32_bf16 v[204:207], a[156:159], a[48:51], v[204:207]\n"
    "s_waitcnt lgkmcnt(10)\n"
    "buffer_load_dwordx4 a[124:127], v255, s[48:51], s69 offen offset:3072 sc1\n"
    "v_mfma_f32_16x16x32_bf16 v[208:211], a[160:163], a[32:35], v[208:211]\n"
    "s_waitcnt lgkmcnt(8)\n"
    "ds_read_b64_tr_b16 a[184:185], v247 offset:9728\n"
    "v_mfma_f32_16x16x32_bf16 v[208:211], a[164:167], a[48:51], v[208:211]\n"
    "v_mfma_f32_16x16x32_bf16 v[144:147], a[160:163], a[36:39], v[144:147]\n"
    "s_add_u32 s69, s69, 0x1000\n"
    "ds_read_b64_tr_b16 a[186:187], v247 offset:9984\n"
    "v_mfma_f32_16x16x32_bf16 v[144:147], a[164:167], a[52:55], v[144:147]\n"
    "buffer_load_dwordx4 a[0:3], v255, s[48:51], s69 offen sc1\n"
    "v_mfma_f32_16x16x32_bf16 v[112:115], a[160:163], a[40:43], v[112:115]\n"
    "ds_read_b64_tr_b16 a[188:189], v245 offset:17920\n"
    "v_mfma_f32_16x16x32_bf16 v[112:115], a[164:167], a[56:59], v[112:115]\n"
    "v_mfma_f32_16x16x32_bf16 v[176:179], a[160:163], a[44:47], v[176:179]\n"
    "ds_read_b64_tr_b16 a[190:191], v245 offset:18176\n"
    "v_mfma_f32_16x16x32_bf16 v[176:179], a[164:167], a[60:63], v[176:179]\n"
    "s_waitcnt lgkmcnt(10)\n"
    "buffer_load_dwordx4 a[4:7], v255, s[48:51], s69 offen offset:1024 sc1\n"
    "v_mfma_f32_16x16x32_bf16 v[180:183], a[168:171], a[44:47], v[180:183]\n"
    "s_waitcnt lgkmcnt(8)\n"
    "s_barrier\n"
    "ds_read_b64_tr_b16 a[128:129], v248 offset:24576\n"
    "v_mfma_f32_16x16x32_bf16 v[180:183], a[172:175], a[60:63], v[180:183]\n"
    "ds_read_b64_tr_b16 a[130:131], v248 offset:24832\n"
    "v_mfma_f32_16x16x32_bf16 v[116:119], a[168:171], a[40:43], v[116:119]\n"
    "v_add_u32_e32 v246, 0x10000, v244\n"
    "v_mfma_f32_16x16x32_bf16 v[116:119], a[172:175], a[56:59], v[116:119]\n"
    "v_mfma_f32_16x16x32_bf16 v[148:151], a[168:171], a[36:39], v[148:151]\n"
    "buffer_load_dwordx4 a[8:11], v255, s[48:51], s69 offen offset:2048 sc1\n"
    "v_add_u32_e32 v245, 0x10000, v242\n"
    "v_mfma_f32_16x16x32_bf16 v[148:151], a[172:175], a[52:55], v[148:151]\n"
    "ds_read_b64_tr_b16 a[132:133], v246 offset:0\n"
    "v_mfma_f32_16x16x32_bf16 v[212:215], a[168:171], a[32:35], v[212:215]\n"
    "ds_read_b64_tr_b16 a[134:135], v246 offset:256\n"
    "v_mfma_f32_16x16x32_bf16 v[212:215], a[172:175], a[48:51], v[212:215]\n"
    "s_waitcnt lgkmcnt(10)\n"
    "v_mfma_f32_16x16x32_bf16 v[216:219], a[176:179], a[32:35], v[216:219]\n"
    "s_waitcnt lgkmcnt(8)\n"
    "ds_read_b64_tr_b16 a[136:137], v247 offset:24576\n"
    "buffer_load_dwordx4 a[12:15], v255, s[48:51], s69 offen offset:3072 sc1\n"
    "v_mfma_f32_16x16x32_bf16 v[216:219], a[180:183], a[48:51], v[216:219]\n"
    "v_mfma_f32_16x16x32_bf16 v[152:155], a[176:179], a[36:39], v[152:155]\n"
    "ds_read_b64_tr_b16 a[138:139], v247 offset:24832\n"
    "v_mfma_f32_16x16x32_bf16 v[152:155], a[180:183], a[52:55], v[152:155]\n"
    "v_mfma_f32_16x16x32_bf16 v[120:123], a[176:179], a[40:43], v[120:123]\n"
    "s_add_u32 s69, s69, 0x1000\n"
    "ds_read_b64_tr_b16 a[140:141], v245 offset:0\n"
    "v_mfma_f32_16x16x32_bf16 v[120:123], a[180:183], a[56:59], v[120:123]\n"
    "s_add_u32 m0, s68, 0x1a000\n"
    "v_mfma_f32_16x16x32_bf16 v[184:187], a[176:179], a[44:47], v[184:187]\n"
    "buffer_load_dwordx4 v254, s[40:43], s62 offen lds\n"
    "ds_read_b64_tr_b16 a[142:143], v245 offset:256\n"
    "v_mfma_f32_16x16x32_bf16 v[184:187], a[180:183], a[60:63], v[184:187]\n"
    "s_waitcnt lgkmcnt(10)\n"
    "v_mfma_f32_16x16x32_bf16 v[188:191], a[184:187], a[44:47], v[188:191]\n"
    "s_waitcnt lgkmcnt(8)\n"
    "s_add_u32 m0, s68, 0x1a400\n"
    "ds_read_b64_tr_b16 a[144:145], v248 offset:25088\n"
    "v_mfma_f32_16x16x32_bf16 v[188:191], a[188:191], a[60:63], v[188:191]\n"
    "buffer_load_dwordx4 v254, s[40:43], s63 offen lds\n"
    "v_mfma_f32_16x16x32_bf16 v[124:127], a[184:187], a[40:43], v[124:127]\n"
    "ds_read_b64_tr_b16 a[146:147], v248 offset:25344\n"
    "v_mfma_f32_16x16x32_bf16 v[124:127], a[188:191], a[56:59], v[124:127]\n"
    "v_mfma_f32_16x16x32_bf16 v[156:159], a[184:187], a[36:39], v[156:159]\n"
    "ds_read_b64_tr_b16 a[148:149], v246 offset:512\n"
    "v_mfma_f32_16x16x32_bf16 v[156:159], a[188:191], a[52:55], v[156:159]\n"
    "s_add_u32 s62, s62, s66\n"
    "v_mfma_f32_16x16x32_bf16 v[220:223], a[184:187], a[32:35], v[220:223]\n"
    "s_add_u32 s63, s63, s66\n"
    "ds_read_b64_tr_b16 a[150:151], v246 offset:768\n"
    "v_mfma_f32_16x16x32_bf16 v[220:223], a[188:191], a[48:51], v[220:223]\n"
    "s_waitcnt vmcnt(21) lgkmcnt(10)\n"
    "v_mfma_f32_16x16x32_bf16 v[192:195], a[128:131], a[64:67], v[192:195]\n"
    "s_waitcnt vmcnt(17) lgkmcnt(8)\n"
    "ds_read_b64_tr_b16 a[152:153], v247 offset:25088\n"
    "buffer_load_dwordx4 a[16:19], v255, s[48:51], s69 offen sc1\n"
    "v_mfma_f32_16x16x32_bf16 v[192:195], a[132:135], a[80:83], v[192:195]\n"
    "v_mfma_f32_16x16x32_bf16 v[128:131], a[128:131], a[68:71], v[128:131]\n"
    "s_waitcnt vmcnt(17)\n"
    "ds_read_b64_tr_b16 a[154:155], v247 offset:25344\n"
    "v_mfma_f32_16x16x32_bf16 v[128:131], a[132:135], a[84:87], v[128:131]\n"
    "v_mfma_f32_16x16x32_bf16 v[96:99], a[128:131], a[72:75], v[96:99]\n"
    "s_waitcnt vmcnt(16)\n"
    "ds_read_b64_tr_b16 a[156:157], v245 offset:512\n"
    "v_mfma_f32_16x16x32_bf16 v[96:99], a[132:135], a[88:91], v[96:99]\n"
    "buffer_load_dwordx4 a[20:23], v255, s[48:51], s69 offen offset:1024 sc1\n"
    "v_mfma_f32_16x16x32_bf16 v[160:163], a[128:131], a[76:79], v[160:163]\n"
    "s_waitcnt vmcnt(16)\n"
    "ds_read_b64_tr_b16 a[158:159], v245 offset:768\n"
    "v_mfma_f32_16x16x32_bf16 v[160:163], a[132:135], a[92:95], v[160:163]\n"
    "s_waitcnt lgkmcnt(10)\n"
    "v_mfma_f32_16x16x32_bf16 v[164:167], a[136:139], a[76:79], v[164:167]\n"
    "s_waitcnt lgkmcnt(8)\n"
    "ds_read_b64_tr_b16 a[160:161], v248 offset:25600\n"
    "v_mfma_f32_16x16x32_bf16 v[164:167], a[140:143], a[92:95], v[164:167]\n"
    "v_mfma_f32_16x16x32_bf16 v[100:103], a[136:139], a[72:75], v[100:103]\n"
    "ds_read_b64_tr_b16 a[162:163], v248 offset:25856\n"
    "buffer_load_dwordx4 a[24:27], v255, s[48:51], s69 offen offset:2048 sc1\n"
    "v_mfma_f32_16x16x32_bf16 v[100:103], a[140:143], a[88:91], v[100:103]\n"
    "v_mfma_f32_16x16x32_bf16 v[132:135], a[136:139], a[68:71], v[132:135]\n"
    "ds_read_b64_tr_b16 a[164:165], v246 offset:1024\n"
    "v_mfma_f32_16x16x32_bf16 v[132:135], a[140:143], a[84:87], v[132:135]\n"
    "v_mfma_f32_16x16x32_bf16 v[196:199], a[136:139], a[64:67], v[196:199]\n"
    "ds_read_b64_tr_b16 a[166:167], v246 offset:1280\n"
    "v_mfma_f32_16x16x32_bf16 v[196:199], a[140:143], a[80:83], v[196:199]\n"
    "s_waitcnt lgkmcnt(10)\n"
    "buffer_load_dwordx4 a[28:31], v255, s[48:51], s69 offen offset:3072 sc1\n"
    "v_mfma_f32_16x16x32_bf16 v[200:203], a[144:147], a[64:67], v[200:203]\n"
    "s_waitcnt lgkmcnt(8)\n"
    "ds_read_b64_tr_b16 a[168:169], v247 offset:25600\n"
    "v_mfma_f32_16x16x32_bf16 v[200:203], a[148:151], a[80:83], v[200:203]\n"
    "v_mfma_f32_16x16x32_bf16 v[136:139], a[144:147], a[68:71], v[136:139]\n"
    "ds_read_b64_tr_b16 a[170:171], v247 offset:25856\n"
    "v_mfma_f32_16x16x32_bf16 v[136:139], a[148:151], a[84:87], v[136:139]\n"
    "s_add_u32 s69, s69, 0x1000\n"
    "v_mfma_f32_16x16x32_bf16 v[104:107], a[144:147], a[72:75], v[104:107]\n"
    "ds_read_b64_tr_b16 a[172:173], v245 offset:1024\n"
    "buffer_load_dwordx4 a[32:35], v255, s[48:51], s69 offen sc1\n"
    "v_mfma_f32_16x16x32_bf16 v[104:107], a[148:151], a[88:91], v[104:107]\n"
    "v_mfma_f32_16x16x32_bf16 v[168:171], a[144:147], a[76:79], v[168:171]\n"
    "ds_read_b64_tr_b16 a[174:175], v245 offset:1280\n"
    "v_mfma_f32_16x16x32_bf16 v[168:171], a[148:151], a[92:95], v[168:171]\n"
    "s_waitcnt lgkmcnt(10)\n"
    "v_mfma_f32_16x16x32_bf16 v[172:175], a[152:155], a[76:79], v[172:175]\n"
    "s_waitcnt lgkmcnt(8)\n"
    "ds_read_b64_tr_b16 a[176:177], v248 offset:26112\n"
    "v_mfma_f32_16x16x32_bf16 v[172:175], a[156:159], a[92:95], v[172:175]\n"
    "buffer_load_dwordx4 a[36:39], v255, s[48:51], s69 offen offset:1024 sc1\n"
    "v_mfma_f32_16x16x32_bf16 v[108:111], a[152:155], a[72:75], v[108:111]\n"
    "ds_read_b64_tr_b16 a[178:179], v248 offset:26368\n"
    "v_mfma_f32_16x16x32_bf16 v[108:111], a[156:159], a[88:91], v[108:111]\n"
    "v_mfma_f32_16x16x32_bf16 v[140:143], a[152:155], a[68:71], v[140:143]\n"
    "ds_read_b64_tr_b16 a[180:181], v246 offset:1536\n"
    "v_mfma_f32_16x16x32_bf16 v[140:143], a[156:159], a[84:87], v[140:143]\n"
    "v_mfma_f32_16x16x32_bf16 v[204:207], a[152:155], a[64:67], v[204:207]\n"
    "ds_read_b64_tr_b16 a[182:183], v246 offset:1792\n"
    "v_mfma_f32_16x16x32_bf16 v[204:207], a[156:159], a[80:83], v[204:207]\n"
    "s_waitcnt lgkmcnt(10)\n"
    "buffer_load_dwordx4 a[40:43], v255, s[48:51], s69 offen offset:2048 sc1\n"
    "v_mfma_f32_16x16x32_bf16 v[208:211], a[160:163], a[64:67], v[208:211]\n"
    "s_waitcnt lgkmcnt(8)\n"
    "ds_read_b64_tr_b16 a[184:185], v247 offset:26112\n"
    "v_mfma_f32_16x16x32_bf16 v[208:211], a[164:167], a[80:83], v[208:211]\n"
    "v_mfma_f32_16x16x32_bf16 v[144:147], a[160:163], a[68:71], v[144:147]\n"
    "ds_read_b64_tr_b16 a[186:187], v247 offset:26368\n"
    "v_mfma_f32_16x16x32_bf16 v[144:147], a[164:167], a[84:87], v[144:147]\n"
    "v_mfma_f32_16x16x32_bf16 v[112:115], a[160:163], a[72:75], v[112:115]\n"
    "ds_read_b64_tr_b16 a[188:189], v245 offset:1536\n"
    "buffer_load_dwordx4 a[44:47], v255, s[48:51], s69 offen offset:3072 sc1\n"
    "v_mfma_f32_16x16x32_bf16 v[112:115], a[164:167], a[88:91], v[112:115]\n"
    "v_mfma_f32_16x16x32_bf16 v[176:179], a[160:163], a[76:79], v[176:179]\n"
    "ds_read_b64_tr_b16 a[190:191], v245 offset:1792\n"
    "v_mfma_f32_16x16x32_bf16 v[176:179], a[164:167], a[92:95], v[176:179]\n"
    "s_waitcnt lgkmcnt(10)\n"
    "v_mfma_f32_16x16x32_bf16 v[180:183], a[168:171], a[76:79], v[180:183]\n"
    "s_waitcnt lgkmcnt(8)\n"
    "s_barrier\n"
    "v_add_u32_e32 v248, 0x10000, v244\n"
    "v_mfma_f32_16x16x32_bf16 v[180:183], a[172:175], a[92:95], v[180:183]\n"
    "s_add_u32 s69, s69, 0x1000\n"
    "v_add_u32_e32 v247, 0x10000, v242\n"
    "v_mfma_f32_16x16x32_bf16 v[116:119], a[168:171], a[72:75], v[116:119]\n"
    "s_add_u32 m0, s68, 0x1c000\n"
    "ds_read_b64_tr_b16 a[128:129], v248 offset:8192\n"
    "v_mfma_f32_16x16x32_bf16 v[116:119], a[172:175], a[88:91], v[116:119]\n"
    "buffer_load_dwordx4 v254, s[40:43], s62 offen lds\n"
    "v_mfma_f32_16x16x32_bf16 v[148:151], a[168:171], a[68:71], v[148:151]\n"
    "ds_read_b64_tr_b16 a[130:131], v248 offset:8448\n"
    "v_mfma_f32_16x16x32_bf16 v[148:151], a[172:175], a[84:87], v[148:151]\n"
    "s_add_u32 m0, s68, 0x1c400\n"
    "ds_read_b64_tr_b16 a[132:133], v246 offset:16384\n"
    "v_mfma_f32_16x16x32_bf16 v[212:215], a[168:171], a[64:67], v[212:215]\n"
    "buffer_load_dwordx4 v254, s[40:43], s63 offen lds\n"
    "ds_read_b64_tr_b16 a[134:135], v246 offset:16640\n"
    "v_mfma_f32_16x16x32_bf16 v[212:215], a[172:175], a[80:83], v[212:215]\n"
    "s_waitcnt lgkmcnt(10)\n"
    "v_mfma_f32_16x16x32_bf16 v[216:219], a[176:179], a[64:67], v[216:219]\n"
    "s_waitcnt lgkmcnt(8)\n"
    "ds_read_b64_tr_b16 a[136:137], v247 offset:8192\n"
    "v_mfma_f32_16x16x32_bf16 v[216:219], a[180:183], a[80:83], v[216:219]\n"
    "v_mfma_f32_16x16x32_bf16 v[152:155], a[176:179], a[68:71], v[152:155]\n"
    "s_add_u32 s62, s62, s66\n"
    "ds_read_b64_tr_b16 a[138:139], v247 offset:8448\n"
    "v_mfma_f32_16x16x32_bf16 v[152:155], a[180:183], a[84:87], v[152:155]\n"
    "s_add_u32 s63, s63, s66\n"
    "v_mfma_f32_16x16x32_bf16 v[120:123], a[176:179], a[72:75], v[120:123]\n"
    "s_add_u32 m0, s68, 0x1e000\n"
    "ds_read_b64_tr_b16 a[140:141], v245 offset:16384\n"
    "v_mfma_f32_16x16x32_bf16 v[120:123], a[180:183], a[88:91], v[120:123]\n"
    "buffer_load_dwordx4 v254, s[40:43], s62 offen lds\n"
    "v_mfma_f32_16x16x32_bf16 v[184:187], a[176:179], a[76:79], v[184:187]\n"
    "ds_read_b64_tr_b16 a[142:143], v245 offset:16640\n"
    "v_mfma_f32_16x16x32_bf16 v[184:187], a[180:183], a[92:95], v[184:187]\n"
    "s_waitcnt lgkmcnt(10)\n"
    "s_add_u32 m0, s68, 0x1e400\n"
    "v_mfma_f32_16x16x32_bf16 v[188:191], a[184:187], a[76:79], v[188:191]\n"
    "s_waitcnt lgkmcnt(8)\n"
    "ds_read_b64_tr_b16 a[144:145], v248 offset:8704\n"
    "buffer_load_dwordx4 v254, s[40:43], s63 offen lds\n"
    "v_mfma_f32_16x16x32_bf16 v[188:191], a[188:191], a[92:95], v[188:191]\n"
    "v_mfma_f32_16x16x32_bf16 v[124:127], a[184:187], a[72:75], v[124:127]\n"
    "ds_read_b64_tr_b16 a[146:147], v248 offset:8960\n"
    "v_mfma_f32_16x16x32_bf16 v[124:127], a[188:191], a[88:91], v[124:127]\n"
    "v_mfma_f32_16x16x32_bf16 v[156:159], a[184:187], a[68:71], v[156:159]\n"
    "ds_read_b64_tr_b16 a[148:149], v246 offset:16896\n"
    "v_mfma_f32_16x16x32_bf16 v[156:159], a[188:191], a[84:87], v[156:159]\n"
    "s_add_u32 s62, s62, s66\n"
    "v_mfma_f32_16x16x32_bf16 v[220:223], a[184:187], a[64:67], v[220:223]\n"
    "s_add_u32 s63, s63, s66\n"
    "ds_read_b64_tr_b16 a[150:151], v246 offset:17152\n"
    "v_mfma_f32_16x16x32_bf16 v[220:223], a[188:191], a[80:83], v[220:223]\n"
    "s_waitcnt vmcnt(25) lgkmcnt(10)\n"
    "v_mfma_f32_16x16x32_bf16 v[192:195], a[128:131], a[96:99], v[192:195]\n"
    "s_waitcnt vmcnt(21) lgkmcnt(8)\n"
    "ds_read_b64_tr_b16 a[152:153], v247 offset:8704\n"
    "buffer_load_dwordx4 a[48:51], v255, s[48:51], s69 offen sc1\n"
    "v_mfma_f32_16x16x32_bf16 v[192:195], a[132:135], a[112:115], v[192:195]\n"
    "v_mfma_f32_16x16x32_bf16 v[128:131], a[128:131], a[100:103], v[128:131]\n"
    "s_waitcnt vmcnt(21)\n"
    "ds_read_b64_tr_b16 a[154:155], v247 offset:8960\n"
    "v_mfma_f32_16x16x32_bf16 v[128:131], a[132:135], a[116:119], v[128:131]\n"
    "v_mfma_f32_16x16x32_bf16 v[96:99], a[128:131], a[104:107], v[96:99]\n"
    "s_waitcnt vmcnt(20)\n"
    "ds_read_b64_tr_b16 a[156:157], v245 offset:16896\n"
    "v_mfma_f32_16x16x32_bf16 v[96:99], a[132:135], a[120:123], v[96:99]\n"
    "buffer_load_dwordx4 a[52:55], v255, s[48:51], s69 offen offset:1024 sc1\n"
    "v_mfma_f32_16x16x32_bf16 v[160:163], a[128:131], a[108:111], v[160:163]\n"
    "s_waitcnt vmcnt(20)\n"
    "ds_read_b64_tr_b16 a[158:159], v245 offset:17152\n"
    "v_mfma_f32_16x16x32_bf16 v[160:163], a[132:135], a[124:127], v[160:163]\n"
    "s_waitcnt lgkmcnt(10)\n"
    "v_mfma_f32_16x16x32_bf16 v[164:167], a[136:139], a[108:111], v[164:167]\n"
    "s_waitcnt lgkmcnt(8)\n"
    "ds_read_b64_tr_b16 a[160:161], v248 offset:9216\n"
    "v_mfma_f32_16x16x32_bf16 v[164:167], a[140:143], a[124:127], v[164:167]\n"
    "v_mfma_f32_16x16x32_bf16 v[100:103], a[136:139], a[104:107], v[100:103]\n"
    "ds_read_b64_tr_b16 a[162:163], v248 offset:9472\n"
    "buffer_load_dwordx4 a[56:59], v255, s[48:51], s69 offen offset:2048 sc1\n"
    "v_mfma_f32_16x16x32_bf16 v[100:103], a[140:143], a[120:123], v[100:103]\n"
    "v_mfma_f32_16x16x32_bf16 v[132:135], a[136:139], a[100:103], v[132:135]\n"
    "ds_read_b64_tr_b16 a[164:165], v246 offset:17408\n"
    "v_mfma_f32_16x16x32_bf16 v[132:135], a[140:143], a[116:119], v[132:135]\n"
    "v_mfma_f32_16x16x32_bf16 v[196:199], a[136:139], a[96:99], v[196:199]\n"
    "ds_read_b64_tr_b16 a[166:167], v246 offset:17664\n"
    "v_mfma_f32_16x16x32_bf16 v[196:199], a[140:143], a[112:115], v[196:199]\n"
    "s_waitcnt lgkmcnt(10)\n"
    "buffer_load_dwordx4 a[60:63], v255, s[48:51], s69 offen offset:3072 sc1\n"
    "v_mfma_f32_16x16x32_bf16 v[200:203], a[144:147], a[96:99], v[200:203]\n"
    "s_waitcnt lgkmcnt(8)\n"
    "ds_read_b64_tr_b16 a[168:169], v247 offset:9216\n"
    "v_mfma_f32_16x16x32_bf16 v[200:203], a[148:151], a[112:115], v[200:203]\n"
    "v_mfma_f32_16x16x32_bf16 v[136:139], a[144:147], a[100:103], v[136:139]\n"
    "ds_read_b64_tr_b16 a[170:171], v247 offset:9472\n"
    "v_mfma_f32_16x16x32_bf16 v[136:139], a[148:151], a[116:119], v[136:139]\n"
    "s_add_u32 s69, s69, 0x1000\n"
    "v_mfma_f32_16x16x32_bf16 v[104:107], a[144:147], a[104:107], v[104:107]\n"
    "ds_read_b64_tr_b16 a[172:173], v245 offset:17408\n"
    "buffer_load_dwordx4 a[64:67], v255, s[48:51], s69 offen sc1\n"
    "v_mfma_f32_16x16x32_bf16 v[104:107], a[148:151], a[120:123], v[104:107]\n"
    "v_mfma_f32_16x16x32_bf16 v[168:171], a[144:147], a[108:111], v[168:171]\n"
    "ds_read_b64_tr_b16 a[174:175], v245 offset:17664\n"
    "v_mfma_f32_16x16x32_bf16 v[168:171], a[148:151], a[124:127], v[168:171]\n"
    "s_waitcnt lgkmcnt(10)\n"
    "v_mfma_f32_16x16x32_bf16 v[172:175], a[152:155], a[108:111], v[172:175]\n"
    "s_waitcnt lgkmcnt(8)\n"
    "ds_read_b64_tr_b16 a[176:177], v248 offset:9728\n"
    "v_mfma_f32_16x16x32_bf16 v[172:175], a[156:159], a[124:127], v[172:175]\n"
    "buffer_load_dwordx4 a[68:71], v255, s[48:51], s69 offen offset:1024 sc1\n"
    "v_mfma_f32_16x16x32_bf16 v[108:111], a[152:155], a[104:107], v[108:111]\n"
    "ds_read_b64_tr_b16 a[178:179], v248 offset:9984\n"
    "v_mfma_f32_16x16x32_bf16 v[108:111], a[156:159], a[120:123], v[108:111]\n"
    "v_mfma_f32_16x16x32_bf16 v[140:143], a[152:155], a[100:103], v[140:143]\n"
    "ds_read_b64_tr_b16 a[180:181], v246 offset:17920\n"
    "v_mfma_f32_16x16x32_bf16 v[140:143], a[156:159], a[116:119], v[140:143]\n"
    "v_mfma_f32_16x16x32_bf16 v[204:207], a[152:155], a[96:99], v[204:207]\n"
    "ds_read_b64_tr_b16 a[182:183], v246 offset:18176\n"
    "v_mfma_f32_16x16x32_bf16 v[204:207], a[156:159], a[112:115], v[204:207]\n"
    "s_waitcnt lgkmcnt(10)\n"
    "buffer_load_dwordx4 a[72:75], v255, s[48:51], s69 offen offset:2048 sc1\n"
    "v_mfma_f32_16x16x32_bf16 v[208:211], a[160:163], a[96:99], v[208:211]\n"
    "s_waitcnt lgkmcnt(8)\n"
    "ds_read_b64_tr_b16 a[184:185], v247 offset:9728\n"
    "v_mfma_f32_16x16x32_bf16 v[208:211], a[164:167], a[112:115], v[208:211]\n"
    "v_mfma_f32_16x16x32_bf16 v[144:147], a[160:163], a[100:103], v[144:147]\n"
    "ds_read_b64_tr_b16 a[186:187], v247 offset:9984\n"
    "v_mfma_f32_16x16x32_bf16 v[144:147], a[164:167], a[116:119], v[144:147]\n"
    "v_mfma_f32_16x16x32_bf16 v[112:115], a[160:163], a[104:107], v[112:115]\n"
    "ds_read_b64_tr_b16 a[188:189], v245 offset:17920\n"
    "buffer_load_dwordx4 a[76:79], v255, s[48:51], s69 offen offset:3072 sc1\n"
    "v_mfma_f32_16x16x32_bf16 v[112:115], a[164:167], a[120:123], v[112:115]\n"
    "v_mfma_f32_16x16x32_bf16 v[176:179], a[160:163], a[108:111], v[176:179]\n"
    "ds_read_b64_tr_b16 a[190:191], v245 offset:18176\n"
    "v_mfma_f32_16x16x32_bf16 v[176:179], a[164:167], a[124:127], v[176:179]\n"
    "s_waitcnt lgkmcnt(10)\n"
    "v_mfma_f32_16x16x32_bf16 v[180:183], a[168:171], a[108:111], v[180:183]\n"
    "s_waitcnt lgkmcnt(8)\n"
    "s_barrier\n"
    "ds_read_b64_tr_b16 a[128:129], v248 offset:24576\n"
    "v_mfma_f32_16x16x32_bf16 v[180:183], a[172:175], a[124:127], v[180:183]\n"
    "s_add_u32 s69, s69, 0x1000\n"
    "ds_read_b64_tr_b16 a[130:131], v248 offset:24832\n"
    "v_mfma_f32_16x16x32_bf16 v[116:119], a[168:171], a[104:107], v[116:119]\n"
    "s_add_u32 m0, s68, 0\n"
    "v_add_u32_e32 v246, 0x18000, v244\n"
    "v_mfma_f32_16x16x32_bf16 v[116:119], a[172:175], a[120:123], v[116:119]\n"
    "buffer_load_dwordx4 v254, s[40:43], s62 offen lds\n"
    "v_mfma_f32_16x16x32_bf16 v[148:151], a[168:171], a[100:103], v[148:151]\n"
    "v_add_u32_e32 v245, 0x18000, v242\n"
    "v_mfma_f32_16x16x32_bf16 v[148:151], a[172:175], a[116:119], v[148:151]\n"
    "s_add_u32 m0, s68, 0x400\n"
    "ds_read_b64_tr_b16 a[132:133], v246 offset:0\n"
    "v_mfma_f32_16x16x32_bf16 v[212:215], a[168:171], a[96:99], v[212:215]\n"
    "buffer_load_dwordx4 v254, s[40:43], s63 offen lds\n"
    "ds_read_b64_tr_b16 a[134:135], v246 offset:256\n"
    "v_mfma_f32_16x16x32_bf16 v[212:215], a[172:175], a[112:115], v[212:215]\n"
    "s_waitcnt lgkmcnt(10)\n"
    "v_mfma_f32_16x16x32_bf16 v[216:219], a[176:179], a[96:99], v[216:219]\n"
    "s_waitcnt lgkmcnt(8)\n"
    "ds_read_b64_tr_b16 a[136:137], v247 offset:24576\n"
    "v_mfma_f32_16x16x32_bf16 v[216:219], a[180:183], a[112:115], v[216:219]\n"
    "v_mfma_f32_16x16x32_bf16 v[152:155], a[176:179], a[100:103], v[152:155]\n"
    "s_add_u32 s62, s62, s66\n"
    "ds_read_b64_tr_b16 a[138:139], v247 offset:24832\n"
    "v_mfma_f32_16x16x32_bf16 v[152:155], a[180:183], a[116:119], v[152:155]\n"
    "s_add_u32 s63, s63, s66\n"
    "v_mfma_f32_16x16x32_bf16 v[120:123], a[176:179], a[104:107], v[120:123]\n"
    "s_add_u32 m0, s68, 0x2000\n"
    "ds_read_b64_tr_b16 a[140:141], v245 offset:0\n"
    "v_mfma_f32_16x16x32_bf16 v[120:123], a[180:183], a[120:123], v[120:123]\n"
    "buffer_load_dwordx4 v254, s[40:43], s62 offen lds\n"
    "v_mfma_f32_16x16x32_bf16 v[184:187], a[176:179], a[108:111], v[184:187]\n"
    "ds_read_b64_tr_b16 a[142:143], v245 offset:256\n"
    "v_mfma_f32_16x16x32_bf16 v[184:187], a[180:183], a[124:127], v[184:187]\n"
    "s_waitcnt lgkmcnt(10)\n"
    "s_add_u32 m0, s68, 0x2400\n"
    "v_mfma_f32_16x16x32_bf16 v[188:191], a[184:187], a[108:111], v[188:191]\n"
    "s_waitcnt lgkmcnt(8)\n"
    "ds_read_b64_tr_b16 a[144:145], v248 offset:25088\n"
    "buffer_load_dwordx4 v254, s[40:43], s63 offen lds\n"
    "v_mfma_f32_16x16x32_bf16 v[188:191], a[188:191], a[124:127], v[188:191]\n"
    "v_mfma_f32_16x16x32_bf16 v[124:127], a[184:187], a[104:107], v[124:127]\n"
    "ds_read_b64_tr_b16 a[146:147], v248 offset:25344\n"
    "v_mfma_f32_16x16x32_bf16 v[124:127], a[188:191], a[120:123], v[124:127]\n"
    "v_mfma_f32_16x16x32_bf16 v[156:159], a[184:187], a[100:103], v[156:159]\n"
    "ds_read_b64_tr_b16 a[148:149], v246 offset:512\n"
    "v_mfma_f32_16x16x32_bf16 v[156:159], a[188:191], a[116:119], v[156:159]\n"
    "s_add_u32 s62, s62, s66\n"
    "v_mfma_f32_16x16x32_bf16 v[220:223], a[184:187], a[96:99], v[220:223]\n"
    "s_add_u32 s63, s63, s66\n"
    "ds_read_b64_tr_b16 a[150:151], v246 offset:768\n"
    "v_mfma_f32_16x16x32_bf16 v[220:223], a[188:191], a[112:115], v[220:223]\n"
    "s_waitcnt vmcnt(29) lgkmcnt(10)\n"
    "v_mfma_f32_16x16x32_bf16 v[192:195], a[128:131], a[0:3], v[192:195]\n"
    "s_waitcnt vmcnt(23) lgkmcnt(8)\n"
    "ds_read_b64_tr_b16 a[152:153], v247 offset:25088\n"
    "buffer_load_dwordx4 a[80:83], v255, s[48:51], s69 offen sc1\n"
    "v_mfma_f32_16x16x32_bf16 v[192:195], a[132:135], a[16:19], v[192:195]\n"
    "v_mfma_f32_16x16x32_bf16 v[128:131], a[128:131], a[4:7], v[128:131]\n"
    "s_waitcnt vmcnt(23)\n"
    "ds_read_b64_tr_b16 a[154:155], v247 offset:25344\n"
    "v_mfma_f32_16x16x32_bf16 v[128:131], a[132:135], a[20:23], v[128:131]\n"
    "v_mfma_f32_16x16x32_bf16 v[96:99], a[128:131], a[8:11], v[96:99]\n"
    "s_waitcnt vmcnt(22)\n"
    "ds_read_b64_tr_b16 a[156:157], v245 offset:512\n"
    "v_mfma_f32_16x16x32_bf16 v[96:99], a[132:135], a[24:27], v[96:99]\n"
    "buffer_load_dwordx4 a[84:87], v255, s[48:51], s69 offen offset:1024 sc1\n"
    "v_mfma_f32_16x16x32_bf16 v[160:163], a[128:131], a[12:15], v[160:163]\n"
    "s_waitcnt vmcnt(22)\n"
    "ds_read_b64_tr_b16 a[158:159], v245 offset:768\n"
    "v_mfma_f32_16x16x32_bf16 v[160:163], a[132:135], a[28:31], v[160:163]\n"
    "s_waitcnt lgkmcnt(10)\n"
    "v_mfma_f32_16x16x32_bf16 v[164:167], a[136:139], a[12:15], v[164:167]\n"
    "s_waitcnt lgkmcnt(8)\n"
    "ds_read_b64_tr_b16 a[160:161], v248 offset:25600\n"
    "v_mfma_f32_16x16x32_bf16 v[164:167], a[140:143], a[28:31], v[164:167]\n"
    "v_mfma_f32_16x16x32_bf16 v[100:103], a[136:139], a[8:11], v[100:103]\n"
    "ds_read_b64_tr_b16 a[162:163], v248 offset:25856\n"
    "buffer_load_dwordx4 a[88:91], v255, s[48:51], s69 offen offset:2048 sc1\n"
    "v_mfma_f32_16x16x32_bf16 v[100:103], a[140:143], a[24:27], v[100:103]\n"
    "v_mfma_f32_16x16x32_bf16 v[132:135], a[136:139], a[4:7], v[132:135]\n"
    "ds_read_b64_tr_b16 a[164:165], v246 offset:1024\n"
    "v_mfma_f32_16x16x32_bf16 v[132:135], a[140:143], a[20:23], v[132:135]\n"
    "v_mfma_f32_16x16x32_bf16 v[196:199], a[136:139], a[0:3], v[196:199]\n"
    "ds_read_b64_tr_b16 a[166:167], v246 offset:1280\n"
    "v_mfma_f32_16x16x32_bf16 v[196:199], a[140:143], a[16:19], v[196:199]\n"
    "s_waitcnt lgkmcnt(10)\n"
    "buffer_load_dwordx4 a[92:95], v255, s[48:51], s69 offen offset:3072 sc1\n"
    "v_mfma_f32_16x16x32_bf16 v[200:203], a[144:147], a[0:3], v[200:203]\n"
    "s_waitcnt lgkmcnt(8)\n"
    "ds_read_b64_tr_b16 a[168:169], v247 offset:25600\n"
    "v_mfma_f32_16x16x32_bf16 v[200:203], a[148:151], a[16:19], v[200:203]\n"
    "v_mfma_f32_16x16x32_bf16 v[136:139], a[144:147], a[4:7], v[136:139]\n"
    "ds_read_b64_tr_b16 a[170:171], v247 offset:25856\n"
    "v_mfma_f32_16x16x32_bf16 v[136:139], a[148:151], a[20:23], v[136:139]\n"
    "s_add_u32 s69, s69, 0x1000\n"
    "v_mfma_f32_16x16x32_bf16 v[104:107], a[144:147], a[8:11], v[104:107]\n"
    "ds_read_b64_tr_b16 a[172:173], v245 offset:1024\n"
    "buffer_load_dwordx4 a[96:99], v255, s[48:51], s69 offen sc1\n"
    "v_mfma_f32_16x16x32_bf16 v[104:107], a[148:151], a[24:27], v[104:107]\n"
    "v_mfma_f32_16x16x32_bf16 v[168:171], a[144:147], a[12:15], v[168:171]\n"
    "ds_read_b64_tr_b16 a[174:175], v245 offset:1280\n"
    "v_mfma_f32_16x16x32_bf16 v[168:171], a[148:151], a[28:31], v[168:171]\n"
    "s_waitcnt lgkmcnt(10)\n"
    "v_mfma_f32_16x16x32_bf16 v[172:175], a[152:155], a[12:15], v[172:175]\n"
    "s_waitcnt lgkmcnt(8)\n"
    "ds_read_b64_tr_b16 a[176:177], v248 offset:26112\n"
    "v_mfma_f32_16x16x32_bf16 v[172:175], a[156:159], a[28:31], v[172:175]\n"
    "buffer_load_dwordx4 a[100:103], v255, s[48:51], s69 offen offset:1024 sc1\n"
    "v_mfma_f32_16x16x32_bf16 v[108:111], a[152:155], a[8:11], v[108:111]\n"
    "ds_read_b64_tr_b16 a[178:179], v248 offset:26368\n"
    "v_mfma_f32_16x16x32_bf16 v[108:111], a[156:159], a[24:27], v[108:111]\n"
    "v_mfma_f32_16x16x32_bf16 v[140:143], a[152:155], a[4:7], v[140:143]\n"
    "ds_read_b64_tr_b16 a[180:181], v246 offset:1536\n"
    "v_mfma_f32_16x16x32_bf16 v[140:143], a[156:159], a[20:23], v[140:143]\n"
    "v_mfma_f32_16x16x32_bf16 v[204:207], a[152:155], a[0:3], v[204:207]\n"
    "ds_read_b64_tr_b16 a[182:183], v246 offset:1792\n"
    "v_mfma_f32_16x16x32_bf16 v[204:207], a[156:159], a[16:19], v[204:207]\n"
    "s_waitcnt lgkmcnt(10)\n"
    "buffer_load_dwordx4 a[104:107], v255, s[48:51], s69 offen offset:2048 sc1\n"
    "v_mfma_f32_16x16x32_bf16 v[208:211], a[160:163], a[0:3], v[208:211]\n"
    "s_waitcnt lgkmcnt(8)\n"
    "ds_read_b64_tr_b16 a[184:185], v247 offset:26112\n"
    "v_mfma_f32_16x16x32_bf16 v[208:211], a[164:167], a[16:19], v[208:211]\n"
    "v_mfma_f32_16x16x32_bf16 v[144:147], a[160:163], a[4:7], v[144:147]\n"
    "ds_read_b64_tr_b16 a[186:187], v247 offset:26368\n"
    "v_mfma_f32_16x16x32_bf16 v[144:147], a[164:167], a[20:23], v[144:147]\n"
    "v_mfma_f32_16x16x32_bf16 v[112:115], a[160:163], a[8:11], v[112:115]\n"
    "ds_read_b64_tr_b16 a[188:189], v245 offset:1536\n"
    "buffer_load_dwordx4 a[108:111], v255, s[48:51], s69 offen offset:3072 sc1\n"
    "v_mfma_f32_16x16x32_bf16 v[112:115], a[164:167], a[24:27], v[112:115]\n"
    "v_mfma_f32_16x16x32_bf16 v[176:179], a[160:163], a[12:15], v[176:179]\n"
    "ds_read_b64_tr_b16 a[190:191], v245 offset:1792\n"
    "v_mfma_f32_16x16x32_bf16 v[176:179], a[164:167], a[28:31], v[176:179]\n"
    "s_waitcnt lgkmcnt(10)\n"
    "v_mfma_f32_16x16x32_bf16 v[180:183], a[168:171], a[12:15], v[180:183]\n"
    "s_waitcnt vmcnt(22) lgkmcnt(8)\n"
    "s_barrier\n"
    "v_add_u32_e32 v248, 0x18000, v244\n"
    "v_mfma_f32_16x16x32_bf16 v[180:183], a[172:175], a[28:31], v[180:183]\n"
    "s_add_u32 s69, s69, 0x1000\n"
    "v_add_u32_e32 v247, 0x18000, v242\n"
    "v_mfma_f32_16x16x32_bf16 v[116:119], a[168:171], a[8:11], v[116:119]\n"
    "s_add_u32 m0, s68, 0x4000\n"
    "ds_read_b64_tr_b16 a[128:129], v248 offset:8192\n"
    "v_mfma_f32_16x16x32_bf16 v[116:119], a[172:175], a[24:27], v[116:119]\n"
    "buffer_load_dwordx4 v254, s[40:43], s62 offen lds\n"
    "v_mfma_f32_16x16x32_bf16 v[148:151], a[168:171], a[4:7], v[148:151]\n"
    "ds_read_b64_tr_b16 a[130:131], v248 offset:8448\n"
    "v_mfma_f32_16x16x32_bf16 v[148:151], a[172:175], a[20:23], v[148:151]\n"
    "s_add_u32 m0, s68, 0x4400\n"
    "ds_read_b64_tr_b16 a[132:133], v246 offset:16384\n"
    "v_mfma_f32_16x16x32_bf16 v[212:215], a[168:171], a[0:3], v[212:215]\n"
    "buffer_load_dwordx4 v254, s[40:43], s63 offen lds\n"
    "ds_read_b64_tr_b16 a[134:135], v246 offset:16640\n"
    "v_mfma_f32_16x16x32_bf16 v[212:215], a[172:175], a[16:19], v[212:215]\n"
    "s_waitcnt lgkmcnt(10)\n"
    "v_mfma_f32_16x16x32_bf16 v[216:219], a[176:179], a[0:3], v[216:219]\n"
    "s_waitcnt lgkmcnt(8)\n"
    "ds_read_b64_tr_b16 a[136:137], v247 offset:8192\n"
    "v_mfma_f32_16x16x32_bf16 v[216:219], a[180:183], a[16:19], v[216:219]\n"
    "v_mfma_f32_16x16x32_bf16 v[152:155], a[176:179], a[4:7], v[152:155]\n"
    "s_add_u32 s62, s62, s66\n"
    "ds_read_b64_tr_b16 a[138:139], v247 offset:8448\n"
    "v_mfma_f32_16x16x32_bf16 v[152:155], a[180:183], a[20:23], v[152:155]\n"
    "s_add_u32 s63, s63, s66\n"
    "v_mfma_f32_16x16x32_bf16 v[120:123], a[176:179], a[8:11], v[120:123]\n"
    "s_add_u32 m0, s68, 0x6000\n"
    "ds_read_b64_tr_b16 a[140:141], v245 offset:16384\n"
    "v_mfma_f32_16x16x32_bf16 v[120:123], a[180:183], a[24:27], v[120:123]\n"
    "buffer_load_dwordx4 v254, s[40:43], s62 offen lds\n"
    "v_mfma_f32_16x16x32_bf16 v[184:187], a[176:179], a[12:15], v[184:187]\n"
    "ds_read_b64_tr_b16 a[142:143], v245 offset:16640\n"
    "v_mfma_f32_16x16x32_bf16 v[184:187], a[180:183], a[28:31], v[184:187]\n"
    "s_waitcnt lgkmcnt(10)\n"
    "s_add_u32 m0, s68, 0x6400\n"
    "v_mfma_f32_16x16x32_bf16 v[188:191], a[184:187], a[12:15], v[188:191]\n"
    "s_waitcnt lgkmcnt(8)\n"
    "ds_read_b64_tr_b16 a[144:145], v248 offset:8704\n"
    "buffer_load_dwordx4 v254, s[40:43], s63 offen lds\n"
    "v_mfma_f32_16x16x32_bf16 v[188:191], a[188:191], a[28:31], v[188:191]\n"
    "v_mfma_f32_16x16x32_bf16 v[124:127], a[184:187], a[8:11], v[124:127]\n"
    "ds_read_b64_tr_b16 a[146:147], v248 offset:8960\n"
    "v_mfma_f32_16x16x32_bf16 v[124:127], a[188:191], a[24:27], v[124:127]\n"
    "v_mfma_f32_16x16x32_bf16 v[156:159], a[184:187], a[4:7], v[156:159]\n"
    "ds_read_b64_tr_b16 a[148:149], v246 offset:16896\n"
    "v_mfma_f32_16x16x32_bf16 v[156:159], a[188:191], a[20:23], v[156:159]\n"
    "s_add_u32 s62, s62, s66\n"
    "v_mfma_f32_16x16x32_bf16 v[220:223], a[184:187], a[0:3], v[220:223]\n"
    "s_add_u32 s63, s63, s66\n"
    "ds_read_b64_tr_b16 a[150:151], v246 offset:17152\n"
    "v_mfma_f32_16x16x32_bf16 v[220:223], a[188:191], a[16:19], v[220:223]\n"
    "s_waitcnt lgkmcnt(10)\n"
    "v_mfma_f32_16x16x32_bf16 v[192:195], a[128:131], a[32:35], v[192:195]\n"
    "s_waitcnt vmcnt(23) lgkmcnt(8)\n"
    "ds_read_b64_tr_b16 a[152:153], v247 offset:8704\n"
    "buffer_load_dwordx4 a[112:115], v255, s[48:51], s69 offen sc1\n"
    "v_mfma_f32_16x16x32_bf16 v[192:195], a[132:135], a[48:51], v[192:195]\n"
    "v_mfma_f32_16x16x32_bf16 v[128:131], a[128:131], a[36:39], v[128:131]\n"
    "s_waitcnt vmcnt(23)\n"
    "ds_read_b64_tr_b16 a[154:155], v247 offset:8960\n"
    "v_mfma_f32_16x16x32_bf16 v[128:131], a[132:135], a[52:55], v[128:131]\n"
    "v_mfma_f32_16x16x32_bf16 v[96:99], a[128:131], a[40:43], v[96:99]\n"
    "s_waitcnt vmcnt(22)\n"
    "ds_read_b64_tr_b16 a[156:157], v245 offset:16896\n"
    "v_mfma_f32_16x16x32_bf16 v[96:99], a[132:135], a[56:59], v[96:99]\n"
    "buffer_load_dwordx4 a[116:119], v255, s[48:51], s69 offen offset:1024 sc1\n"
    "v_mfma_f32_16x16x32_bf16 v[160:163], a[128:131], a[44:47], v[160:163]\n"
    "s_waitcnt vmcnt(22)\n"
    "ds_read_b64_tr_b16 a[158:159], v245 offset:17152\n"
    "v_mfma_f32_16x16x32_bf16 v[160:163], a[132:135], a[60:63], v[160:163]\n"
    "s_waitcnt lgkmcnt(10)\n"
    "v_mfma_f32_16x16x32_bf16 v[164:167], a[136:139], a[44:47], v[164:167]\n"
    "s_waitcnt lgkmcnt(8)\n"
    "ds_read_b64_tr_b16 a[160:161], v248 offset:9216\n"
    "v_mfma_f32_16x16x32_bf16 v[164:167], a[140:143], a[60:63], v[164:167]\n"
    "v_mfma_f32_16x16x32_bf16 v[100:103], a[136:139], a[40:43], v[100:103]\n"
    "ds_read_b64_tr_b16 a[162:163], v248 offset:9472\n"
    "buffer_load_dwordx4 a[120:123], v255, s[48:51], s69 offen offset:2048 sc1\n"
    "v_mfma_f32_16x16x32_bf16 v[100:103], a[140:143], a[56:59], v[100:103]\n"
    "v_mfma_f32_16x16x32_bf16 v[132:135], a[136:139], a[36:39], v[132:135]\n"
    "ds_read_b64_tr_b16 a[164:165], v246 offset:17408\n"
    "v_mfma_f32_16x16x32_bf16 v[132:135], a[140:143], a[52:55], v[132:135]\n"
    "v_mfma_f32_16x16x32_bf16 v[196:199], a[136:139], a[32:35], v[196:199]\n"
    "ds_read_b64_tr_b16 a[166:167], v246 offset:17664\n"
    "v_mfma_f32_16x16x32_bf16 v[196:199], a[140:143], a[48:51], v[196:199]\n"
    "s_waitcnt lgkmcnt(10)\n"
    "buffer_load_dwordx4 a[124:127], v255, s[48:51], s69 offen offset:3072 sc1\n"
    "v_mfma_f32_16x16x32_bf16 v[200:203], a[144:147], a[32:35], v[200:203]\n"
    "s_waitcnt lgkmcnt(8)\n"
    "ds_read_b64_tr_b16 a[168:169], v247 offset:9216\n"
    "v_mfma_f32_16x16x32_bf16 v[200:203], a[148:151], a[48:51], v[200:203]\n"
    "PG2_%=:\n"
    "s_cbranch_execz PG6_%=\n"
    "v_mfma_f32_16x16x32_bf16 v[136:139], a[144:147], a[36:39], v[136:139]\n"
    "ds_read_b64_tr_b16 a[170:171], v247 offset:9472\n"
    "v_mfma_f32_16x16x32_bf16 v[136:139], a[148:151], a[52:55], v[136:139]\n"
    "s_add_u32 s69, s69, 0x1000\n"
    "v_mfma_f32_16x16x32_bf16 v[104:107], a[144:147], a[40:43], v[104:107]\n"
    "ds_read_b64_tr_b16 a[172:173], v245 offset:17408\n"
    "buffer_load_dwordx4 a[0:3], v255, s[48:51], s69 offen sc1\n"
    "v_mfma_f32_16x16x32_bf16 v[104:107], a[148:151], a[56:59], v[104:107]\n"
    "v_mfma_f32_16x16x32_bf16 v[168:171], a[144:147], a[44:47], v[168:171]\n"
    "ds_read_b64_tr_b16 a[174:175], v245 offset:17664\n"
    "v_mfma_f32_16x16x32_bf16 v[168:171], a[148:151], a[60:63], v[168:171]\n"
    "s_waitcnt lgkmcnt(10)\n"
    "v_mfma_f32_16x16x32_bf16 v[172:175], a[152:155], a[44:47], v[172:175]\n"
    "s_waitcnt lgkmcnt(8)\n"
    "ds_read_b64_tr_b16 a[176:177], v248 offset:9728\n"
    "v_mfma_f32_16x16x32_bf16 v[172:175], a[156:159], a[60:63], v[172:175]\n"
    "buffer_load_dwordx4 a[4:7], v255, s[48:51], s69 offen offset:1024 sc1\n"
    "v_mfma_f32_16x16x32_bf16 v[108:111], a[152:155], a[40:43], v[108:111]\n"
    "ds_read_b64_tr_b16 a[178:179], v248 offset:9984\n"
    "v_mfma_f32_16x16x32_bf16 v[108:111], a[156:159], a[56:59], v[108:111]\n"
    "v_mfma_f32_16x16x32_bf16 v[140:143], a[152:155], a[36:39], v[140:143]\n"
    "ds_read_b64_tr_b16 a[180:181], v246 offset:17920\n"
    "v_mfma_f32_16x16x32_bf16 v[140:143], a[156:159], a[52:55], v[140:143]\n"
    "v_mfma_f32_16x16x32_bf16 v[204:207], a[152:155], a[32:35], v[204:207]\n"
    "ds_read_b64_tr_b16 a[182:183], v246 offset:18176\n"
    "v_mfma_f32_16x16x32_bf16 v[204:207], a[156:159], a[48:51], v[204:207]\n"
    "s_waitcnt lgkmcnt(10)\n"
    "buffer_load_dwordx4 a[8:11], v255, s[48:51], s69 offen offset:2048 sc1\n"
    "v_mfma_f32_16x16x32_bf16 v[208:211], a[160:163], a[32:35], v[208:211]\n"
    "s_waitcnt lgkmcnt(8)\n"
    "ds_read_b64_tr_b16 a[184:185], v247 offset:9728\n"
    "v_mfma_f32_16x16x32_bf16 v[208:211], a[164:167], a[48:51], v[208:211]\n"
    "v_mfma_f32_16x16x32_bf16 v[144:147], a[160:163], a[36:39], v[144:147]\n"
    "ds_read_b64_tr_b16 a[186:187], v247 offset:9984\n"
    "v_mfma_f32_16x16x32_bf16 v[144:147], a[164:167], a[52:55], v[144:147]\n"
    "v_mfma_f32_16x16x32_bf16 v[112:115], a[160:163], a[40:43], v[112:115]\n"
    "ds_read_b64_tr_b16 a[188:189], v245 offset:17920\n"
    "buffer_load_dwordx4 a[12:15], v255, s[48:51], s69 offen offset:3072 sc1\n"
    "v_mfma_f32_16x16x32_bf16 v[112:115], a[164:167], a[56:59], v[112:115]\n"
    "v_mfma_f32_16x16x32_bf16 v[176:179], a[160:163], a[44:47], v[176:179]\n"
    "ds_read_b64_tr_b16 a[190:191], v245 offset:18176\n"
    "v_mfma_f32_16x16x32_bf16 v[176:179], a[164:167], a[60:63], v[176:179]\n"
    "s_waitcnt lgkmcnt(10)\n"
    "v_mfma_f32_16x16x32_bf16 v[180:183], a[168:171], a[44:47], v[180:183]\n"
    "s_waitcnt vmcnt(22) lgkmcnt(8)\n"
    "s_barrier\n"
    "ds_read_b64_tr_b16 a[128:129], v248 offset:24576\n"
    "v_mfma_f32_16x16x32_bf16 v[180:183], a[172:175], a[60:63], v[180:183]\n"
    "s_add_u32 s69, s69, 0x1000\n"
    "ds_read_b64_tr_b16 a[130:131], v248 offset:24832\n"
    "v_mfma_f32_16x16x32_bf16 v[116:119], a[168:171], a[40:43], v[116:119]\n"
    "s_add_u32 m0, s68, 0x8000\n"
    "v_mov_b32_e32 v246, v244\n"
    "v_mfma_f32_16x16x32_bf16 v[116:119], a[172:175], a[56:59], v[116:119]\n"
    "buffer_load_dwordx4 v254, s[40:43], s62 offen lds\n"
    "v_mfma_f32_16x16x32_bf16 v[148:151], a[168:171], a[36:39], v[148:151]\n"
    "v_mov_b32_e64 v245, v242\n"
    "v_mfma_f32_16x16x32_bf16 v[148:151], a[172:175], a[52:55], v[148:151]\n"
    "s_add_u32 m0, s68, 0x8400\n"
    "ds_read_b64_tr_b16 a[132:133], v246 offset:0\n"
    "v_mfma_f32_16x16x32_bf16 v[212:215], a[168:171], a[32:35], v[212:215]\n"
    "buffer_load_dwordx4 v254, s[40:43], s63 offen lds\n"
    "ds_read_b64_tr_b16 a[134:135], v246 offset:256\n"
    "v_mfma_f32_16x16x32_bf16 v[212:215], a[172:175], a[48:51], v[212:215]\n"
    "s_waitcnt lgkmcnt(10)\n"
    "v_mfma_f32_16x16x32_bf16 v[216:219], a[176:179], a[32:35], v[216:219]\n"
    "s_waitcnt lgkmcnt(8)\n"
    "ds_read_b64_tr_b16 a[136:137], v247 offset:24576\n"
    "v_mfma_f32_16x16x32_bf16 v[216:219], a[180:183], a[48:51], v[216:219]\n"
    "v_mfma_f32_16x16x32_bf16 v[152:155], a[176:179], a[36:39], v[152:155]\n"
    "s_add_u32 s62, s62, s66\n"
    "ds_read_b64_tr_b16 a[138:139], v247 offset:24832\n"
    "v_mfma_f32_16x16x32_bf16 v[152:155], a[180:183], a[52:55], v[152:155]\n"
    "s_add_u32 s63, s63, s66\n"
    "v_mfma_f32_16x16x32_bf16 v[120:123], a[176:179], a[40:43], v[120:123]\n"
    "s_add_u32 m0, s68, 0xa000\n"
    "ds_read_b64_tr_b16 a[140:141], v245 offset:0\n"
    "v_mfma_f32_16x16x32_bf16 v[120:123], a[180:183], a[56:59], v[120:123]\n"
    "buffer_load_dwordx4 v254, s[40:43], s62 offen lds\n"
    "v_mfma_f32_16x16x32_bf16 v[184:187], a[176:179], a[44:47], v[184:187]\n"
    "ds_read_b64_tr_b16 a[142:143], v245 offset:256\n"
    "v_mfma_f32_16x16x32_bf16 v[184:187], a[180:183], a[60:63], v[184:187]\n"
    "s_waitcnt lgkmcnt(10)\n"
    "s_add_u32 m0, s68, 0xa400\n"
    "v_mfma_f32_16x16x32_bf16 v[188:191], a[184:187], a[44:47], v[188:191]\n"
    "s_waitcnt lgkmcnt(8)\n"
    "ds_read_b64_tr_b16 a[144:145], v248 offset:25088\n"
    "buffer_load_dwordx4 v254, s[40:43], s63 offen lds\n"
    "v_mfma_f32_16x16x32_bf16 v[188:191], a[188:191], a[60:63], v[188:191]\n"
    "v_mfma_f32_16x16x32_bf16 v[124:127], a[184:187], a[40:43], v[124:127]\n"
    "ds_read_b64_tr_b16 a[146:147], v248 offset:25344\n"
    "v_mfma_f32_16x16x32_bf16 v[124:127], a[188:191], a[56:59], v[124:127]\n"
    "v_mfma_f32_16x16x32_bf16 v[156:159], a[184:187], a[36:39], v[156:159]\n"
    "ds_read_b64_tr_b16 a[148:149], v246 offset:512\n"
    "v_mfma_f32_16x16x32_bf16 v[156:159], a[188:191], a[52:55], v[156:159]\n"
    "s_add_u32 s62, s62, s66\n"
    "v_mfma_f32_16x16x32_bf16 v[220:223], a[184:187], a[32:35], v[220:223]\n"
    "s_add_u32 s63, s63, s66\n"
    "ds_read_b64_tr_b16 a[150:151], v246 offset:768\n"
    "v_mfma_f32_16x16x32_bf16 v[220:223], a[188:191], a[48:51], v[220:223]\n"
    "s_waitcnt lgkmcnt(10)\n"
    "v_mfma_f32_16x16x32_bf16 v[192:195], a[128:131], a[64:67], v[192:195]\n"
    "s_waitcnt vmcnt(23) lgkmcnt(8)\n"
    "ds_read_b64_tr_b16 a[152:153], v247 offset:25088\n"
    "buffer_load_dwordx4 a[16:19], v255, s[48:51], s69 offen sc1\n"
    "v_mfma_f32_16x16x32_bf16 v[192:195], a[132:135], a[80:83], v[192:195]\n"
    "v_mfma_f32_16x16x32_bf16 v[128:131], a[128:131], a[68:71], v[128:131]\n"
    "s_waitcnt vmcnt(23)\n"
    "ds_read_b64_tr_b16 a[154:155], v247 offset:25344\n"
    "v_mfma_f32_16x16x32_bf16 v[128:131], a[132:135], a[84:87], v[128:131]\n"
    "v_mfma_f32_16x16x32_bf16 v[96:99], a[128:131], a[72:75], v[96:99]\n"
    "s_waitcnt vmcnt(22)\n"
    "ds_read_b64_tr_b16 a[156:157], v245 offset:512\n"
    "v_mfma_f32_16x16x32_bf16 v[96:99], a[132:135], a[88:91], v[96:99]\n"
    "buffer_load_dwordx4 a[20:23], v255, s[48:51], s69 offen offset:1024 sc1\n"
    "v_mfma_f32_16x16x32_bf16 v[160:163], a[128:131], a[76:79], v[160:163]\n"
    "s_waitcnt vmcnt(22)\n"
    "ds_read_b64_tr_b16 a[158:159], v245 offset:768\n"
    "v_mfma_f32_16x16x32_bf16 v[160:163], a[132:135], a[92:95], v[160:163]\n"
    "s_waitcnt lgkmcnt(10)\n"
    "v_mfma_f32_16x16x32_bf16 v[164:167], a[136:139], a[76:79], v[164:167]\n"
    "s_waitcnt lgkmcnt(8)\n"
    "ds_read_b64_tr_b16 a[160:161], v248 offset:25600\n"
    "v_mfma_f32_16x16x32_bf16 v[164:167], a[140:143], a[92:95], v[164:167]\n"
    "v_mfma_f32_16x16x32_bf16 v[100:103], a[136:139], a[72:75], v[100:103]\n"
    "ds_read_b64_tr_b16 a[162:163], v248 offset:25856\n"
    "buffer_load_dwordx4 a[24:27], v255, s[48:51], s69 offen offset:2048 sc1\n"
    "v_mfma_f32_16x16x32_bf16 v[100:103], a[140:143], a[88:91], v[100:103]\n"
    "v_mfma_f32_16x16x32_bf16 v[132:135], a[136:139], a[68:71], v[132:135]\n"
    "ds_read_b64_tr_b16 a[164:165], v246 offset:1024\n"
    "v_mfma_f32_16x16x32_bf16 v[132:135], a[140:143], a[84:87], v[132:135]\n"
    "v_mfma_f32_16x16x32_bf16 v[196:199], a[136:139], a[64:67], v[196:199]\n"
    "ds_read_b64_tr_b16 a[166:167], v246 offset:1280\n"
    "v_mfma_f32_16x16x32_bf16 v[196:199], a[140:143], a[80:83], v[196:199]\n"
    "s_waitcnt lgkmcnt(10)\n"
    "buffer_load_dwordx4 a[28:31], v255, s[48:51], s69 offen offset:3072 sc1\n"
    "v_mfma_f32_16x16x32_bf16 v[200:203], a[144:147], a[64:67], v[200:203]\n"
    "s_waitcnt lgkmcnt(8)\n"
    "ds_read_b64_tr_b16 a[168:169], v247 offset:25600\n"
    "v_mfma_f32_16x16x32_bf16 v[200:203], a[148:151], a[80:83], v[200:203]\n"
    "v_mfma_f32_16x16x32_bf16 v[136:139], a[144:147], a[68:71], v[136:139]\n"
    "ds_read_b64_tr_b16 a[170:171], v247 offset:25856\n"
    "v_mfma_f32_16x16x32_bf16 v[136:139], a[148:151], a[84:87], v[136:139]\n"
    "s_add_u32 s69, s69, 0x1000\n"
    "v_mfma_f32_16x16x32_bf16 v[104:107], a[144:147], a[72:75], v[104:107]\n"
    "ds_read_b64_tr_b16 a[172:173], v245 offset:1024\n"
    "buffer_load_dwordx4 a[32:35], v255, s[48:51], s69 offen sc1\n"
    "v_mfma_f32_16x16x32_bf16 v[104:107], a[148:151], a[88:91], v[104:107]\n"
    "v_mfma_f32_16x16x32_bf16 v[168:171], a[144:147], a[76:79], v[168:171]\n"
    "ds_read_b64_tr_b16 a[174:175], v245 offset:1280\n"
    "v_mfma_f32_16x16x32_bf16 v[168:171], a[148:151], a[92:95], v[168:171]\n"
    "s_waitcnt lgkmcnt(10)\n"
    "v_mfma_f32_16x16x32_bf16 v[172:175], a[152:155], a[76:79], v[172:175]\n"
    "s_waitcnt lgkmcnt(8)\n"
    "ds_read_b64_tr_b16 a[176:177], v248 offset:26112\n"
    "v_mfma_f32_16x16x32_bf16 v[172:175], a[156:159], a[92:95], v[172:175]\n"
    "buffer_load_dwordx4 a[36:39], v255, s[48:51], s69 offen offset:1024 sc1\n"
    "v_mfma_f32_16x16x32_bf16 v[108:111], a[152:155], a[72:75], v[108:111]\n"
    "ds_read_b64_tr_b16 a[178:179], v248 offset:26368\n"
    "v_mfma_f32_16x16x32_bf16 v[108:111], a[156:159], a[88:91], v[108:111]\n"
    "v_mfma_f32_16x16x32_bf16 v[140:143], a[152:155], a[68:71], v[140:143]\n"
    "ds_read_b64_tr_b16 a[180:181], v246 offset:1536\n"
    "v_mfma_f32_16x16x32_bf16 v[140:143], a[156:159], a[84:87], v[140:143]\n"
    "v_mfma_f32_16x16x32_bf16 v[204:207], a[152:155], a[64:67], v[204:207]\n"
    "ds_read_b64_tr_b16 a[182:183], v246 offset:1792\n"
    "v_mfma_f32_16x16x32_bf16 v[204:207], a[156:159], a[80:83], v[204:207]\n"
    "s_waitcnt lgkmcnt(10)\n"
    "buffer_load_dwordx4 a[40:43], v255, s[48:51], s69 offen offset:2048 sc1\n"
    "v_mfma_f32_16x16x32_bf16 v[208:211], a[160:163], a[64:67], v[208:211]\n"
    "s_waitcnt lgkmcnt(8)\n"
    "ds_read_b64_tr_b16 a[184:185], v247 offset:26112\n"
    "v_mfma_f32_16x16x32_bf16 v[208:211], a[164:167], a[80:83], v[208:211]\n"
    "v_mfma_f32_16x16x32_bf16 v[144:147], a[160:163], a[68:71], v[144:147]\n"
    "ds_read_b64_tr_b16 a[186:187], v247 offset:26368\n"
    "v_mfma_f32_16x16x32_bf16 v[144:147], a[164:167], a[84:87], v[144:147]\n"
    "v_mfma_f32_16x16x32_bf16 v[112:115], a[160:163], a[72:75], v[112:115]\n"
    "ds_read_b64_tr_b16 a[188:189], v245 offset:1536\n"
    "buffer_load_dwordx4 a[44:47], v255, s[48:51], s69 offen offset:3072 sc1\n"
    "v_mfma_f32_16x16x32_bf16 v[112:115], a[164:167], a[88:91], v[112:115]\n"
    "v_mfma_f32_16x16x32_bf16 v[176:179], a[160:163], a[76:79], v[176:179]\n"
    "ds_read_b64_tr_b16 a[190:191], v245 offset:1792\n"
    "v_mfma_f32_16x16x32_bf16 v[176:179], a[164:167], a[92:95], v[176:179]\n"
    "s_waitcnt lgkmcnt(10)\n"
    "v_mfma_f32_16x16x32_bf16 v[180:183], a[168:171], a[76:79], v[180:183]\n"
    "s_waitcnt vmcnt(22) lgkmcnt(8)\n"
    "s_barrier\n"
    "v_mov_b32_e64 v248, v244\n"
    "v_mfma_f32_16x16x32_bf16 v[180:183], a[172:175], a[92:95], v[180:183]\n"
    "s_add_u32 s69, s69, 0x1000\n"
    "v_mov_b32_e64 v247, v242\n"
    "v_mfma_f32_16x16x32_bf16 v[116:119], a[168:171], a[72:75], v[116:119]\n"
    "s_add_u32 m0, s68, 0xc000\n"
    "ds_read_b64_tr_b16 a[128:129], v248 offset:8192\n"
    "v_mfma_f32_16x16x32_bf16 v[116:119], a[172:175], a[88:91], v[116:119]\n"
    "buffer_load_dwordx4 v254, s[40:43], s62 offen lds\n"
    "v_mfma_f32_16x16x32_bf16 v[148:151], a[168:171], a[68:71], v[148:151]\n"
    "ds_read_b64_tr_b16 a[130:131], v248 offset:8448\n"
    "v_mfma_f32_16x16x32_bf16 v[148:151], a[172:175], a[84:87], v[148:151]\n"
    "s_add_u32 m0, s68, 0xc400\n"
    "ds_read_b64_tr_b16 a[132:133], v246 offset:16384\n"
    "v_mfma_f32_16x16x32_bf16 v[212:215], a[168:171], a[64:67], v[212:215]\n"
    "buffer_load_dwordx4 v254, s[40:43], s63 offen lds\n"
    "ds_read_b64_tr_b16 a[134:135], v246 offset:16640\n"
    "v_mfma_f32_16x16x32_bf16 v[212:215], a[172:175], a[80:83], v[212:215]\n"
    "s_waitcnt lgkmcnt(10)\n"
    "v_mfma_f32_16x16x32_bf16 v[216:219], a[176:179], a[64:67], v[216:219]\n"
    "s_waitcnt lgkmcnt(8)\n"
    "ds_read_b64_tr_b16 a[136:137], v247 offset:8192\n"
    "v_mfma_f32_16x16x32_bf16 v[216:219], a[180:183], a[80:83], v[216:219]\n"
    "v_mfma_f32_16x16x32_bf16 v[152:155], a[176:179], a[68:71], v[152:155]\n"
    "s_add_u32 s62, s62, s66\n"
    "ds_read_b64_tr_b16 a[138:139], v247 offset:8448\n"
    "v_mfma_f32_16x16x32_bf16 v[152:155], a[180:183], a[84:87], v[152:155]\n"
    "s_add_u32 s63, s63, s66\n"
    "v_mfma_f32_16x16x32_bf16 v[120:123], a[176:179], a[72:75], v[120:123]\n"
    "s_mov_b32 s40, s10\n"
    "s_and_b32 s41, s11, 0xffff\n"
    "s_mov_b32 s42, 0x1000000\n"
    "v_mfma_f32_16x16x32_bf16 v[120:123], a[180:183], a[88:91], v[120:123]\n"
    "s_mov_b32 s43, 0x20000\n"
    "s_mov_b32 s62, s64\n"
    "s_mov_b32 s63, s65\n"
    "v_mfma_f32_16x16x32_bf16 v[184:187], a[176:179], a[76:79], v[184:187]\n"
    "s_mov_b32 s66, 0x2000\n"
    "s_add_u32 m0, s68, 0xe000\n"
    "ds_read_b64_tr_b16 a[140:141], v245 offset:16384\n"
    "v_mfma_f32_16x16x32_bf16 v[184:187], a[180:183], a[92:95], v[184:187]\n"
    "s_waitcnt lgkmcnt(9)\n"
    "buffer_load_dwordx4 v253, s[40:43], s62 offen lds\n"
    "ds_read_b64_tr_b16 a[142:143], v245 offset:16640\n"
    "v_mfma_f32_16x16x32_bf16 v[188:191], a[184:187], a[76:79], v[188:191]\n"
    "s_waitcnt lgkmcnt(8)\n"
    "s_add_u32 m0, s68, 0xe400\n"
    "ds_read_b64_tr_b16 a[144:145], v248 offset:8704\n"
    "v_mfma_f32_16x16x32_bf16 v[188:191], a[188:191], a[92:95], v[188:191]\n"
    "buffer_load_dwordx4 v253, s[40:43], s63 offen lds\n"
    "v_mfma_f32_16x16x32_bf16 v[124:127], a[184:187], a[72:75], v[124:127]\n"
    "ds_read_b64_tr_b16 a[146:147], v248 offset:8960\n"
    "v_mfma_f32_16x16x32_bf16 v[124:127], a[188:191], a[88:91], v[124:127]\n"
    "v_mfma_f32_16x16x32_bf16 v[156:159], a[184:187], a[68:71], v[156:159]\n"
    "ds_read_b64_tr_b16 a[148:149], v246 offset:16896\n"
    "v_mfma_f32_16x16x32_bf16 v[156:159], a[188:191], a[84:87], v[156:159]\n"
    "s_add_u32 s62, s62, s66\n"
    "v_mfma_f32_16x16x32_bf16 v[220:223], a[184:187], a[64:67], v[220:223]\n"
    "s_add_u32 s63, s63, s66\n"
    "ds_read_b64_tr_b16 a[150:151], v246 offset:17152\n"
    "v_mfma_f32_16x16x32_bf16 v[220:223], a[188:191], a[80:83], v[220:223]\n"
    "s_waitcnt lgkmcnt(10)\n"
    "v_mfma_f32_16x16x32_bf16 v[192:195], a[128:131], a[96:99], v[192:195]\n"
    "s_waitcnt vmcnt(23) lgkmcnt(8)\n"
    "ds_read_b64_tr_b16 a[152:153], v247 offset:8704\n"
    "buffer_load_dwordx4 a[48:51], v255, s[48:51], s69 offen sc1\n"
    "v_mfma_f32_16x16x32_bf16 v[192:195], a[132:135], a[112:115], v[192:195]\n"
    "v_mfma_f32_16x16x32_bf16 v[128:131], a[128:131], a[100:103], v[128:131]\n"
    "s_waitcnt vmcnt(23)\n"
    "ds_read_b64_tr_b16 a[154:155], v247 offset:8960\n"
    "v_mfma_f32_16x16x32_bf16 v[128:131], a[132:135], a[116:119], v[128:131]\n"
    "v_mfma_f32_16x16x32_bf16 v[96:99], a[128:131], a[104:107], v[96:99]\n"
    "s_waitcnt vmcnt(22)\n"
    "ds_read_b64_tr_b16 a[156:157], v245 offset:16896\n"
    "v_mfma_f32_16x16x32_bf16 v[96:99], a[132:135], a[120:123], v[96:99]\n"
    "buffer_load_dwordx4 a[52:55], v255, s[48:51], s69 offen offset:1024 sc1\n"
    "v_mfma_f32_16x16x32_bf16 v[160:163], a[128:131], a[108:111], v[160:163]\n"
    "s_waitcnt vmcnt(22)\n"
    "ds_read_b64_tr_b16 a[158:159], v245 offset:17152\n"
    "v_mfma_f32_16x16x32_bf16 v[160:163], a[132:135], a[124:127], v[160:163]\n"
    "s_waitcnt lgkmcnt(10)\n"
    "v_mfma_f32_16x16x32_bf16 v[164:167], a[136:139], a[108:111], v[164:167]\n"
    "s_waitcnt lgkmcnt(8)\n"
    "ds_read_b64_tr_b16 a[160:161], v248 offset:9216\n"
    "v_mfma_f32_16x16x32_bf16 v[164:167], a[140:143], a[124:127], v[164:167]\n"
    "v_mfma_f32_16x16x32_bf16 v[100:103], a[136:139], a[104:107], v[100:103]\n"
    "ds_read_b64_tr_b16 a[162:163], v248 offset:9472\n"
    "buffer_load_dwordx4 a[56:59], v255, s[48:51], s69 offen offset:2048 sc1\n"
    "v_mfma_f32_16x16x32_bf16 v[100:103], a[140:143], a[120:123], v[100:103]\n"
    "v_mfma_f32_16x16x32_bf16 v[132:135], a[136:139], a[100:103], v[132:135]\n"
    "ds_read_b64_tr_b16 a[164:165], v246 offset:17408\n"
    "v_mfma_f32_16x16x32_bf16 v[132:135], a[140:143], a[116:119], v[132:135]\n"
    "v_mfma_f32_16x16x32_bf16 v[196:199], a[136:139], a[96:99], v[196:199]\n"
    "ds_read_b64_tr_b16 a[166:167], v246 offset:17664\n"
    "v_mfma_f32_16x16x32_bf16 v[196:199], a[140:143], a[112:115], v[196:199]\n"
    "s_waitcnt lgkmcnt(10)\n"
    "buffer_load_dwordx4 a[60:63], v255, s[48:51], s69 offen offset:3072 sc1\n"
    "v_mfma_f32_16x16x32_bf16 v[200:203], a[144:147], a[96:99], v[200:203]\n"
    "s_waitcnt lgkmcnt(8)\n"
    "ds_read_b64_tr_b16 a[168:169], v247 offset:9216\n"
    "v_mfma_f32_16x16x32_bf16 v[200:203], a[148:151], a[112:115], v[200:203]\n"
    "v_mfma_f32_16x16x32_bf16 v[136:139], a[144:147], a[100:103], v[136:139]\n"
    "ds_read_b64_tr_b16 a[170:171], v247 offset:9472\n"
    "v_mfma_f32_16x16x32_bf16 v[136:139], a[148:151], a[116:119], v[136:139]\n"
    "s_add_u32 s69, s69, 0x1000\n"
    "v_mfma_f32_16x16x32_bf16 v[104:107], a[144:147], a[104:107], v[104:107]\n"
    "s_add_u32 m0, s68, 0x10000\n"
    "ds_read_b64_tr_b16 a[172:173], v245 offset:17408\n"
    "v_mfma_f32_16x16x32_bf16 v[104:107], a[148:151], a[120:123], v[104:107]\n"
    "buffer_load_dwordx4 v253, s[40:43], s62 offen lds\n"
    "v_mfma_f32_16x16x32_bf16 v[168:171], a[144:147], a[108:111], v[168:171]\n"
    "ds_read_b64_tr_b16 a[174:175], v245 offset:17664\n"
    "v_mfma_f32_16x16x32_bf16 v[168:171], a[148:151], a[124:127], v[168:171]\n"
    "s_waitcnt lgkmcnt(10)\n"
    "v_mfma_f32_16x16x32_bf16 v[172:175], a[152:155], a[108:111], v[172:175]\n"
    "s_waitcnt lgkmcnt(8)\n"
    "s_add_u32 m0, s68, 0x10400\n"
    "ds_read_b64_tr_b16 a[176:177], v248 offset:9728\n"
    "v_mfma_f32_16x16x32_bf16 v[172:175], a[156:159], a[124:127], v[172:175]\n"
    "buffer_load_dwordx4 v253, s[40:43], s63 offen lds\n"
    "v_mfma_f32_16x16x32_bf16 v[108:111], a[152:155], a[104:107], v[108:111]\n"
    "ds_read_b64_tr_b16 a[178:179], v248 offset:9984\n"
    "v_mfma_f32_16x16x32_bf16 v[108:111], a[156:159], a[120:123], v[108:111]\n"
    "v_mfma_f32_16x16x32_bf16 v[140:143], a[152:155], a[100:103], v[140:143]\n"
    "ds_read_b64_tr_b16 a[180:181], v246 offset:17920\n"
    "v_mfma_f32_16x16x32_bf16 v[140:143], a[156:159], a[116:119], v[140:143]\n"
    "v_mfma_f32_16x16x32_bf16 v[204:207], a[152:155], a[96:99], v[204:207]\n"
    "s_add_u32 s62, s62, s66\n"
    "ds_read_b64_tr_b16 a[182:183], v246 offset:18176\n"
    "v_mfma_f32_16x16x32_bf16 v[204:207], a[156:159], a[112:115], v[204:207]\n"
    "s_waitcnt lgkmcnt(10)\n"
    "s_add_u32 s63, s63, s66\n"
    "s_add_u32 m0, s68, 0x12000\n"
    "v_mfma_f32_16x16x32_bf16 v[208:211], a[160:163], a[96:99], v[208:211]\n"
    "s_waitcnt lgkmcnt(8)\n"
    "buffer_load_dwordx4 v253, s[44:47], s64 offen lds\n"
    "ds_read_b64_tr_b16 a[184:185], v247 offset:9728\n"
    "v_mfma_f32_16x16x32_bf16 v[208:211], a[164:167], a[112:115], v[208:211]\n"
    "v_mfma_f32_16x16x32_bf16 v[144:147], a[160:163], a[100:103], v[144:147]\n"
    "ds_read_b64_tr_b16 a[186:187], v247 offset:9984\n"
    "v_mfma_f32_16x16x32_bf16 v[144:147], a[164:167], a[116:119], v[144:147]\n"
    "s_add_u32 m0, s68, 0x12400\n"
    "v_mfma_f32_16x16x32_bf16 v[112:115], a[160:163], a[104:107], v[112:115]\n"
    "ds_read_b64_tr_b16 a[188:189], v245 offset:17920\n"
    "buffer_load_dwordx4 v253, s[44:47], s65 offen lds\n"
    "v_mfma_f32_16x16x32_bf16 v[112:115], a[164:167], a[120:123], v[112:115]\n"
    "v_mfma_f32_16x16x32_bf16 v[176:179], a[160:163], a[108:111], v[176:179]\n"
    "ds_read_b64_tr_b16 a[190:191], v245 offset:18176\n"
    "v_mfma_f32_16x16x32_bf16 v[176:179], a[164:167], a[124:127], v[176:179]\n"
    "s_waitcnt lgkmcnt(10)\n"
    "v_mfma_f32_16x16x32_bf16 v[180:183], a[168:171], a[108:111], v[180:183]\n"
    "s_waitcnt vmcnt(22) lgkmcnt(8)\n"
    "s_barrier\n"
    "ds_read_b64_tr_b16 a[128:129], v248 offset:24576\n"
    "v_mfma_f32_16x16x32_bf16 v[180:183], a[172:175], a[124:127], v[180:183]\n"
    "s_add_u32 s64, s64, s67\n"
    "ds_read_b64_tr_b16 a[130:131], v248 offset:24832\n"
    "v_mfma_f32_16x16x32_bf16 v[116:119], a[168:171], a[104:107], v[116:119]\n"
    "s_add_u32 s65, s65, s67\n"
    "s_add_u32 m0, s68, 0x14000\n"
    "v_add_u32_e32 v246, 0x8000, v244\n"
    "v_mfma_f32_16x16x32_bf16 v[116:119], a[172:175], a[120:123], v[116:119]\n"
    "buffer_load_dwordx4 v253, s[40:43], s62 offen lds\n"
    "v_mfma_f32_16x16x32_bf16 v[148:151], a[168:171], a[100:103], v[148:151]\n"
    "v_add_u32_e32 v245, 0x8000, v242\n"
    "v_mfma_f32_16x16x32_bf16 v[148:151], a[172:175], a[116:119], v[148:151]\n"
    "s_add_u32 m0, s68, 0x14400\n"
    "ds_read_b64_tr_b16 a[132:133], v246 offset:0\n"
    "v_mfma_f32_16x16x32_bf16 v[212:215], a[168:171], a[96:99], v[212:215]\n"
    "buffer_load_dwordx4 v253, s[40:43], s63 offen lds\n"
    "ds_read_b64_tr_b16 a[134:135], v246 offset:256\n"
    "v_mfma_f32_16x16x32_bf16 v[212:215], a[172:175], a[112:115], v[212:215]\n"
    "s_waitcnt lgkmcnt(10)\n"
    "v_mfma_f32_16x16x32_bf16 v[216:219], a[176:179], a[96:99], v[216:219]\n"
    "s_waitcnt lgkmcnt(8)\n"
    "ds_read_b64_tr_b16 a[136:137], v247 offset:24576\n"
    "v_mfma_f32_16x16x32_bf16 v[216:219], a[180:183], a[112:115], v[216:219]\n"
    "v_mfma_f32_16x16x32_bf16 v[152:155], a[176:179], a[100:103], v[152:155]\n"
    "s_add_u32 s62, s62, s66\n"
    "ds_read_b64_tr_b16 a[138:139], v247 offset:24832\n"
    "v_mfma_f32_16x16x32_bf16 v[152:155], a[180:183], a[116:119], v[152:155]\n"
    "s_add_u32 s63, s63, s66\n"
    "v_readlane_b32 s96, v228, 0\n"
    "v_readlane_b32 s97, v228, 8\n"
    "v_mfma_f32_16x16x32_bf16 v[120:123], a[176:179], a[104:107], v[120:123]\n"
    "v_readlane_b32 s98, v228, 16\n"
    "v_readlane_b32 s82, v228, 24\n"
    "v_mfma_f32_16x16x32_bf16 v[120:123], a[180:183], a[120:123], v[120:123]\n"
    "v_and_b32_e32 v229, 0xff, v253\n"
    "v_mad_u32_u24 v229, v240, s82, v229\n"
    "v_mfma_f32_16x16x32_bf16 v[184:187], a[176:179], a[108:111], v[184:187]\n"
    "s_add_u32 m0, s68, 0x20000\n"
    "ds_read_b64_tr_b16 a[140:141], v245 offset:0\n"
    "ds_read_b64_tr_b16 a[142:143], v245 offset:256\n"
    "v_mfma_f32_16x16x32_bf16 v[184:187], a[180:183], a[124:127], v[184:187]\n"
    "s_waitcnt lgkmcnt(10)\n"
    "buffer_load_dwordx4 v229, s[96:99], 0 offen lds\n"
    "v_mfma_f32_16x16x32_bf16 v[188:191], a[184:187], a[108:111], v[188:191]\n"
    "s_waitcnt lgkmcnt(8)\n"
    "s_add_u32 m0, m0, 0x380\n"
    "ds_read_b64_tr_b16 a[144:145], v248 offset:25088\n"
    "v_mfma_f32_16x16x32_bf16 v[188:191], a[188:191], a[124:127], v[188:191]\n"
    "buffer_load_dwordx4 v229, s[96:99], 0 offen offset:128 lds\n"
    "v_mfma_f32_16x16x32_bf16 v[124:127], a[184:187], a[104:107], v[124:127]\n"
    "ds_read_b64_tr_b16 a[146:147], v248 offset:25344\n"
    "v_mfma_f32_16x16x32_bf16 v[124:127], a[188:191], a[120:123], v[124:127]\n"
    "v_mfma_f32_16x16x32_bf16 v[156:159], a[184:187], a[100:103], v[156:159]\n"
    "ds_read_b64_tr_b16 a[148:149], v246 offset:512\n"
    "v_mfma_f32_16x16x32_bf16 v[156:159], a[188:191], a[116:119], v[156:159]\n"
    "v_mfma_f32_16x16x32_bf16 v[220:223], a[184:187], a[96:99], v[220:223]\n"
    "ds_read_b64_tr_b16 a[150:151], v246 offset:768\n"
    "v_mfma_f32_16x16x32_bf16 v[220:223], a[188:191], a[112:115], v[220:223]\n"
    "s_waitcnt lgkmcnt(10)\n"
    "v_mfma_f32_16x16x32_bf16 v[192:195], a[128:131], a[0:3], v[192:195]\n"
    "s_waitcnt vmcnt(23) lgkmcnt(8)\n"
    "s_add_u32 m0, s68, 0x16000\n"
    "ds_read_b64_tr_b16 a[152:153], v247 offset:25088\n"
    "v_mfma_f32_16x16x32_bf16 v[192:195], a[132:135], a[16:19], v[192:195]\n"
    "s_and_b32 s38, s73, 7\n"
    "buffer_load_dwordx4 v253, s[44:47], s64 offen lds\n"
    "ds_read_b64_tr_b16 a[154:155], v247 offset:25344\n"
    "v_mfma_f32_16x16x32_bf16 v[128:131], a[128:131], a[4:7], v[128:131]\n"
    "s_waitcnt vmcnt(23)\n"
    "s_mul_i32 s39, s38, 0x5000\n"
    "v_mfma_f32_16x16x32_bf16 v[128:131], a[132:135], a[20:23], v[128:131]\n"
    "ds_read_b64_tr_b16 a[156:157], v245 offset:512\n"
    "v_mfma_f32_16x16x32_bf16 v[96:99], a[128:131], a[8:11], v[96:99]\n"
    "s_waitcnt vmcnt(22)\n"
    "s_add_u32 s8, s80, s39\n"
    "ds_read_b64_tr_b16 a[158:159], v245 offset:768\n"
    "v_mfma_f32_16x16x32_bf16 v[96:99], a[132:135], a[24:27], v[96:99]\n"
    "v_mfma_f32_16x16x32_bf16 v[160:163], a[128:131], a[12:15], v[160:163]\n"
    "s_waitcnt vmcnt(21) lgkmcnt(10)\n"
    "s_add_u32 m0, s68, 0x16400\n"
    "ds_read_b64_tr_b16 a[160:161], v248 offset:25600\n"
    "v_mfma_f32_16x16x32_bf16 v[160:163], a[132:135], a[28:31], v[160:163]\n"
    "s_cmp_gt_u32 s38, 0\n"
    "s_cselect_b32 s39, 0, 1\n"
    "ds_read_b64_tr_b16 a[162:163], v248 offset:25856\n"
    "buffer_load_dwordx4 v253, s[44:47], s65 offen lds\n"
    "v_mfma_f32_16x16x32_bf16 v[164:167], a[136:139], a[12:15], v[164:167]\n"
    "s_waitcnt lgkmcnt(10)\n"
    "v_mfma_f32_16x16x32_bf16 v[164:167], a[140:143], a[28:31], v[164:167]\n"
    "s_mul_i32 s39, s39, 0x5000\n"
    "ds_read_b64_tr_b16 a[164:165], v246 offset:1024\n"
    "v_mfma_f32_16x16x32_bf16 v[100:103], a[136:139], a[8:11], v[100:103]\n"
    "ds_read_b64_tr_b16 a[166:167], v246 offset:1280\n"
    "v_mfma_f32_16x16x32_bf16 v[100:103], a[140:143], a[24:27], v[100:103]\n"
    "s_add_u32 s9, s80, s39\n"
    "v_mfma_f32_16x16x32_bf16 v[132:135], a[136:139], a[4:7], v[132:135]\n"
    "ds_read_b64_tr_b16 a[168:169], v247 offset:25600\n"
    "v_mfma_f32_16x16x32_bf16 v[132:135], a[140:143], a[20:23], v[132:135]\n"
    "s_cmp_gt_u32 s38, 1\n"
    "s_cselect_b32 s39, 1, 2\n"
    "ds_read_b64_tr_b16 a[170:171], v247 offset:25856\n"
    "v_mfma_f32_16x16x32_bf16 v[196:199], a[136:139], a[0:3], v[196:199]\n"
    "s_add_u32 s64, s64, s67\n"
    "v_mfma_f32_16x16x32_bf16 v[196:199], a[140:143], a[16:19], v[196:199]\n"
    "s_waitcnt lgkmcnt(12)\n"
    "s_add_u32 s65, s65, s67\n"
    "ds_read_b64_tr_b16 a[172:173], v245 offset:1024\n"
    "v_mfma_f32_16x16x32_bf16 v[200:203], a[144:147], a[0:3], v[200:203]\n"
    "s_waitcnt lgkmcnt(11)\n"
    "s_add_u32 m0, s68, 0x18000\n"
    "ds_read_b64_tr_b16 a[174:175], v245 offset:1280\n"
    "v_mfma_f32_16x16x32_bf16 v[200:203], a[148:151], a[16:19], v[200:203]\n"
    "s_mul_i32 s39, s39, 0x5000\n"
    "buffer_load_dwordx4 v253, s[40:43], s62 offen lds\n"
    "v_mfma_f32_16x16x32_bf16 v[136:139], a[144:147], a[4:7], v[136:139]\n"
    "s_add_u32 s10, s80, s39\n"
    "ds_read_b64_tr_b16 a[176:177], v248 offset:26112\n"
    "v_mfma_f32_16x16x32_bf16 v[136:139], a[148:151], a[20:23], v[136:139]\n"
    "ds_read_b64_tr_b16 a[178:179], v248 offset:26368\n"
    "v_mfma_f32_16x16x32_bf16 v[104:107], a[144:147], a[8:11], v[104:107]\n"
    "s_cmp_gt_u32 s38, 2\n"
    "s_cselect_b32 s39, 2, 3\n"
    "v_mfma_f32_16x16x32_bf16 v[104:107], a[148:151], a[24:27], v[104:107]\n"
    "ds_read_b64_tr_b16 a[180:181], v246 offset:1536\n"
    "v_mfma_f32_16x16x32_bf16 v[168:171], a[144:147], a[12:15], v[168:171]\n"
    "s_add_u32 m0, s68, 0x18400\n"
    "ds_read_b64_tr_b16 a[182:183], v246 offset:1792\n"
    "v_mfma_f32_16x16x32_bf16 v[168:171], a[148:151], a[28:31], v[168:171]\n"
    "s_waitcnt lgkmcnt(14)\n"
    "s_mul_i32 s39, s39, 0x5000\n"
    "buffer_load_dwordx4 v253, s[40:43], s63 offen lds\n"
    "v_mfma_f32_16x16x32_bf16 v[172:175], a[152:155], a[12:15], v[172:175]\n"
    "s_waitcnt lgkmcnt(12)\n"
    "ds_read_b64_tr_b16 a[184:185], v247 offset:26112\n"
    "v_mfma_f32_16x16x32_bf16 v[172:175], a[156:159], a[28:31], v[172:175]\n"
    "s_add_u32 s11, s80, s39\n"
    "ds_read_b64_tr_b16 a[186:187], v247 offset:26368\n"
    "v_mfma_f32_16x16x32_bf16 v[108:111], a[152:155], a[8:11], v[108:111]\n"
    "v_mfma_f32_16x16x32_bf16 v[108:111], a[156:159], a[24:27], v[108:111]\n"
    "s_cmp_gt_u32 s38, 3\n"
    "s_cselect_b32 s39, 3, 4\n"
    "ds_read_b64_tr_b16 a[188:189], v245 offset:1536\n"
    "v_mfma_f32_16x16x32_bf16 v[140:143], a[152:155], a[4:7], v[140:143]\n"
    "ds_read_b64_tr_b16 a[190:191], v245 offset:1792\n"
    "v_mfma_f32_16x16x32_bf16 v[140:143], a[156:159], a[20:23], v[140:143]\n"
    "v_mfma_f32_16x16x32_bf16 v[204:207], a[152:155], a[0:3], v[204:207]\n"
    "s_waitcnt vmcnt(18) lgkmcnt(12)\n"
    "s_add_u32 s62, s62, s66\n"
    "v_mfma_f32_16x16x32_bf16 v[204:207], a[156:159], a[16:19], v[204:207]\n"
    "s_add_u32 s63, s63, s66\n"
    "s_add_u32 m0, s68, 0x1a000\n"
    "s_mul_i32 s39, s39, 0x5000\n"
    "v_mfma_f32_16x16x32_bf16 v[208:211], a[160:163], a[0:3], v[208:211]\n"
    "s_barrier\n"
    "buffer_load_dwordx4 v253, s[44:47], s64 offen lds\n"
    "v_add_u32_e32 v248, 0x8000, v244\n"
    "v_mfma_f32_16x16x32_bf16 v[208:211], a[164:167], a[16:19], v[208:211]\n"
    "s_add_u32 s12, s80, s39\n"
    "v_add_u32_e32 v247, 0x8000, v242\n"
    "v_mfma_f32_16x16x32_bf16 v[144:147], a[160:163], a[4:7], v[144:147]\n"
    "s_cmp_gt_u32 s38, 4\n"
    "s_cselect_b32 s39, 4, 5\n"
    "ds_read_b64_tr_b16 a[128:129], v248 offset:8192\n"
    "ds_read_b64_tr_b16 a[130:131], v248 offset:8448\n"
    "v_mfma_f32_16x16x32_bf16 v[144:147], a[164:167], a[20:23], v[144:147]\n"
    "ds_read_b64_tr_b16 a[132:133], v246 offset:16384\n"
    "ds_read_b64_tr_b16 a[134:135], v246 offset:16640\n"
    "v_mfma_f32_16x16x32_bf16 v[112:115], a[160:163], a[8:11], v[112:115]\n"
    "s_mul_i32 s39, s39, 0x5000\n"
    "ds_read_b64_tr_b16 a[136:137], v247 offset:8192\n"
    "ds_read_b64_tr_b16 a[138:139], v247 offset:8448\n"
    "v_mfma_f32_16x16x32_bf16 v[112:115], a[164:167], a[24:27], v[112:115]\n"
    "s_add_u32 m0, s68, 0x1a400\n"
    "ds_read_b64_tr_b16 a[140:141], v245 offset:16384\n"
    "v_mfma_f32_16x16x32_bf16 v[176:179], a[160:163], a[12:15], v[176:179]\n"
    "ds_read_b64_tr_b16 a[142:143], v245 offset:16640\n"
    "buffer_load_dwordx4 v253, s[44:47], s65 offen lds\n"
    "v_mfma_f32_16x16x32_bf16 v[176:179], a[164:167], a[28:31], v[176:179]\n"
    "s_waitcnt lgkmcnt(15)\n"
    "s_add_u32 s13, s80, s39\n"
    "v_mfma_f32_16x16x32_bf16 v[180:183], a[168:171], a[12:15], v[180:183]\n"
    "ds_read_b64_tr_b16 a[144:145], v248 offset:8704\n"
    "ds_read_b64_tr_b16 a[146:147], v248 offset:8960\n"
    "v_mfma_f32_16x16x32_bf16 v[180:183], a[172:175], a[28:31], v[180:183]\n"
    "s_cmp_gt_u32 s38, 5\n"
    "s_cselect_b32 s39, 5, 6\n"
    "ds_read_b64_tr_b16 a[148:149], v246 offset:16896\n"
    "v_mfma_f32_16x16x32_bf16 v[116:119], a[168:171], a[8:11], v[116:119]\n"
    "ds_read_b64_tr_b16 a[150:151], v246 offset:17152\n"
    "v_mfma_f32_16x16x32_bf16 v[116:119], a[172:175], a[24:27], v[116:119]\n"
    "s_mul_i32 s39, s39, 0x5000\n"
    "v_mfma_f32_16x16x32_bf16 v[148:151], a[168:171], a[4:7], v[148:151]\n"
    "ds_read_b64_tr_b16 a[152:153], v247 offset:8704\n"
    "ds_read_b64_tr_b16 a[154:155], v247 offset:8960\n"
    "v_mfma_f32_16x16x32_bf16 v[148:151], a[172:175], a[20:23], v[148:151]\n"
    "s_waitcnt lgkmcnt(15)\n"
    "s_add_u32 s64, s64, s67\n"
    "ds_read_b64_tr_b16 a[156:157], v245 offset:16896\n"
    "v_mfma_f32_16x16x32_bf16 v[212:215], a[168:171], a[0:3], v[212:215]\n"
    "s_add_u32 s65, s65, s67\n"
    "v_readlane_b32 s96, v228, 1\n"
    "v_readlane_b32 s97, v228, 9\n"
    "v_mfma_f32_16x16x32_bf16 v[212:215], a[172:175], a[16:19], v[212:215]\n"
    "v_readlane_b32 s98, v228, 17\n"
    "v_readlane_b32 s82, v228, 25\n"
    "v_mfma_f32_16x16x32_bf16 v[216:219], a[176:179], a[0:3], v[216:219]\n"
    "v_and_b32_e32 v229, 0xff, v253\n"
    "v_mad_u32_u24 v229, v240, s82, v229\n"
    "v_mfma_f32_16x16x32_bf16 v[216:219], a[180:183], a[16:19], v[216:219]\n"
    "s_add_u32 m0, s68, 0x22000\n"
    "s_add_u32 s14, s80, s39\n"
    "ds_read_b64_tr_b16 a[158:159], v245 offset:17152\n"
    "v_mfma_f32_16x16x32_bf16 v[152:155], a[176:179], a[4:7], v[152:155]\n"
    "s_add_u32 s16, s87, s39\n"
    "buffer_load_dwordx4 v229, s[96:99], 0 offen lds\n"
    "ds_read_b64_tr_b16 a[160:161], v248 offset:9216\n"
    "v_mfma_f32_16x16x32_bf16 v[152:155], a[180:183], a[20:23], v[152:155]\n"
    "s_cmp_gt_u32 s38, 6\n"
    "s_cselect_b32 s39, 6, 7\n"
    "ds_read_b64_tr_b16 a[162:163], v248 offset:9472\n"
    "ds_read_b64_tr_b16 a[164:165], v246 offset:17408\n"
    "v_mfma_f32_16x16x32_bf16 v[120:123], a[176:179], a[8:11], v[120:123]\n"
    "ds_read_b64_tr_b16 a[166:167], v246 offset:17664\n"
    "ds_read_b64_tr_b16 a[168:169], v247 offset:9216\n"
    "v_mfma_f32_16x16x32_bf16 v[120:123], a[180:183], a[24:27], v[120:123]\n"
    "s_mul_i32 s39, s39, 0x5000\n"
    "ds_read_b64_tr_b16 a[170:171], v247 offset:9472\n"
    "ds_read_b64_tr_b16 a[172:173], v245 offset:17408\n"
    "v_mfma_f32_16x16x32_bf16 v[184:187], a[176:179], a[12:15], v[184:187]\n"
    "s_add_u32 m0, m0, 0x380\n"
    "ds_read_b64_tr_b16 a[174:175], v245 offset:17664\n"
    "PG3_%=:\n"
    "s_cbranch_execz PG7_%=\n"
    "v_mfma_f32_16x16x32_bf16 v[184:187], a[180:183], a[28:31], v[184:187]\n"
    "s_add_u32 s15, s80, s39\n"
    "buffer_load_dwordx4 v229, s[96:99], 0 offen offset:128 lds\n"
    "v_mfma_f32_16x16x32_bf16 v[188:191], a[184:187], a[12:15], v[188:191]\n"
    "s_waitcnt lgkmcnt(15)\n"
    "ds_read_b64_tr_b16 a[176:177], v248 offset:9728\n"
    "ds_read_b64_tr_b16 a[178:179], v248 offset:9984\n"
    "v_mfma_f32_16x16x32_bf16 v[188:191], a[188:191], a[28:31], v[188:191]\n"
    "s_add_u32 s17, s87, s39\n"
    "ds_read_b64_tr_b16 a[180:181], v246 offset:17920\n"
    "v_mfma_f32_16x16x32_bf16 v[124:127], a[184:187], a[8:11], v[124:127]\n"
    "ds_read_b64_tr_b16 a[182:183], v246 offset:18176\n"
    "v_mfma_f32_16x16x32_bf16 v[124:127], a[188:191], a[24:27], v[124:127]\n"
    "v_mfma_f32_16x16x32_bf16 v[156:159], a[184:187], a[4:7], v[156:159]\n"
    "v_mfma_f32_16x16x32_bf16 v[156:159], a[188:191], a[20:23], v[156:159]\n"
    "v_mfma_f32_16x16x32_bf16 v[220:223], a[184:187], a[0:3], v[220:223]\n"
    "v_mfma_f32_16x16x32_bf16 v[220:223], a[188:191], a[16:19], v[220:223]\n"
    "v_mfma_f32_16x16x32_bf16 v[192:195], a[128:131], a[32:35], v[192:195]\n"
    "s_waitcnt vmcnt(19)\n"
    "s_add_u32 m0, s68, 0x1c000\n"
    "ds_read_b64_tr_b16 a[184:185], v247 offset:9728\n"
    "ds_read_b64_tr_b16 a[186:187], v247 offset:9984\n"
    "v_mfma_f32_16x16x32_bf16 v[192:195], a[132:135], a[48:51], v[192:195]\n"
    "buffer_load_dwordx4 v253, s[40:43], s62 offen lds\n"
    "ds_read_b64_tr_b16 a[188:189], v245 offset:17920\n"
    "v_mfma_f32_16x16x32_bf16 v[196:199], a[136:139], a[32:35], v[196:199]\n"
    "ds_read_b64_tr_b16 a[190:191], v245 offset:18176\n"
    "v_mfma_f32_16x16x32_bf16 v[196:199], a[140:143], a[48:51], v[196:199]\n"
    "s_waitcnt vmcnt(13) lgkmcnt(15)\n"
    "s_barrier\n"
    "v_mfma_f32_16x16x32_bf16 v[200:203], a[144:147], a[32:35], v[200:203]\n"
    "v_add_u32_e32 v246, 0x8000, v250\n"
    "v_add_u32_e32 v245, 0x8000, v249\n"
    "v_mfma_f32_16x16x32_bf16 v[200:203], a[148:151], a[48:51], v[200:203]\n"
    "ds_read_b64 a[192:193], v246 offset:24576\n"
    "v_mfma_f32_16x16x32_bf16 v[204:207], a[152:155], a[32:35], v[204:207]\n"
    "s_add_u32 m0, s68, 0x1c400\n"
    "ds_read_b64 a[194:195], v245 offset:24576\n"
    "ds_read_b64 a[196:197], v246 offset:25088\n"
    "v_mfma_f32_16x16x32_bf16 v[204:207], a[156:159], a[48:51], v[204:207]\n"
    "s_waitcnt lgkmcnt(15)\n"
    "ds_read_b64 a[198:199], v245 offset:25088\n"
    "buffer_load_dwordx4 v253, s[40:43], s63 offen lds\n"
    "v_mfma_f32_16x16x32_bf16 v[208:211], a[160:163], a[32:35], v[208:211]\n"
    "ds_read_b64 a[200:201], v246 offset:25600\n"
    "v_cvt_pk_bf16_f32 v0, v192, v193\n"
    "v_mfma_f32_16x16x32_bf16 v[208:211], a[164:167], a[48:51], v[208:211]\n"
    "s_waitcnt lgkmcnt(15)\n"
    "ds_read_b64 a[202:203], v245 offset:25600\n"
    "v_cvt_pk_bf16_f32 v1, v194, v195\n"
    "v_mfma_f32_16x16x32_bf16 v[212:215], a[168:171], a[32:35], v[212:215]\n"
    "s_waitcnt lgkmcnt(14)\n"
    "ds_read_b64 a[204:205], v246 offset:26112\n"
    "v_cvt_pk_bf16_f32 v2, v196, v197\n"
    "v_mfma_f32_16x16x32_bf16 v[212:215], a[172:175], a[48:51], v[212:215]\n"
    "s_waitcnt lgkmcnt(13)\n"
    "ds_read_b64 a[206:207], v245 offset:26112\n"
    "v_cvt_pk_bf16_f32 v3, v198, v199\n"
    "v_mfma_f32_16x16x32_bf16 v[216:219], a[176:179], a[32:35], v[216:219]\n"
    "s_waitcnt lgkmcnt(12)\n"
    "ds_read_b64 a[208:209], v246 offset:28672\n"
    "v_cvt_pk_bf16_f32 v4, v200, v201\n"
    "v_mfma_f32_16x16x32_bf16 v[216:219], a[180:183], a[48:51], v[216:219]\n"
    "s_waitcnt lgkmcnt(9)\n"
    "ds_read_b64 a[210:211], v245 offset:28672\n"
    "ds_read_b64 a[212:213], v246 offset:29184\n"
    "v_mfma_f32_16x16x32_bf16 v[220:223], a[184:187], a[32:35], v[220:223]\n"
    "s_add_u32 s62, s62, s66\n"
    "ds_read_b64 a[214:215], v245 offset:29184\n"
    "v_cvt_pk_bf16_f32 v5, v202, v203\n"
    "v_mfma_f32_16x16x32_bf16 v[220:223], a[188:191], a[48:51], v[220:223]\n"
    "s_add_u32 s63, s63, s66\n"
    "ds_read_b64 a[216:217], v246 offset:29696\n"
    "v_cvt_pk_bf16_f32 v6, v204, v205\n"
    "v_mfma_f32_16x16x32_bf16 v[128:131], a[128:131], a[36:39], v[128:131]\n"
    "s_add_u32 m0, s68, 0x1e000\n"
    "ds_read_b64 a[218:219], v245 offset:29696\n"
    "v_cvt_pk_bf16_f32 v7, v206, v207\n"
    "v_mfma_f32_16x16x32_bf16 v[128:131], a[132:135], a[52:55], v[128:131]\n"
    "ds_read_b64 a[220:221], v246 offset:30208\n"
    "v_cvt_pk_bf16_f32 v8, v208, v209\n"
    "v_mfma_f32_16x16x32_bf16 v[132:135], a[136:139], a[36:39], v[132:135]\n"
    "ds_read_b64 a[222:223], v245 offset:30208\n"
    "v_cvt_pk_bf16_f32 v9, v210, v211\n"
    "v_mfma_f32_16x16x32_bf16 v[132:135], a[140:143], a[52:55], v[132:135]\n"
    "buffer_load_dwordx4 v253, s[44:47], s64 offen lds\n"
    "v_add_u32_e32 v246, 0x10000, v250\n"
    "v_mfma_f32_16x16x32_bf16 v[136:139], a[144:147], a[36:39], v[136:139]\n"
    "v_cvt_pk_bf16_f32 v10, v212, v213\n"
    "v_cvt_pk_bf16_f32 v11, v214, v215\n"
    "v_mfma_f32_16x16x32_bf16 v[136:139], a[148:151], a[52:55], v[136:139]\n"
    "v_add_u32_e32 v245, 0x10000, v249\n"
    "v_cvt_pk_bf16_f32 v12, v216, v217\n"
    "v_mfma_f32_16x16x32_bf16 v[140:143], a[152:155], a[36:39], v[140:143]\n"
    "s_add_u32 m0, s68, 0x1e400\n"
    "ds_read_b64 a[64:65], v246 offset:0\n"
    "v_cvt_pk_bf16_f32 v13, v218, v219\n"
    "v_mfma_f32_16x16x32_bf16 v[140:143], a[156:159], a[52:55], v[140:143]\n"
    "ds_read_b64 a[66:67], v245 offset:0\n"
    "v_cvt_pk_bf16_f32 v14, v220, v221\n"
    "v_mfma_f32_16x16x32_bf16 v[144:147], a[160:163], a[36:39], v[144:147]\n"
    "ds_read_b64 a[68:69], v246 offset:512\n"
    "v_cvt_pk_bf16_f32 v15, v222, v223\n"
    "v_mfma_f32_16x16x32_bf16 v[144:147], a[164:167], a[52:55], v[144:147]\n"
    "ds_read_b64 a[70:71], v245 offset:512\n"
    "ds_read_b64 a[72:73], v246 offset:1024\n"
    "v_mfma_f32_16x16x32_bf16 v[148:151], a[168:171], a[36:39], v[148:151]\n"
    "ds_read_b64 a[74:75], v245 offset:1024\n"
    "buffer_load_dwordx4 v253, s[44:47], s65 offen lds\n"
    "v_mfma_f32_16x16x32_bf16 v[148:151], a[172:175], a[52:55], v[148:151]\n"
    "ds_read_b64 a[76:77], v246 offset:1536\n"
    "v_cvt_pk_bf16_f32 v16, v128, v129\n"
    "v_mfma_f32_16x16x32_bf16 v[152:155], a[176:179], a[36:39], v[152:155]\n"
    "ds_read_b64 a[78:79], v245 offset:1536\n"
    "v_cvt_pk_bf16_f32 v17, v130, v131\n"
    "v_mfma_f32_16x16x32_bf16 v[152:155], a[180:183], a[52:55], v[152:155]\n"
    "s_waitcnt lgkmcnt(15)\n"
    "ds_read_b64 a[80:81], v246 offset:4096\n"
    "v_cvt_pk_bf16_f32 v18, v132, v133\n"
    "v_mfma_f32_16x16x32_bf16 v[156:159], a[184:187], a[36:39], v[156:159]\n"
    "s_add_u32 s64, s64, s67\n"
    "ds_read_b64 a[82:83], v245 offset:4096\n"
    "v_cvt_pk_bf16_f32 v19, v134, v135\n"
    "v_mfma_f32_16x16x32_bf16 v[156:159], a[188:191], a[52:55], v[156:159]\n"
    "s_add_u32 s65, s65, s67\n"
    "ds_read_b64 a[84:85], v246 offset:4608\n"
    "v_cvt_pk_bf16_f32 v20, v136, v137\n"
    "v_mfma_f32_16x16x32_bf16 v[64:67], a[192:195], v[0:3], 0\n"
    "s_add_u32 m0, s68, 0\n"
    "ds_read_b64 a[86:87], v245 offset:4608\n"
    "v_cvt_pk_bf16_f32 v21, v138, v139\n"
    "v_mfma_f32_16x16x32_bf16 v[64:67], a[196:199], v[4:7], v[64:67]\n"
    "ds_read_b64 a[88:89], v246 offset:5120\n"
    "v_cvt_pk_bf16_f32 v22, v140, v141\n"
    "v_mfma_f32_16x16x32_bf16 v[64:67], a[200:203], v[8:11], v[64:67]\n"
    "ds_read_b64 a[90:91], v245 offset:5120\n"
    "v_cvt_pk_bf16_f32 v23, v142, v143\n"
    "v_mfma_f32_16x16x32_bf16 v[64:67], a[204:207], v[12:15], v[64:67]\n"
    "s_waitcnt lgkmcnt(15)\n"
    "buffer_load_dwordx4 v253, s[40:43], s62 offen lds\n"
    "v_cvt_pk_bf16_f32 v24, v144, v145\n"
    "v_mfma_f32_16x16x32_bf16 v[68:71], a[208:211], v[0:3], 0\n"
    "ds_read_b64 a[92:93], v246 offset:5632\n"
    "v_cvt_pk_bf16_f32 v25, v146, v147\n"
    "v_mfma_f32_16x16x32_bf16 v[68:71], a[212:215], v[4:7], v[68:71]\n"
    "s_add_u32 m0, s68, 0x400\n"
    "ds_read_b64 a[94:95], v245 offset:5632\n"
    "v_cvt_pk_bf16_f32 v26, v148, v149\n"
    "v_mfma_f32_16x16x32_bf16 v[68:71], a[216:219], v[8:11], v[68:71]\n"
    "s_waitcnt lgkmcnt(15)\n"
    "v_cvt_pk_bf16_f32 v27, v150, v151\n"
    "v_add_u32_e32 v248, 0x10000, v252\n"
    "v_mfma_f32_16x16x32_bf16 v[68:71], a[220:223], v[12:15], v[68:71]\n"
    "v_cvt_pk_bf16_f32 v28, v152, v153\n"
    "v_add_u32_e32 v247, 0x10000, v251\n"
    "v_mfma_f32_16x16x32_bf16 v[96:99], a[128:131], a[40:43], v[96:99]\n"
    "ds_read_b64_tr_b16 a[224:225], v248 offset:8192\n"
    "v_cvt_pk_bf16_f32 v29, v154, v155\n"
    "v_mfma_f32_16x16x32_bf16 v[96:99], a[132:135], a[56:59], v[96:99]\n"
    "ds_read_b64_tr_b16 a[226:227], v248 offset:12288\n"
    "v_cvt_pk_bf16_f32 v30, v156, v157\n"
    "v_mfma_f32_16x16x32_bf16 v[100:103], a[136:139], a[40:43], v[100:103]\n"
    "ds_read_b64_tr_b16 a[228:229], v247 offset:8192\n"
    "v_cvt_pk_bf16_f32 v31, v158, v159\n"
    "v_mfma_f32_16x16x32_bf16 v[100:103], a[140:143], a[56:59], v[100:103]\n"
    "ds_read_b64_tr_b16 a[230:231], v247 offset:12288\n"
    "ds_read_b64_tr_b16 a[232:233], v248 offset:8704\n"
    "v_max3_f32 v236, v64, v65, v66\n"
    "v_max3_f32 v237, v67, v68, v69\n"
    "v_mfma_f32_16x16x32_bf16 v[104:107], a[144:147], a[40:43], v[104:107]\n"
    "buffer_load_dwordx4 v253, s[40:43], s63 offen lds\n"
    "ds_read_b64_tr_b16 a[234:235], v248 offset:12800\n"
    "v_max3_f32 v236, v236, v70, v71\n"
    "v_mfma_f32_16x16x32_bf16 v[104:107], a[148:151], a[56:59], v[104:107]\n"
    "ds_read_b64_tr_b16 a[236:237], v247 offset:8704\n"
    "ds_read_b64_tr_b16 a[238:239], v247 offset:12800\n"
    "v_max_f32_e32 v236, v236, v237\n"
    "v_mov_b32_e32 v237, v236\n"
    "v_mfma_f32_16x16x32_bf16 v[108:111], a[152:155], a[40:43], v[108:111]\n"
    "s_add_u32 s62, s62, s66\n"
    "ds_read_b64_tr_b16 a[240:241], v248 offset:9216\n"
    "ds_read_b64_tr_b16 a[242:243], v248 offset:13312\n"
    "v_permlane32_swap_b32 v236, v237\n"
    "v_mfma_f32_16x16x32_bf16 v[108:111], a[156:159], a[56:59], v[108:111]\n"
    "s_add_u32 s63, s63, s66\n"
    "ds_read_b64_tr_b16 a[244:245], v247 offset:9216\n"
    "ds_read_b64_tr_b16 a[246:247], v247 offset:13312\n"
    "v_max_f32_e32 v236, v236, v237\n"
    "v_mfma_f32_16x16x32_bf16 v[112:115], a[160:163], a[40:43], v[112:115]\n"
    "s_add_u32 m0, s68, 0x2000\n"
    "ds_read_b64_tr_b16 a[248:249], v248 offset:9728\n"
    "v_mov_b32_e32 v237, v236\n"
    "s_nop 1\n"
    "v_permlane16_swap_b32 v236, v237\n"
    "v_mfma_f32_16x16x32_bf16 v[112:115], a[164:167], a[56:59], v[112:115]\n"
    "buffer_load_dwordx4 v253, s[44:47], s64 offen lds\n"
    "ds_read_b64_tr_b16 a[250:251], v248 offset:13824\n"
    "v_max_f32_e32 v236, v236, v237\n"
    "v_mfma_f32_16x16x32_bf16 v[116:119], a[168:171], a[40:43], v[116:119]\n"
    "ds_read_b64_tr_b16 a[252:253], v247 offset:9728\n"
    "v_cvt_pk_bf16_f32 v32, v96, v97\n"
    "v_sub_f32_e32 v192, 0, v236\n"
    "v_sub_f32_e32 v193, 0, v236\n"
    "v_mfma_f32_16x16x32_bf16 v[116:119], a[172:175], a[56:59], v[116:119]\n"
    "ds_read_b64_tr_b16 a[254:255], v247 offset:13824\n"
    "v_cvt_pk_bf16_f32 v33, v98, v99\n"
    "v_sub_f32_e64 v194, 0, v236\n"
    "v_mfma_f32_16x16x32_bf16 v[120:123], a[176:179], a[40:43], v[120:123]\n"
    "v_cvt_pk_bf16_f32 v34, v100, v101\n"
    "v_cvt_pk_bf16_f32 v35, v102, v103\n"
    "v_sub_f32_e64 v195, 0, v236\n"
    "v_mfma_f32_16x16x32_bf16 v[120:123], a[180:183], a[56:59], v[120:123]\n"
    "s_add_u32 m0, s68, 0x2400\n"
    "v_cvt_pk_bf16_f32 v36, v104, v105\n"
    "v_cvt_pk_bf16_f32 v37, v106, v107\n"
    "v_mfma_f32_16x16x32_bf16 v[124:127], a[184:187], a[40:43], v[124:127]\n"
    "v_cvt_pk_bf16_f32 v38, v108, v109\n"
    "v_cvt_pk_bf16_f32 v39, v110, v111\n"
    "v_mfma_f32_16x16x32_bf16 v[124:127], a[188:191], a[56:59], v[124:127]\n"
    "buffer_load_dwordx4 v253, s[44:47], s65 offen lds\n"
    "v_add_f32_e64 v64, v192, v64\n"
    "v_mfma_f32_16x16x32_bf16 v[72:75], a[192:195], v[16:19], 0\n"
    "v_cvt_pk_bf16_f32 v40, v112, v113\n"
    "v_add_f32_e64 v65, v192, v65\n"
    "v_mfma_f32_16x16x32_bf16 v[72:75], a[196:199], v[20:23], v[72:75]\n"
    "v_cvt_pk_bf16_f32 v41, v114, v115\n"
    "v_add_f32_e64 v66, v192, v66\n"
    "v_mfma_f32_16x16x32_bf16 v[72:75], a[200:203], v[24:27], v[72:75]\n"
    "v_add_f32_e64 v67, v192, v67\n"
    "v_cvt_pk_bf16_f32 v42, v116, v117\n"
    "v_mfma_f32_16x16x32_bf16 v[72:75], a[204:207], v[28:31], v[72:75]\n"
    "v_add_f32_e64 v68, v192, v68\n"
    "v_cvt_pk_bf16_f32 v43, v118, v119\n"
    "v_mfma_f32_16x16x32_bf16 v[76:79], a[208:211], v[16:19], 0\n"
    "v_add_f32_e64 v69, v192, v69\n"
    "v_cvt_pk_bf16_f32 v44, v120, v121\n"
    "v_mfma_f32_16x16x32_bf16 v[76:79], a[212:215], v[20:23], v[76:79]\n"
    "s_add_u32 s64, s64, s67\n"
    "v_add_f32_e32 v70, v192, v70\n"
    "v_cvt_pk_bf16_f32 v45, v122, v123\n"
    "v_mfma_f32_16x16x32_bf16 v[76:79], a[216:219], v[24:27], v[76:79]\n"
    "s_add_u32 s65, s65, s67\n"
    "v_add_f32_e32 v71, v192, v71\n"
    "v_cvt_pk_bf16_f32 v46, v124, v125\n"
    "v_mfma_f32_16x16x32_bf16 v[76:79], a[220:223], v[28:31], v[76:79]\n"
    "v_exp_f32_e64 v64, v64\n"
    "v_mfma_f32_16x16x32_bf16 v[160:163], a[128:131], a[44:47], v[160:163]\n"
    "v_cvt_pk_bf16_f32 v47, v126, v127\n"
    "v_mfma_f32_16x16x32_bf16 v[160:163], a[132:135], a[60:63], v[160:163]\n"
    "v_exp_f32_e64 v65, v65\n"
    "v_mfma_f32_16x16x32_bf16 v[164:167], a[136:139], a[44:47], v[164:167]\n"
    "v_cvt_pk_bf16_f32 v128, v64, v65\n"
    "v_max3_f32 v236, v72, v73, v74\n"
    "v_mfma_f32_16x16x32_bf16 v[164:167], a[140:143], a[60:63], v[164:167]\n"
    "v_exp_f32_e64 v66, v66\n"
    "v_max3_f32 v237, v75, v76, v77\n"
    "v_mfma_f32_16x16x32_bf16 v[168:171], a[144:147], a[44:47], v[168:171]\n"
    "v_exp_f32_e64 v67, v67\n"
    "v_max3_f32 v236, v236, v78, v79\n"
    "v_mfma_f32_16x16x32_bf16 v[168:171], a[148:151], a[60:63], v[168:171]\n"
    "v_cvt_pk_bf16_f32 v129, v66, v67\n"
    "v_max_f32_e32 v236, v236, v237\n"
    "v_mov_b32_e32 v237, v236\n"
    "v_mfma_f32_16x16x32_bf16 v[172:175], a[152:155], a[44:47], v[172:175]\n"
    "v_exp_f32_e32 v68, v68\n"
    "v_permlane32_swap_b32 v236, v237\n"
    "v_mfma_f32_16x16x32_bf16 v[172:175], a[156:159], a[60:63], v[172:175]\n"
    "v_exp_f32_e32 v69, v69\n"
    "v_max_f32_e32 v236, v236, v237\n"
    "v_mfma_f32_16x16x32_bf16 v[176:179], a[160:163], a[44:47], v[176:179]\n"
    "v_cvt_pk_bf16_f32 v130, v68, v69\n"
    "v_mov_b32_e32 v237, v236\n"
    "s_nop 1\n"
    "v_permlane16_swap_b32 v236, v237\n"
    "v_mfma_f32_16x16x32_bf16 v[176:179], a[164:167], a[60:63], v[176:179]\n"
    "v_exp_f32_e32 v70, v70\n"
    "v_max_f32_e64 v236, v236, v237\n"
    "v_mfma_f32_16x16x32_bf16 v[180:183], a[168:171], a[44:47], v[180:183]\n"
    "v_exp_f32_e32 v71, v71\n"
    "v_sub_f32_e32 v196, 0, v236\n"
    "v_sub_f32_e64 v197, 0, v236\n"
    "v_mfma_f32_16x16x32_bf16 v[180:183], a[172:175], a[60:63], v[180:183]\n"
    "v_cvt_pk_bf16_f32 v131, v70, v71\n"
    "v_cvt_pk_bf16_f32 v48, v160, v161\n"
    "v_sub_f32_e64 v198, 0, v236\n"
    "v_mfma_f32_16x16x32_bf16 v[184:187], a[176:179], a[44:47], v[184:187]\n"
    "v_cvt_pk_bf16_f32 v49, v162, v163\n"
    "v_cvt_pk_bf16_f32 v50, v164, v165\n"
    "v_sub_f32_e64 v199, 0, v236\n"
    "v_mfma_f32_16x16x32_bf16 v[184:187], a[180:183], a[60:63], v[184:187]\n"
    "v_cvt_pk_bf16_f32 v51, v166, v167\n"
    "v_cvt_pk_bf16_f32 v52, v168, v169\n"
    "v_mfma_f32_16x16x32_bf16 v[188:191], a[184:187], a[44:47], v[188:191]\n"
    "v_cvt_pk_bf16_f32 v53, v170, v171\n"
    "v_cvt_pk_bf16_f32 v54, v172, v173\n"
    "v_mfma_f32_16x16x32_bf16 v[188:191], a[188:191], a[60:63], v[188:191]\n"
    "v_cvt_pk_bf16_f32 v55, v174, v175\n"
    "v_add_f32_e64 v72, v196, v72\n"
    "v_mfma_f32_16x16x32_bf16 v[80:83], a[192:195], v[32:35], 0\n"
    "v_cvt_pk_bf16_f32 v56, v176, v177\n"
    "v_add_f32_e64 v73, v196, v73\n"
    "v_mfma_f32_16x16x32_bf16 v[80:83], a[196:199], v[36:39], v[80:83]\n"
    "v_cvt_pk_bf16_f32 v57, v178, v179\n"
    "v_add_f32_e64 v74, v196, v74\n"
    "v_mfma_f32_16x16x32_bf16 v[80:83], a[200:203], v[40:43], v[80:83]\n"
    "v_add_f32_e64 v75, v196, v75\n"
    "v_cvt_pk_bf16_f32 v58, v180, v181\n"
    "v_mfma_f32_16x16x32_bf16 v[80:83], a[204:207], v[44:47], v[80:83]\n"
    "v_add_f32_e64 v76, v196, v76\n"
    "v_cvt_pk_bf16_f32 v59, v182, v183\n"
    "v_mfma_f32_16x16x32_bf16 v[84:87], a[208:211], v[32:35], 0\n"
    "v_add_f32_e64 v77, v196, v77\n"
    "v_cvt_pk_bf16_f32 v60, v184, v185\n"
    "v_mfma_f32_16x16x32_bf16 v[84:87], a[212:215], v[36:39], v[84:87]\n"
    "v_add_f32_e64 v78, v196, v78\n"
    "v_cvt_pk_bf16_f32 v61, v186, v187\n"
    "v_mfma_f32_16x16x32_bf16 v[84:87], a[216:219], v[40:43], v[84:87]\n"
    "v_add_f32_e64 v79, v196, v79\n"
    "v_cvt_pk_bf16_f32 v62, v188, v189\n"
    "v_mfma_f32_16x16x32_bf16 v[84:87], a[220:223], v[44:47], v[84:87]\n"
    "s_waitcnt lgkmcnt(15)\n"
    "v_exp_f32_e32 v72, v72\n"
    "v_mfma_f32_16x16x32_bf16 v[96:99], a[64:67], v[0:3], v[192:195]\n"
    "v_cvt_pk_bf16_f32 v63, v190, v191\n"
    "v_mfma_f32_16x16x32_bf16 v[96:99], a[68:71], v[4:7], v[96:99]\n"
    "v_exp_f32_e64 v73, v73\n"
    "v_mfma_f32_16x16x32_bf16 v[96:99], a[72:75], v[8:11], v[96:99]\n"
    "v_cvt_pk_bf16_f32 v132, v72, v73\n"
    "v_max3_f32 v236, v80, v81, v82\n"
    "v_mfma_f32_16x16x32_bf16 v[96:99], a[76:79], v[12:15], v[96:99]\n"
    "v_exp_f32_e64 v74, v74\n"
    "v_max3_f32 v237, v83, v84, v85\n"
    "v_mfma_f32_16x16x32_bf16 v[100:103], a[80:83], v[0:3], v[192:195]\n"
    "v_exp_f32_e64 v75, v75\n"
    "v_max3_f32 v236, v236, v86, v87\n"
    "v_mfma_f32_16x16x32_bf16 v[100:103], a[84:87], v[4:7], v[100:103]\n"
    "v_cvt_pk_bf16_f32 v133, v74, v75\n"
    "v_max_f32_e32 v236, v236, v237\n"
    "v_mov_b32_e32 v237, v236\n"
    "v_mfma_f32_16x16x32_bf16 v[100:103], a[88:91], v[8:11], v[100:103]\n"
    "v_exp_f32_e32 v76, v76\n"
    "v_permlane32_swap_b32 v236, v237\n"
    "v_mfma_f32_16x16x32_bf16 v[100:103], a[92:95], v[12:15], v[100:103]\n"
    "v_exp_f32_e32 v77, v77\n"
    "v_max_f32_e32 v236, v236, v237\n"
    "v_mfma_f32_16x16x32_bf16 v[88:91], a[192:195], v[48:51], 0\n"
    "v_cvt_pk_bf16_f32 v134, v76, v77\n"
    "v_mov_b32_e32 v237, v236\n"
    "s_nop 1\n"
    "v_permlane16_swap_b32 v236, v237\n"
    "v_mfma_f32_16x16x32_bf16 v[88:91], a[196:199], v[52:55], v[88:91]\n"
    "v_exp_f32_e32 v78, v78\n"
    "v_max_f32_e64 v236, v236, v237\n"
    "v_mfma_f32_16x16x32_bf16 v[88:91], a[200:203], v[56:59], v[88:91]\n"
    "v_exp_f32_e32 v79, v79\n"
    "v_sub_f32_e32 v200, 0, v236\n"
    "v_sub_f32_e64 v201, 0, v236\n"
    "v_mfma_f32_16x16x32_bf16 v[88:91], a[204:207], v[60:63], v[88:91]\n"
    "v_cvt_pk_bf16_f32 v135, v78, v79\n"
    "v_sub_f32_e64 v202, 0, v236\n"
    "v_mfma_f32_16x16x32_bf16 v[92:95], a[208:211], v[48:51], 0\n"
    "v_sub_f32_e64 v203, 0, v236\n"
    "v_mfma_f32_16x16x32_bf16 v[92:95], a[212:215], v[52:55], v[92:95]\n"
    "v_mfma_f32_16x16x32_bf16 v[92:95], a[216:219], v[56:59], v[92:95]\n"
    "v_add_f32_e32 v80, v200, v80\n"
    "v_add_f32_e32 v81, v200, v81\n"
    "v_mfma_f32_16x16x32_bf16 v[92:95], a[220:223], v[60:63], v[92:95]\n"
    "v_add_f32_e64 v82, v200, v82\n"
    "v_mfma_f32_16x16x32_bf16 v[104:107], a[64:67], v[16:19], v[196:199]\n"
    "v_add_f32_e32 v83, v200, v83\n"
    "v_add_f32_e32 v84, v200, v84\n"
    "v_mfma_f32_16x16x32_bf16 v[104:107], a[68:71], v[20:23], v[104:107]\n"
    "v_add_f32_e64 v85, v200, v85\n"
    "v_mfma_f32_16x16x32_bf16 v[104:107], a[72:75], v[24:27], v[104:107]\n"
    "v_max3_f32 v236, v88, v89, v90\n"
    "v_add_f32_e64 v86, v200, v86\n"
    "v_mfma_f32_16x16x32_bf16 v[104:107], a[76:79], v[28:31], v[104:107]\n"
    "v_max3_f32 v237, v91, v92, v93\n"
    "v_add_f32_e64 v87, v200, v87\n"
    "v_mfma_f32_16x16x32_bf16 v[108:111], a[80:83], v[16:19], v[196:199]\n"
    "v_max3_f32 v236, v236, v94, v95\n"
    "v_max_f32_e64 v236, v236, v237\n"
    "v_mfma_f32_16x16x32_bf16 v[108:111], a[84:87], v[20:23], v[108:111]\n"
    "v_exp_f32_e32 v80, v80\n"
    "v_mov_b32_e32 v237, v236\n"
    "v_mfma_f32_16x16x32_bf16 v[108:111], a[88:91], v[24:27], v[108:111]\n"
    "v_exp_f32_e32 v81, v81\n"
    "v_permlane32_swap_b32 v236, v237\n"
    "v_mfma_f32_16x16x32_bf16 v[108:111], a[92:95], v[28:31], v[108:111]\n"
    "v_cvt_pk_bf16_f32 v136, v80, v81\n"
    "v_max_f32_e64 v236, v236, v237\n"
    "v_mfma_f32_16x16x32_bf16 v[112:115], a[64:67], v[32:35], v[200:203]\n"
    "v_exp_f32_e32 v82, v82\n"
    "v_mov_b32_e32 v237, v236\n"
    "s_nop 1\n"
    "v_permlane16_swap_b32 v236, v237\n"
    "v_mfma_f32_16x16x32_bf16 v[112:115], a[68:71], v[36:39], v[112:115]\n"
    "v_exp_f32_e32 v83, v83\n"
    "v_max_f32_e32 v236, v236, v237\n"
    "v_mfma_f32_16x16x32_bf16 v[112:115], a[72:75], v[40:43], v[112:115]\n"
    "v_cvt_pk_bf16_f32 v137, v82, v83\n"
    "v_sub_f32_e32 v204, 0, v236\n"
    "v_sub_f32_e32 v205, 0, v236\n"
    "v_mfma_f32_16x16x32_bf16 v[112:115], a[76:79], v[44:47], v[112:115]\n"
    "v_exp_f32_e32 v84, v84\n"
    "v_sub_f32_e32 v206, 0, v236\n"
    "v_mfma_f32_16x16x32_bf16 v[116:119], a[80:83], v[32:35], v[200:203]\n"
    "v_exp_f32_e32 v85, v85\n"
    "v_sub_f32_e32 v207, 0, v236\n"
    "v_mfma_f32_16x16x32_bf16 v[116:119], a[84:87], v[36:39], v[116:119]\n"
    "v_cvt_pk_bf16_f32 v138, v84, v85\n"
    "v_mfma_f32_16x16x32_bf16 v[116:119], a[88:91], v[40:43], v[116:119]\n"
    "v_exp_f32_e64 v86, v86\n"
    "v_mfma_f32_16x16x32_bf16 v[116:119], a[92:95], v[44:47], v[116:119]\n"
    "v_exp_f32_e64 v87, v87\n"
    "v_mfma_f32_16x16x32_bf16 v[120:123], a[64:67], v[48:51], v[204:207]\n"
    "v_cvt_pk_bf16_f32 v139, v86, v87\n"
    "v_add_f32_e64 v88, v204, v88\n"
    "v_mfma_f32_16x16x32_bf16 v[120:123], a[68:71], v[52:55], v[120:123]\n"
    "v_add_f32_e32 v89, v204, v89\n"
    "v_add_f32_e32 v90, v204, v90\n"
    "v_mfma_f32_16x16x32_bf16 v[120:123], a[72:75], v[56:59], v[120:123]\n"
    "v_add_f32_e32 v91, v204, v91\n"
    "v_add_f32_e32 v92, v204, v92\n"
    "v_mfma_f32_16x16x32_bf16 v[120:123], a[76:79], v[60:63], v[120:123]\n"
    "v_add_f32_e32 v93, v204, v93\n"
    "v_add_f32_e32 v94, v204, v94\n"
    "v_mfma_f32_16x16x32_bf16 v[124:127], a[80:83], v[48:51], v[204:207]\n"
    "v_add_f32_e64 v95, v204, v95\n"
    "v_mfma_f32_16x16x32_bf16 v[124:127], a[84:87], v[52:55], v[124:127]\n"
    "v_exp_f32_e64 v88, v88\n"
    "v_mfma_f32_16x16x32_bf16 v[124:127], a[88:91], v[56:59], v[124:127]\n"
    "v_exp_f32_e64 v89, v89\n"
    "v_mfma_f32_16x16x32_bf16 v[124:127], a[92:95], v[60:63], v[124:127]\n"
    "v_cvt_pk_bf16_f32 v140, v88, v89\n"
    "v_exp_f32_e32 v90, v90\n"
    "v_exp_f32_e32 v91, v91\n"
    "s_nop 0\n"
    "v_cvt_pk_bf16_f32 v141, v90, v91\n"
    "v_exp_f32_e32 v92, v92\n"
    "v_exp_f32_e32 v93, v93\n"
    "s_nop 0\n"
    "v_cvt_pk_bf16_f32 v142, v92, v93\n"
    "v_exp_f32_e32 v94, v94\n"
    "v_exp_f32_e32 v95, v95\n"
    "s_nop 0\n"
    "v_cvt_pk_bf16_f32 v143, v94, v95\n"
    "s_lshl_b32 s83, s36, 9\n"
    "s_add_u32 s83, s83, 0x20000\n"
    "v_add_u32_e32 v233, s83, v252\n"
    "v_add_u32_e32 v235, s83, v251\n"
    "v_mfma_f32_16x16x32_bf16 v[208:211], v[224:227], v[128:131], 0\n"
    "v_exp_f32_e64 v96, v96\n"
    "v_mfma_f32_16x16x32_bf16 v[212:215], v[224:227], v[132:135], 0\n"
    "v_mfma_f32_16x16x32_bf16 v[216:219], v[224:227], v[136:139], 0\n"
    "v_exp_f32_e64 v97, v97\n"
    "v_mfma_f32_16x16x32_bf16 v[220:223], v[224:227], v[140:143], 0\n"
    "s_waitcnt lgkmcnt(14)\n"
    "v_cvt_pk_bf16_f32 v144, v96, v97\n"
    "v_mfma_f32_16x16x32_bf16 a[0:3], a[224:227], v[128:131], 0\n"
    "v_exp_f32_e32 v98, v98\n"
    "v_mfma_f32_16x16x32_bf16 a[4:7], a[224:227], v[132:135], 0\n"
    "s_waitcnt vmcnt(14)\n"
    "s_barrier\n"
    "v_mfma_f32_16x16x32_bf16 a[8:11], a[224:227], v[136:139], 0\n"
    "v_exp_f32_e64 v99, v99\n"
    "v_mfma_f32_16x16x32_bf16 a[12:15], a[224:227], v[140:143], 0\n"
    "s_waitcnt lgkmcnt(12)\n"
    "ds_read_b64_tr_b16 v[164:165], v233 offset:0\n"
    "v_cvt_pk_bf16_f32 v145, v98, v99\n"
    "v_mfma_f32_16x16x32_bf16 a[28:31], a[228:231], v[140:143], 0\n"
    "s_add_u32 m0, s68, 0x4000\n"
    "ds_read_b64 a[192:193], v246 offset:16384\n"
    "ds_read_b64 a[194:195], v245 offset:16384\n"
    "v_mfma_f32_16x16x32_bf16 a[24:27], a[228:231], v[136:139], 0\n"
    "v_exp_f32_e32 v100, v100\n"
    "v_mfma_f32_16x16x32_bf16 a[20:23], a[228:231], v[132:135], 0\n"
    "buffer_load_dwordx4 v253, s[40:43], s62 offen lds\n"
    "ds_read_b64_tr_b16 v[166:167], v233 offset:4096\n"
    "v_mfma_f32_16x16x32_bf16 a[16:19], a[228:231], v[128:131], 0\n"
    "s_waitcnt lgkmcnt(14)\n"
    "ds_read_b64 a[196:197], v246 offset:16896\n"
    "ds_read_b64_tr_b16 v[168:169], v235 offset:0\n"
    "v_mfma_f32_16x16x32_bf16 a[32:35], a[232:235], v[128:131], 0\n"
    "v_exp_f32_e32 v101, v101\n"
    "v_mfma_f32_16x16x32_bf16 a[36:39], a[232:235], v[132:135], 0\n"
    "s_add_u32 m0, s68, 0x4400\n"
    "ds_read_b64 a[198:199], v245 offset:16896\n"
    "v_cvt_pk_bf16_f32 v146, v100, v101\n"
    "v_mfma_f32_16x16x32_bf16 a[40:43], a[232:235], v[136:139], 0\n"
    "v_exp_f32_e64 v102, v102\n"
    "v_mfma_f32_16x16x32_bf16 a[44:47], a[232:235], v[140:143], 0\n"
    "s_waitcnt lgkmcnt(15)\n"
    "ds_read_b64_tr_b16 v[170:171], v235 offset:4096\n"
    "ds_read_b64 a[200:201], v246 offset:17408\n"
    "v_mfma_f32_16x16x32_bf16 a[60:63], a[236:239], v[140:143], 0\n"
    "v_exp_f32_e32 v103, v103\n"
    "v_mfma_f32_16x16x32_bf16 a[56:59], a[236:239], v[136:139], 0\n"
    "buffer_load_dwordx4 v253, s[40:43], s63 offen lds\n"
    "v_cvt_pk_bf16_f32 v147, v102, v103\n"
    "v_mfma_f32_16x16x32_bf16 a[52:55], a[236:239], v[132:135], 0\n"
    "s_waitcnt lgkmcnt(15)\n"
    "ds_read_b64 a[202:203], v245 offset:17408\n"
    "ds_read_b64 a[204:205], v246 offset:17920\n"
    "v_mfma_f32_16x16x32_bf16 a[48:51], a[236:239], v[128:131], 0\n"
    "s_add_u32 s62, s62, s66\n"
    "v_exp_f32_e64 v104, v104\n"
    "v_mfma_f32_16x16x32_bf16 a[64:67], a[240:243], v[128:131], 0\n"
    "s_add_u32 s63, s63, s66\n"
    "ds_read_b64 a[206:207], v245 offset:17920\n"
    "ds_read_b64 a[208:209], v246 offset:20480\n"
    "v_mfma_f32_16x16x32_bf16 a[68:71], a[240:243], v[132:135], 0\n"
    "v_exp_f32_e32 v105, v105\n"
    "v_mfma_f32_16x16x32_bf16 a[72:75], a[240:243], v[136:139], 0\n"
    "s_waitcnt lgkmcnt(15)\n"
    "v_cvt_pk_bf16_f32 v148, v104, v105\n"
    "v_readlane_b32 s82, v228, 32\n"
    "v_mfma_f32_16x16x32_bf16 a[76:79], a[240:243], v[140:143], 0\n"
    "ds_read_b64 a[210:211], v245 offset:20480\n"
    "v_readlane_b32 s83, v228, 40\n"
    "v_mfma_f32_16x16x32_bf16 a[92:95], a[244:247], v[140:143], 0\n"
    "v_exp_f32_e32 v106, v106\n"
    "v_mfma_f32_16x16x32_bf16 a[88:91], a[244:247], v[136:139], 0\n"
    "s_waitcnt lgkmcnt(10)\n"
    "buffer_store_dwordx4 v[164:167], v255, s[48:51], s82 offen sc1\n"
    "ds_read_b64 a[212:213], v246 offset:20992\n"
    "v_mfma_f32_16x16x32_bf16 a[84:87], a[244:247], v[132:135], 0\n"
    "v_exp_f32_e32 v107, v107\n"
    "v_mfma_f32_16x16x32_bf16 a[80:83], a[244:247], v[128:131], 0\n"
    "ds_read_b64 a[214:215], v245 offset:20992\n"
    "v_cvt_pk_bf16_f32 v149, v106, v107\n"
    "v_mfma_f32_16x16x32_bf16 a[96:99], a[248:251], v[128:131], 0\n"
    "v_exp_f32_e64 v108, v108\n"
    "v_mfma_f32_16x16x32_bf16 a[100:103], a[248:251], v[132:135], 0\n"
    "s_waitcnt lgkmcnt(8)\n"
    "ds_read_b64 a[216:217], v246 offset:21504\n"
    "buffer_store_dwordx4 v[168:171], v255, s[48:51], s83 offen sc1\n"
    "v_mfma_f32_16x16x32_bf16 a[104:107], a[248:251], v[136:139], 0\n"
    "v_exp_f32_e32 v109, v109\n"
    "v_mfma_f32_16x16x32_bf16 a[108:111], a[248:251], v[140:143], 0\n"
    "ds_read_b64 a[218:219], v245 offset:21504\n"
    "v_cvt_pk_bf16_f32 v150, v108, v109\n"
    "v_mfma_f32_16x16x32_bf16 a[124:127], a[252:255], v[140:143], 0\n"
    "v_exp_f32_e64 v110, v110\n"
    "v_mfma_f32_16x16x32_bf16 a[120:123], a[252:255], v[136:139], 0\n"
    "ds_read_b64 a[220:221], v246 offset:22016\n"
    "ds_read_b64 a[222:223], v245 offset:22016\n"
    "v_mfma_f32_16x16x32_bf16 a[116:119], a[252:255], v[132:135], 0\n"
    "v_exp_f32_e64 v111, v111\n"
    "v_mfma_f32_16x16x32_bf16 a[112:115], a[252:255], v[128:131], 0\n"
    "v_cvt_pk_bf16_f32 v151, v110, v111\n"
    "v_lshlrev_b32_e64 v238, 22, v255\n"
    "v_mfma_f32_16x16x32_bf16 v[64:67], a[192:195], v[0:3], v[192:195]\n"
    "v_exp_f32_e64 v112, v112\n"
    "v_mfma_f32_16x16x32_bf16 v[64:67], a[196:199], v[4:7], v[64:67]\n"
    "s_waitcnt lgkmcnt(8)\n"
    "buffer_store_dword v230, v238, s[48:51], s79 offen sc1\n"
    "ds_read_b64_tr_b16 a[224:225], v248 offset:24576\n"
    "v_mfma_f32_16x16x32_bf16 v[64:67], a[200:203], v[8:11], v[64:67]\n"
    "s_add_u32 m0, s68, 0x6000\n"
    "v_exp_f32_e32 v113, v113\n"
    "v_mfma_f32_16x16x32_bf16 v[64:67], a[204:207], v[12:15], v[64:67]\n"
    "s_waitcnt lgkmcnt(7)\n"
    "ds_read_b64_tr_b16 a[226:227], v248 offset:28672\n"
    "v_cvt_pk_bf16_f32 v152, v112, v113\n"
    "v_mfma_f32_16x16x32_bf16 v[68:71], a[208:211], v[0:3], v[192:195]\n"
    "s_waitcnt lgkmcnt(6)\n"
    "v_exp_f32_e64 v114, v114\n"
    "v_mfma_f32_16x16x32_bf16 v[68:71], a[212:215], v[4:7], v[68:71]\n"
    "s_waitcnt lgkmcnt(4)\n"
    "v_exp_f32_e32 v115, v115\n"
    "v_mfma_f32_16x16x32_bf16 v[68:71], a[216:219], v[8:11], v[68:71]\n"
    "s_waitcnt lgkmcnt(2)\n"
    "ds_read_b64_tr_b16 a[228:229], v247 offset:24576\n"
    "v_cvt_pk_bf16_f32 v153, v114, v115\n"
    "PG4_%=:\n"
    "s_cbranch_execz PG8_%=\n"
    "v_mfma_f32_16x16x32_bf16 v[68:71], a[220:223], v[12:15], v[68:71]\n"
    "buffer_load_dwordx4 v253, s[44:47], s64 offen lds\n"
    "ds_read_b64_tr_b16 a[230:231], v247 offset:28672\n"
    "v_mfma_f32_16x16x32_bf16 v[72:75], a[192:195], v[16:19], v[196:199]\n"
    "s_add_u32 m0, s68, 0x6400\n"
    "v_exp_f32_e32 v116, v116\n"
    "v_mfma_f32_16x16x32_bf16 v[72:75], a[196:199], v[20:23], v[72:75]\n"
    "v_exp_f32_e64 v117, v117\n"
    "v_mfma_f32_16x16x32_bf16 v[72:75], a[200:203], v[24:27], v[72:75]\n"
    "ds_read_b64_tr_b16 a[232:233], v248 offset:25088\n"
    "v_cvt_pk_bf16_f32 v154, v116, v117\n"
    "v_mfma_f32_16x16x32_bf16 v[72:75], a[204:207], v[28:31], v[72:75]\n"
    "v_exp_f32_e64 v118, v118\n"
    "v_mfma_f32_16x16x32_bf16 v[76:79], a[208:211], v[16:19], v[196:199]\n"
    "ds_read_b64_tr_b16 a[234:235], v248 offset:29184\n"
    "ds_read_b64_tr_b16 a[236:237], v247 offset:25088\n"
    "v_mfma_f32_16x16x32_bf16 v[76:79], a[212:215], v[20:23], v[76:79]\n"
    "buffer_load_dwordx4 v253, s[44:47], s65 offen lds\n"
    "ds_read_b64_tr_b16 a[238:239], v247 offset:29184\n"
    "v_mfma_f32_16x16x32_bf16 v[76:79], a[216:219], v[24:27], v[76:79]\n"
    "s_add_u32 s64, s64, s67\n"
    "v_exp_f32_e32 v119, v119\n"
    "v_mfma_f32_16x16x32_bf16 v[76:79], a[220:223], v[28:31], v[76:79]\n"
    "s_add_u32 s65, s65, s67\n"
    "ds_read_b64_tr_b16 a[240:241], v248 offset:25600\n"
    "v_cvt_pk_bf16_f32 v155, v118, v119\n"
    "v_mfma_f32_16x16x32_bf16 v[80:83], a[192:195], v[32:35], v[200:203]\n"
    "v_exp_f32_e32 v120, v120\n"
    "v_mfma_f32_16x16x32_bf16 v[80:83], a[196:199], v[36:39], v[80:83]\n"
    "v_exp_f32_e64 v121, v121\n"
    "v_mfma_f32_16x16x32_bf16 v[80:83], a[200:203], v[40:43], v[80:83]\n"
    "v_readlane_b32 s96, v228, 2\n"
    "v_readlane_b32 s97, v228, 10\n"
    "v_mfma_f32_16x16x32_bf16 v[80:83], a[204:207], v[44:47], v[80:83]\n"
    "v_readlane_b32 s98, v228, 18\n"
    "v_readlane_b32 s82, v228, 26\n"
    "v_mfma_f32_16x16x32_bf16 v[84:87], a[208:211], v[32:35], v[200:203]\n"
    "v_and_b32_e32 v229, 0xff, v253\n"
    "v_mad_u32_u24 v229, v240, s82, v229\n"
    "v_mfma_f32_16x16x32_bf16 v[84:87], a[212:215], v[36:39], v[84:87]\n"
    "s_add_u32 m0, s68, 0x24000\n"
    "ds_read_b64_tr_b16 a[242:243], v248 offset:29696\n"
    "v_cvt_pk_bf16_f32 v156, v120, v121\n"
    "v_mfma_f32_16x16x32_bf16 v[84:87], a[216:219], v[40:43], v[84:87]\n"
    "buffer_load_dwordx4 v229, s[96:99], 0 offen lds\n"
    "ds_read_b64_tr_b16 a[244:245], v247 offset:25600\n"
    "v_mfma_f32_16x16x32_bf16 v[84:87], a[220:223], v[44:47], v[84:87]\n"
    "s_add_u32 m0, m0, 0x380\n"
    "v_exp_f32_e64 v122, v122\n"
    "v_mfma_f32_16x16x32_bf16 v[88:91], a[192:195], v[48:51], v[204:207]\n"
    "ds_read_b64_tr_b16 a[246:247], v247 offset:29696\n"
    "ds_read_b64_tr_b16 a[248:249], v248 offset:26112\n"
    "v_mfma_f32_16x16x32_bf16 v[88:91], a[196:199], v[52:55], v[88:91]\n"
    "v_exp_f32_e64 v123, v123\n"
    "v_mfma_f32_16x16x32_bf16 v[88:91], a[200:203], v[56:59], v[88:91]\n"
    "ds_read_b64_tr_b16 a[250:251], v248 offset:30208\n"
    "v_cvt_pk_bf16_f32 v157, v122, v123\n"
    "v_mfma_f32_16x16x32_bf16 v[88:91], a[204:207], v[60:63], v[88:91]\n"
    "v_exp_f32_e64 v124, v124\n"
    "v_mfma_f32_16x16x32_bf16 v[92:95], a[208:211], v[48:51], v[204:207]\n"
    "buffer_load_dwordx4 v229, s[96:99], 0 offen offset:128 lds\n"
    "ds_read_b64_tr_b16 a[252:253], v247 offset:26112\n"
    "v_mfma_f32_16x16x32_bf16 v[92:95], a[212:215], v[52:55], v[92:95]\n"
    "s_lshl_b32 s83, s36, 9\n"
    "v_exp_f32_e32 v125, v125\n"
    "v_mfma_f32_16x16x32_bf16 v[92:95], a[216:219], v[56:59], v[92:95]\n"
    "s_add_u32 s83, s83, 0x22000\n"
    "ds_read_b64_tr_b16 a[254:255], v247 offset:30208\n"
    "v_cvt_pk_bf16_f32 v158, v124, v125\n"
    "v_mfma_f32_16x16x32_bf16 v[92:95], a[220:223], v[60:63], v[92:95]\n"
    "v_exp_f32_e32 v126, v126\n"
    "v_exp_f32_e32 v127, v127\n"
    "s_nop 0\n"
    "v_cvt_pk_bf16_f32 v159, v126, v127\n"
    "v_add_u32_e32 v233, s83, v252\n"
    "v_add_u32_e32 v235, s83, v251\n"
    "s_waitcnt vmcnt(13)\n"
    "s_mov_b32 s70, 0x18000\n"
    "s_mov_b32 s71, 5\n"
    "s_mov_b32 s69, 0x40000000\n"
    "s_mov_b32 s85, 0x6000\n"
    "s_mov_b32 s75, 0x22000\n"
    "s_mov_b32 s38, 0x40000000\n"
    "s_mov_b32 s39, 0x40000000\n"
    ".p2align 6\n"
    "A_LOOP%=:\n"
    "v_mfma_f32_16x16x32_bf16 v[208:211], v[224:227], v[144:147], v[208:211]\n"
    "s_add_u32 s60, s70, 0x10000\n"
    "v_exp_f32_e64 v64, v64\n"
    "v_mfma_f32_16x16x32_bf16 v[212:215], v[224:227], v[148:151], v[212:215]\n"
    "v_add_u32_e64 v246, s70, v250\n"
    "v_mfma_f32_16x16x32_bf16 v[216:219], v[224:227], v[152:155], v[216:219]\n"
    "s_and_b32 s60, s60, 0x1ffff\n"
    "v_exp_f32_e64 v65, v65\n"
    "v_mfma_f32_16x16x32_bf16 v[220:223], v[224:227], v[156:159], v[220:223]\n"
    "s_waitcnt lgkmcnt(14)\n"
    "v_cvt_pk_bf16_f32 v128, v64, v65\n"
    "v_add_u32_e32 v245, s70, v249\n"
    "v_mfma_f32_16x16x32_bf16 a[0:3], a[224:227], v[144:147], a[0:3]\n"
    "s_add_u32 s60, s60, s68\n"
    "v_exp_f32_e32 v66, v66\n"
    "v_mfma_f32_16x16x32_bf16 a[4:7], a[224:227], v[148:151], a[4:7]\n"
    "s_waitcnt vmcnt(11)\n"
    "s_barrier\n"
    "v_mfma_f32_16x16x32_bf16 a[8:11], a[224:227], v[152:155], a[8:11]\n"
    "v_exp_f32_e64 v67, v67\n"
    "v_mfma_f32_16x16x32_bf16 a[12:15], a[224:227], v[156:159], a[12:15]\n"
    "s_waitcnt lgkmcnt(12)\n"
    "buffer_store_dword v231, v241, s[48:51], s38 offen sc1\n"
    "v_cvt_pk_bf16_f32 v129, v66, v67\n"
    "v_mfma_f32_16x16x32_bf16 a[28:31], a[228:231], v[156:159], a[28:31]\n"
    "ds_read_b64 a[192:193], v246 offset:0\n"
    "ds_read_b64 a[194:195], v245 offset:0\n"
    "v_mfma_f32_16x16x32_bf16 a[24:27], a[228:231], v[152:155], a[24:27]\n"
    "v_exp_f32_e32 v68, v68\n"
    "v_mfma_f32_16x16x32_bf16 a[20:23], a[228:231], v[148:151], a[20:23]\n"
    "ds_read_b64_tr_b16 v[164:165], v233 offset:0\n"
    "ds_read_b64 a[196:197], v246 offset:512\n"
    "v_mfma_f32_16x16x32_bf16 a[16:19], a[228:231], v[144:147], a[16:19]\n"
    "s_waitcnt lgkmcnt(14)\n"
    "v_exp_f32_e32 v69, v69\n"
    "v_mfma_f32_16x16x32_bf16 a[32:35], a[232:235], v[144:147], a[32:35]\n"
    "ds_read_b64 a[198:199], v245 offset:512\n"
    "v_cvt_pk_bf16_f32 v130, v68, v69\n"
    "v_mfma_f32_16x16x32_bf16 a[36:39], a[232:235], v[148:151], a[36:39]\n"
    "ds_read_b64_tr_b16 v[166:167], v233 offset:4096\n"
    "ds_read_b64 a[200:201], v246 offset:1024\n"
    "v_mfma_f32_16x16x32_bf16 a[40:43], a[232:235], v[152:155], a[40:43]\n"
    "v_exp_f32_e64 v70, v70\n"
    "v_mfma_f32_16x16x32_bf16 a[44:47], a[232:235], v[156:159], a[44:47]\n"
    "s_waitcnt lgkmcnt(15)\n"
    "v_exp_f32_e32 v71, v71\n"
    "v_mfma_f32_16x16x32_bf16 a[60:63], a[236:239], v[156:159], a[60:63]\n"
    "ds_read_b64_tr_b16 v[168:169], v235 offset:0\n"
    "v_cvt_pk_bf16_f32 v131, v70, v71\n"
    "v_mfma_f32_16x16x32_bf16 a[56:59], a[236:239], v[152:155], a[56:59]\n"
    "ds_read_b64 a[202:203], v245 offset:1024\n"
    "ds_read_b64 a[204:205], v246 offset:1536\n"
    "v_mfma_f32_16x16x32_bf16 a[52:55], a[236:239], v[148:151], a[52:55]\n"
    "v_exp_f32_e64 v72, v72\n"
    "v_mfma_f32_16x16x32_bf16 a[48:51], a[236:239], v[144:147], a[48:51]\n"
    "s_waitcnt lgkmcnt(15)\n"
    "ds_read_b64_tr_b16 v[170:171], v235 offset:4096\n"
    "ds_read_b64 a[206:207], v245 offset:1536\n"
    "v_mfma_f32_16x16x32_bf16 a[64:67], a[240:243], v[144:147], a[64:67]\n"
    "v_exp_f32_e32 v73, v73\n"
    "v_mfma_f32_16x16x32_bf16 a[68:71], a[240:243], v[148:151], a[68:71]\n"
    "ds_read_b64 a[208:209], v246 offset:4096\n"
    "v_cvt_pk_bf16_f32 v132, v72, v73\n"
    "v_mfma_f32_16x16x32_bf16 a[72:75], a[240:243], v[152:155], a[72:75]\n"
    "s_waitcnt lgkmcnt(15)\n"
    "ds_read_b64 a[210:211], v245 offset:4096\n"
    "v_readlane_b32 s82, v228, 33\n"
    "v_mfma_f32_16x16x32_bf16 a[76:79], a[240:243], v[156:159], a[76:79]\n"
    "v_exp_f32_e32 v74, v74\n"
    "v_mfma_f32_16x16x32_bf16 a[92:95], a[244:247], v[156:159], a[92:95]\n"
    "v_exp_f32_e64 v75, v75\n"
    "v_mfma_f32_16x16x32_bf16 a[88:91], a[244:247], v[152:155], a[88:91]\n"
    "v_readlane_b32 s83, v228, 41\n"
    "v_cvt_pk_bf16_f32 v133, v74, v75\n"
    "v_mfma_f32_16x16x32_bf16 a[84:87], a[244:247], v[148:151], a[84:87]\n"
    "ds_read_b64 a[212:213], v246 offset:4608\n"
    "ds_read_b64 a[214:215], v245 offset:4608\n"
    "v_mfma_f32_16x16x32_bf16 a[80:83], a[244:247], v[144:147], a[80:83]\n"
    "v_exp_f32_e64 v76, v76\n"
    "v_mfma_f32_16x16x32_bf16 a[96:99], a[248:251], v[144:147], a[96:99]\n"
    "s_waitcnt lgkmcnt(10)\n"
    "buffer_store_dwordx4 v[164:167], v255, s[48:51], s82 offen sc1\n"
    "ds_read_b64 a[216:217], v246 offset:5120\n"
    "v_mfma_f32_16x16x32_bf16 a[100:103], a[248:251], v[148:151], a[100:103]\n"
    "v_exp_f32_e32 v77, v77\n"
    "v_mfma_f32_16x16x32_bf16 a[104:107], a[248:251], v[152:155], a[104:107]\n"
    "ds_read_b64 a[218:219], v245 offset:5120\n"
    "v_cvt_pk_bf16_f32 v134, v76, v77\n"
    "v_mfma_f32_16x16x32_bf16 a[108:111], a[248:251], v[156:159], a[108:111]\n"
    "s_waitcnt lgkmcnt(7)\n"
    "buffer_store_dwordx4 v[168:171], v255, s[48:51], s83 offen sc1\n"
    "ds_read_b64 a[220:221], v246 offset:5632\n"
    "v_mfma_f32_16x16x32_bf16 a[124:127], a[252:255], v[156:159], a[124:127]\n"
    "v_exp_f32_e32 v78, v78\n"
    "v_mfma_f32_16x16x32_bf16 a[120:123], a[252:255], v[152:155], a[120:123]\n"
    "v_exp_f32_e64 v79, v79\n"
    "v_mfma_f32_16x16x32_bf16 a[116:119], a[252:255], v[148:151], a[116:119]\n"
    "s_add_u32 m0, s60, 0\n"
    "v_cvt_pk_bf16_f32 v135, v78, v79\n"
    "v_add_u32_e32 v248, s70, v252\n"
    "v_mfma_f32_16x16x32_bf16 a[112:115], a[252:255], v[144:147], a[112:115]\n"
    "ds_read_b64 a[222:223], v245 offset:5632\n"
    "v_add_u32_e64 v247, s70, v251\n"
    "v_mfma_f32_16x16x32_bf16 v[96:99], a[192:195], v[0:3], v[192:195]\n"
    "v_exp_f32_e64 v80, v80\n"
    "v_mfma_f32_16x16x32_bf16 v[96:99], a[196:199], v[4:7], v[96:99]\n"
    "v_exp_f32_e64 v81, v81\n"
    "v_mfma_f32_16x16x32_bf16 v[96:99], a[200:203], v[8:11], v[96:99]\n"
    "s_waitcnt lgkmcnt(6)\n"
    "buffer_load_dwordx4 v253, s[40:43], s62 offen lds\n"
    "v_cvt_pk_bf16_f32 v136, v80, v81\n"
    "v_mfma_f32_16x16x32_bf16 v[96:99], a[204:207], v[12:15], v[96:99]\n"
    "s_add_u32 m0, s60, 0x400\n"
    "v_exp_f32_e32 v82, v82\n"
    "v_mfma_f32_16x16x32_bf16 v[100:103], a[208:211], v[0:3], v[192:195]\n"
    "s_waitcnt lgkmcnt(4)\n"
    "ds_read_b64_tr_b16 a[224:225], v248 offset:8192\n"
    "ds_read_b64_tr_b16 a[226:227], v248 offset:12288\n"
    "v_mfma_f32_16x16x32_bf16 v[100:103], a[212:215], v[4:7], v[100:103]\n"
    "s_waitcnt lgkmcnt(4)\n"
    "v_exp_f32_e64 v83, v83\n"
    "v_mfma_f32_16x16x32_bf16 v[100:103], a[216:219], v[8:11], v[100:103]\n"
    "s_waitcnt lgkmcnt(2)\n"
    "ds_read_b64_tr_b16 a[228:229], v247 offset:8192\n"
    "v_cvt_pk_bf16_f32 v137, v82, v83\n"
    "v_mfma_f32_16x16x32_bf16 v[100:103], a[220:223], v[12:15], v[100:103]\n"
    "v_exp_f32_e32 v84, v84\n"
    "v_mfma_f32_16x16x32_bf16 v[104:107], a[192:195], v[16:19], v[196:199]\n"
    "ds_read_b64_tr_b16 a[230:231], v247 offset:12288\n"
    "buffer_load_dwordx4 v253, s[40:43], s63 offen lds\n"
    "v_mfma_f32_16x16x32_bf16 v[104:107], a[196:199], v[20:23], v[104:107]\n"
    "s_add_u32 s62, s62, s66\n"
    "v_exp_f32_e32 v85, v85\n"
    "v_mfma_f32_16x16x32_bf16 v[104:107], a[200:203], v[24:27], v[104:107]\n"
    "s_add_u32 s63, s63, s66\n"
    "ds_read_b64_tr_b16 a[232:233], v248 offset:8704\n"
    "v_cvt_pk_bf16_f32 v138, v84, v85\n"
    "v_mfma_f32_16x16x32_bf16 v[104:107], a[204:207], v[28:31], v[104:107]\n"
    "v_exp_f32_e32 v86, v86\n"
    "v_mfma_f32_16x16x32_bf16 v[108:111], a[208:211], v[16:19], v[196:199]\n"
    "ds_read_b64_tr_b16 a[234:235], v248 offset:12800\n"
    "ds_read_b64_tr_b16 a[236:237], v247 offset:8704\n"
    "v_mfma_f32_16x16x32_bf16 v[108:111], a[212:215], v[20:23], v[108:111]\n"
    "s_add_u32 m0, s60, 0x2000\n"
    "v_exp_f32_e64 v87, v87\n"
    "v_mfma_f32_16x16x32_bf16 v[108:111], a[216:219], v[24:27], v[108:111]\n"
    "ds_read_b64_tr_b16 a[238:239], v247 offset:12800\n"
    "v_cvt_pk_bf16_f32 v139, v86, v87\n"
    "v_mfma_f32_16x16x32_bf16 v[108:111], a[220:223], v[28:31], v[108:111]\n"
    "v_exp_f32_e64 v88, v88\n"
    "v_mfma_f32_16x16x32_bf16 v[112:115], a[192:195], v[32:35], v[200:203]\n"
    "ds_read_b64_tr_b16 a[240:241], v248 offset:9216\n"
    "ds_read_b64_tr_b16 a[242:243], v248 offset:13312\n"
    "v_mfma_f32_16x16x32_bf16 v[112:115], a[196:199], v[36:39], v[112:115]\n"
    "v_exp_f32_e64 v89, v89\n"
    "v_mfma_f32_16x16x32_bf16 v[112:115], a[200:203], v[40:43], v[112:115]\n"
    "buffer_load_dwordx4 v253, s[44:47], s64 offen lds\n"
    "v_cvt_pk_bf16_f32 v140, v88, v89\n"
    "v_mfma_f32_16x16x32_bf16 v[112:115], a[204:207], v[44:47], v[112:115]\n"
    "s_add_u32 m0, s60, 0x2400\n"
    "v_exp_f32_e64 v90, v90\n"
    "v_mfma_f32_16x16x32_bf16 v[116:119], a[208:211], v[32:35], v[200:203]\n"
    "ds_read_b64_tr_b16 a[244:245], v247 offset:9216\n"
    "ds_read_b64_tr_b16 a[246:247], v247 offset:13312\n"
    "v_mfma_f32_16x16x32_bf16 v[116:119], a[212:215], v[36:39], v[116:119]\n"
    "v_exp_f32_e64 v91, v91\n"
    "v_mfma_f32_16x16x32_bf16 v[116:119], a[216:219], v[40:43], v[116:119]\n"
    "ds_read_b64_tr_b16 a[248:249], v248 offset:9728\n"
    "v_cvt_pk_bf16_f32 v141, v90, v91\n"
    "v_mfma_f32_16x16x32_bf16 v[116:119], a[220:223], v[44:47], v[116:119]\n"
    "v_exp_f32_e64 v92, v92\n"
    "v_mfma_f32_16x16x32_bf16 v[120:123], a[192:195], v[48:51], v[204:207]\n"
    "buffer_load_dwordx4 v253, s[44:47], s65 offen lds\n"
    "ds_read_b64_tr_b16 a[250:251], v248 offset:13824\n"
    "v_mfma_f32_16x16x32_bf16 v[120:123], a[196:199], v[52:55], v[120:123]\n"
    "s_add_u32 s64, s64, s67\n"
    "v_exp_f32_e32 v93, v93\n"
    "v_mfma_f32_16x16x32_bf16 v[120:123], a[200:203], v[56:59], v[120:123]\n"
    "s_add_u32 s65, s65, s67\n"
    "ds_read_b64_tr_b16 a[252:253], v247 offset:9728\n"
    "v_cvt_pk_bf16_f32 v142, v92, v93\n"
    "v_mfma_f32_16x16x32_bf16 v[120:123], a[204:207], v[60:63], v[120:123]\n"
    "v_exp_f32_e32 v94, v94\n"
    "v_mfma_f32_16x16x32_bf16 v[124:127], a[208:211], v[48:51], v[204:207]\n"
    "ds_read_b64_tr_b16 a[254:255], v247 offset:13824\n"
    "v_mfma_f32_16x16x32_bf16 v[124:127], a[212:215], v[52:55], v[124:127]\n"
    "v_exp_f32_e64 v95, v95\n"
    "v_mfma_f32_16x16x32_bf16 v[124:127], a[216:219], v[56:59], v[124:127]\n"
    "v_cvt_pk_bf16_f32 v143, v94, v95\n"
    "v_mfma_f32_16x16x32_bf16 v[124:127], a[220:223], v[60:63], v[124:127]\n"
    "v_mfma_f32_16x16x32_bf16 v[208:211], v[224:227], v[128:131], v[208:211]\n"
    "v_exp_f32_e64 v96, v96\n"
    "v_mfma_f32_16x16x32_bf16 v[212:215], v[224:227], v[132:135], v[212:215]\n"
    "s_waitcnt lgkmcnt(14)\n"
    "s_add_u32 s70, s70, 0x8000\n"
    "buffer_load_dwordx4 v[160:163], v255, s[48:51], s39 offen sc1\n"
    "v_mfma_f32_16x16x32_bf16 v[216:219], v[224:227], v[136:139], v[216:219]\n"
    "s_add_u32 m0, s60, 0x4000\n"
    "v_exp_f32_e32 v97, v97\n"
    "v_mfma_f32_16x16x32_bf16 v[220:223], v[224:227], v[140:143], v[220:223]\n"
    "s_and_b32 s70, s70, 0x1ffff\n"
    "buffer_load_dwordx4 v253, s[40:43], s62 offen lds\n"
    "v_cvt_pk_bf16_f32 v144, v96, v97\n"
    "v_mfma_f32_16x16x32_bf16 a[0:3], a[224:227], v[128:131], a[0:3]\n"
    "v_exp_f32_e64 v98, v98\n"
    "v_mfma_f32_16x16x32_bf16 a[4:7], a[224:227], v[132:135], a[4:7]\n"
    "s_sub_u32 s71, s71, 1\n"
    "v_mfma_f32_16x16x32_bf16 a[8:11], a[224:227], v[136:139], a[8:11]\n"
    "s_add_u32 m0, s60, 0x4400\n"
    "v_exp_f32_e32 v99, v99\n"
    "v_mfma_f32_16x16x32_bf16 a[12:15], a[224:227], v[140:143], a[12:15]\n"
    "s_waitcnt lgkmcnt(12)\n"
    "ds_read_b64 a[192:193], v246 offset:16384\n"
    "v_cvt_pk_bf16_f32 v145, v98, v99\n"
    "v_mfma_f32_16x16x32_bf16 a[28:31], a[228:231], v[140:143], a[28:31]\n"
    "ds_read_b64 a[194:195], v245 offset:16384\n"
    "buffer_load_dwordx4 v253, s[40:43], s63 offen lds\n"
    "v_mfma_f32_16x16x32_bf16 a[24:27], a[228:231], v[136:139], a[24:27]\n"
    "s_add_u32 s62, s62, s66\n"
    "v_exp_f32_e64 v100, v100\n"
    "v_mfma_f32_16x16x32_bf16 a[20:23], a[228:231], v[132:135], a[20:23]\n"
    "s_waitcnt lgkmcnt(12)\n"
    "ds_read_b64 a[196:197], v246 offset:16896\n"
    "v_mfma_f32_16x16x32_bf16 a[16:19], a[228:231], v[128:131], a[16:19]\n"
    "s_add_u32 s63, s63, s66\n"
    "v_exp_f32_e64 v101, v101\n"
    "v_mfma_f32_16x16x32_bf16 a[32:35], a[232:235], v[128:131], a[32:35]\n"
    "ds_read_b64 a[198:199], v245 offset:16896\n"
    "v_cvt_pk_bf16_f32 v146, v100, v101\n"
    "v_mfma_f32_16x16x32_bf16 a[36:39], a[232:235], v[132:135], a[36:39]\n"
    "s_add_u32 m0, s60, 0x6000\n"
    "v_exp_f32_e64 v102, v102\n"
    "v_mfma_f32_16x16x32_bf16 a[40:43], a[232:235], v[136:139], a[40:43]\n"
    "ds_read_b64 a[200:201], v246 offset:17408\n"
    "v_mfma_f32_16x16x32_bf16 a[44:47], a[232:235], v[140:143], a[44:47]\n"
    "s_waitcnt lgkmcnt(13)\n"
    "v_exp_f32_e32 v103, v103\n"
    "v_mfma_f32_16x16x32_bf16 a[60:63], a[236:239], v[140:143], a[60:63]\n"
    "ds_read_b64 a[202:203], v245 offset:17408\n"
    "v_cvt_pk_bf16_f32 v147, v102, v103\n"
    "v_mfma_f32_16x16x32_bf16 a[56:59], a[236:239], v[136:139], a[56:59]\n"
    "ds_read_b64 a[204:205], v246 offset:17920\n"
    "buffer_load_dwordx4 v253, s[44:47], s64 offen lds\n"
    "v_mfma_f32_16x16x32_bf16 a[52:55], a[236:239], v[132:135], a[52:55]\n"
    "s_add_u32 m0, s60, 0x6400\n"
    "v_exp_f32_e64 v104, v104\n"
    "v_mfma_f32_16x16x32_bf16 a[48:51], a[236:239], v[128:131], a[48:51]\n"
    "s_waitcnt lgkmcnt(13)\n"
    "ds_read_b64 a[206:207], v245 offset:17920\n"
    "ds_read_b64 a[208:209], v246 offset:20480\n"
    "v_mfma_f32_16x16x32_bf16 a[64:67], a[240:243], v[128:131], a[64:67]\n"
    "v_exp_f32_e32 v105, v105\n"
    "v_mfma_f32_16x16x32_bf16 a[68:71], a[240:243], v[132:135], a[68:71]\n"
    "s_waitcnt lgkmcnt(13)\n"
    "buffer_load_dwordx4 v253, s[44:47], s65 offen lds\n"
    "v_cvt_pk_bf16_f32 v148, v104, v105\n"
    "v_mfma_f32_16x16x32_bf16 a[72:75], a[240:243], v[136:139], a[72:75]\n"
    "s_add_u32 s64, s64, s67\n"
    "v_exp_f32_e64 v106, v106\n"
    "v_mfma_f32_16x16x32_bf16 a[76:79], a[240:243], v[140:143], a[76:79]\n"
    "s_add_u32 s65, s65, s67\n"
    "ds_read_b64 a[210:211], v245 offset:20480\n"
    "ds_read_b64 a[212:213], v246 offset:20992\n"
    "v_mfma_f32_16x16x32_bf16 a[92:95], a[244:247], v[140:143], a[92:95]\n"
    "v_exp_f32_e32 v107, v107\n"
    "v_mfma_f32_16x16x32_bf16 a[88:91], a[244:247], v[136:139], a[88:91]\n"
    "ds_read_b64 a[214:215], v245 offset:20992\n"
    "v_cvt_pk_bf16_f32 v149, v106, v107\n"
    "v_mfma_f32_16x16x32_bf16 a[84:87], a[244:247], v[132:135], a[84:87]\n"
    "v_exp_f32_e64 v108, v108\n"
    "v_mfma_f32_16x16x32_bf16 a[80:83], a[244:247], v[128:131], a[80:83]\n"
    "s_waitcnt lgkmcnt(14)\n"
    "ds_read_b64 a[216:217], v246 offset:21504\n"
    "v_readlane_b32 s96, v228, 3\n"
    "v_mfma_f32_16x16x32_bf16 a[96:99], a[248:251], v[128:131], a[96:99]\n"
    "v_exp_f32_e32 v109, v109\n"
    "v_mfma_f32_16x16x32_bf16 a[100:103], a[248:251], v[132:135], a[100:103]\n"
    "s_waitcnt lgkmcnt(13)\n"
    "v_cvt_pk_bf16_f32 v150, v108, v109\n"
    "v_readlane_b32 s97, v228, 11\n"
    "v_mfma_f32_16x16x32_bf16 a[104:107], a[248:251], v[136:139], a[104:107]\n"
    "ds_read_b64 a[218:219], v245 offset:21504\n"
    "v_readlane_b32 s98, v228, 19\n"
    "v_mfma_f32_16x16x32_bf16 a[108:111], a[248:251], v[140:143], a[108:111]\n"
    "v_exp_f32_e32 v110, v110\n"
    "v_mfma_f32_16x16x32_bf16 a[124:127], a[252:255], v[140:143], a[124:127]\n"
    "ds_read_b64 a[220:221], v246 offset:22016\n"
    "v_readlane_b32 s82, v228, 27\n"
    "v_mfma_f32_16x16x32_bf16 a[120:123], a[252:255], v[136:139], a[120:123]\n"
    "v_exp_f32_e64 v111, v111\n"
    "v_mfma_f32_16x16x32_bf16 a[116:119], a[252:255], v[132:135], a[116:119]\n"
    "ds_read_b64 a[222:223], v245 offset:22016\n"
    "v_cvt_pk_bf16_f32 v151, v110, v111\n"
    "v_mfma_f32_16x16x32_bf16 a[112:115], a[252:255], v[128:131], a[112:115]\n"
    "s_waitcnt lgkmcnt(14)\n"
    "v_exp_f32_e32 v112, v112\n"
    "v_mfma_f32_16x16x32_bf16 v[64:67], a[192:195], v[0:3], v[192:195]\n"
    "s_waitcnt lgkmcnt(12)\n"
    "v_and_b32_e32 v229, 0xff, v253\n"
    "v_mad_u32_u24 v229, v240, s82, v229\n"
    "v_mfma_f32_16x16x32_bf16 v[64:67], a[196:199], v[4:7], v[64:67]\n"
    "s_waitcnt lgkmcnt(10)\n"
    "v_exp_f32_e64 v113, v113\n"
    "v_mfma_f32_16x16x32_bf16 v[64:67], a[200:203], v[8:11], v[64:67]\n"
    "s_waitcnt lgkmcnt(4)\n"
    "v_cvt_pk_bf16_f32 v152, v112, v113\n"
    "v_mov_b32_dpp v228, v228 row_shl:1 row_mask:0xf bank_mask:0xf\n"
    "v_mfma_f32_16x16x32_bf16 v[64:67], a[204:207], v[12:15], v[64:67]\n"
    "s_xor_b32 s83, s75, 0x4000\n"
    "v_exp_f32_e32 v114, v114\n"
    "v_mfma_f32_16x16x32_bf16 v[68:71], a[208:211], v[0:3], v[192:195]\n"
    "s_add_u32 m0, s68, s83\n"
    "ds_read_b64_tr_b16 a[224:225], v248 offset:24576\n"
    "ds_read_b64_tr_b16 a[226:227], v248 offset:28672\n"
    "v_mfma_f32_16x16x32_bf16 v[68:71], a[212:215], v[4:7], v[68:71]\n"
    "s_waitcnt lgkmcnt(4)\n"
    "v_exp_f32_e64 v115, v115\n"
    "v_mfma_f32_16x16x32_bf16 v[68:71], a[216:219], v[8:11], v[68:71]\n"
    "s_waitcnt lgkmcnt(2)\n"
    "ds_read_b64_tr_b16 a[228:229], v247 offset:24576\n"
    "v_cvt_pk_bf16_f32 v153, v114, v115\n"
    "v_mfma_f32_16x16x32_bf16 v[68:71], a[220:223], v[12:15], v[68:71]\n"
    "v_exp_f32_e32 v116, v116\n"
    "v_mfma_f32_16x16x32_bf16 v[72:75], a[192:195], v[16:19], v[196:199]\n"
    "ds_read_b64_tr_b16 a[230:231], v247 offset:28672\n"
    "buffer_load_dwordx4 v229, s[96:99], 0 offen lds\n"
    "v_mfma_f32_16x16x32_bf16 v[72:75], a[196:199], v[20:23], v[72:75]\n"
    "s_add_u32 m0, m0, 0x380\n"
    "v_exp_f32_e64 v117, v117\n"
    "v_mfma_f32_16x16x32_bf16 v[72:75], a[200:203], v[24:27], v[72:75]\n"
    "ds_read_b64_tr_b16 a[232:233], v248 offset:25088\n"
    "v_cvt_pk_bf16_f32 v154, v116, v117\n"
    "v_mfma_f32_16x16x32_bf16 v[72:75], a[204:207], v[28:31], v[72:75]\n"
    "v_exp_f32_e64 v118, v118\n"
    "v_mfma_f32_16x16x32_bf16 v[76:79], a[208:211], v[16:19], v[196:199]\n"
    "ds_read_b64_tr_b16 a[234:235], v248 offset:29184\n"
    "ds_read_b64_tr_b16 a[236:237], v247 offset:25088\n"
    "v_mfma_f32_16x16x32_bf16 v[76:79], a[212:215], v[20:23], v[76:79]\n"
    "v_exp_f32_e64 v119, v119\n"
    "v_mfma_f32_16x16x32_bf16 v[76:79], a[216:219], v[24:27], v[76:79]\n"
    "buffer_load_dwordx4 v229, s[96:99], 0 offen offset:128 lds\n"
    "v_cvt_pk_bf16_f32 v155, v118, v119\n"
    "v_mfma_f32_16x16x32_bf16 v[76:79], a[220:223], v[28:31], v[76:79]\n"
    "s_add_u32 s75, s75, 0x2000\n"
    "ds_read_b64_tr_b16 a[238:239], v247 offset:29184\n"
    "ds_read_b64_tr_b16 a[240:241], v248 offset:25600\n"
    "v_mfma_f32_16x16x32_bf16 v[80:83], a[192:195], v[32:35], v[200:203]\n"
    "s_and_b32 s75, s75, 0x27fff\n"
    "v_exp_f32_e64 v120, v120\n"
    "v_mfma_f32_16x16x32_bf16 v[80:83], a[196:199], v[36:39], v[80:83]\n"
    "s_lshl_b32 s83, s36, 9\n"
    "v_exp_f32_e32 v121, v121\n"
    "v_mfma_f32_16x16x32_bf16 v[80:83], a[200:203], v[40:43], v[80:83]\n"
    "s_add_u32 s83, s83, s75\n"
    "ds_read_b64_tr_b16 a[242:243], v248 offset:29696\n"
    "v_cvt_pk_bf16_f32 v156, v120, v121\n"
    "v_mfma_f32_16x16x32_bf16 v[80:83], a[204:207], v[44:47], v[80:83]\n"
    "v_exp_f32_e32 v122, v122\n"
    "v_mfma_f32_16x16x32_bf16 v[84:87], a[208:211], v[32:35], v[200:203]\n"
    "ds_read_b64_tr_b16 a[244:245], v247 offset:25600\n"
    "ds_read_b64_tr_b16 a[246:247], v247 offset:29696\n"
    "v_mfma_f32_16x16x32_bf16 v[84:87], a[212:215], v[36:39], v[84:87]\n"
    "v_exp_f32_e64 v123, v123\n"
    "v_mfma_f32_16x16x32_bf16 v[84:87], a[216:219], v[40:43], v[84:87]\n"
    "ds_read_b64_tr_b16 a[248:249], v248 offset:26112\n"
    "v_cvt_pk_bf16_f32 v157, v122, v123\n"
    "v_mfma_f32_16x16x32_bf16 v[84:87], a[220:223], v[44:47], v[84:87]\n"
    "v_exp_f32_e64 v124, v124\n"
    "v_mfma_f32_16x16x32_bf16 v[88:91], a[192:195], v[48:51], v[204:207]\n"
    "ds_read_b64_tr_b16 a[250:251], v248 offset:30208\n"
    "v_add_u32_e64 v233, s83, v252\n"
    "v_mfma_f32_16x16x32_bf16 v[88:91], a[196:199], v[52:55], v[88:91]\n"
    "v_exp_f32_e64 v125, v125\n"
    "v_mfma_f32_16x16x32_bf16 v[88:91], a[200:203], v[56:59], v[88:91]\n"
    "v_add_u32_e64 v235, s83, v251\n"
    "v_cvt_pk_bf16_f32 v158, v124, v125\n"
    "v_mfma_f32_16x16x32_bf16 v[88:91], a[204:207], v[60:63], v[88:91]\n"
    "ds_read_b64_tr_b16 a[252:253], v247 offset:26112\n"
    "ds_read_b64_tr_b16 a[254:255], v247 offset:30208\n"
    "v_mfma_f32_16x16x32_bf16 v[92:95], a[208:211], v[48:51], v[204:207]\n"
    "s_cmp_eq_u32 s71, 3\n"
    "s_cselect_b32 s38, s84, s69\n"
    "v_exp_f32_e64 v126, v126\n"
    "v_mfma_f32_16x16x32_bf16 v[92:95], a[212:215], v[52:55], v[92:95]\n"
    "s_cmp_eq_u32 s71, 1\n"
    "s_cselect_b32 s39, s85, s69\n"
    "v_exp_f32_e64 v127, v127\n"
    "v_mfma_f32_16x16x32_bf16 v[92:95], a[216:219], v[56:59], v[92:95]\n"
    "s_cmp_lg_u32 s71, 0\n"
    "v_cvt_pk_bf16_f32 v159, v126, v127\n"
    "v_mfma_f32_16x16x32_bf16 v[92:95], a[220:223], v[60:63], v[92:95]\n"
    "s_cbranch_scc1 A_LOOP%=\n"
    "s_waitcnt vmcnt(6)\n"
    "v_xor_b32_e32 v161, v160, v161\n"
    "v_xor_b32_e32 v162, v160, v162\n"
    "v_xor_b32_e32 v163, v160, v163\n"
    "v_xor_b32_e64 v160, s78, v160\n"
    "v_mfma_f32_16x16x32_bf16 v[208:211], v[224:227], v[144:147], v[208:211]\n"
    "v_exp_f32_e64 v64, v64\n"
    "v_mfma_f32_16x16x32_bf16 v[212:215], v[224:227], v[148:151], v[212:215]\n"
    "v_or3_b32 v160, v160, v161, v162\n"
    "v_or_b32_e64 v160, v160, v163\n"
    "v_mfma_f32_16x16x32_bf16 v[216:219], v[224:227], v[152:155], v[216:219]\n"
    "v_exp_f32_e64 v65, v65\n"
    "v_mfma_f32_16x16x32_bf16 v[220:223], v[224:227], v[156:159], v[220:223]\n"
    "s_waitcnt lgkmcnt(14)\n"
    "v_cmp_eq_u32_e32 vcc, 0, v160\n"
    "v_cvt_pk_bf16_f32 v128, v64, v65\n"
    "v_mfma_f32_16x16x32_bf16 a[0:3], a[224:227], v[144:147], a[0:3]\n"
    "v_exp_f32_e64 v66, v66\n"
    "v_mfma_f32_16x16x32_bf16 a[4:7], a[224:227], v[148:151], a[4:7]\n"
    "s_cmp_eq_u64 vcc, -1\n"
    "s_cbranch_scc1 POK2_%=\n"
    "s_mov_b32 s61, 20000\n"
    "PSP2_%=:\n"
    "s_sleep 4\n"
    "buffer_load_dwordx4 v[160:163], v255, s[48:51], s85 offen sc1\n"
    "s_waitcnt vmcnt(0)\n"
    "v_xor_b32_e64 v161, v160, v161\n"
    "v_xor_b32_e64 v162, v160, v162\n"
    "v_xor_b32_e64 v163, v160, v163\n"
    "v_xor_b32_e64 v160, s78, v160\n"
    "v_or3_b32 v160, v160, v161, v162\n"
    "v_or_b32_e64 v160, v160, v163\n"
    "v_cmp_eq_u32_e32 vcc, 0, v160\n"
    "s_nop 1\n"
    "s_cmp_eq_u64 vcc, -1\n"
    "s_cbranch_scc1 POK2_%=\n"
    "s_sub_u32 s61, s61, 1\n"
    "s_cmp_lg_u32 s61, 0\n"
    "s_cbranch_scc1 PSP2_%=\n"
    "POK2_%=:\n"
    "s_add_u32 s82, s84, 0x1000\n"
    "s_waitcnt vmcnt(11) lgkmcnt(0)\n"
    "s_barrier\n"
    "v_mfma_f32_16x16x32_bf16 a[8:11], a[224:227], v[152:155], a[8:11]\n"
    "v_exp_f32_e32 v67, v67\n"
    "v_mfma_f32_16x16x32_bf16 a[12:15], a[224:227], v[156:159], a[12:15]\n"
    "buffer_store_dword v231, v241, s[48:51], s82 offen sc1\n"
    "v_cvt_pk_bf16_f32 v129, v66, v67\n"
    "v_mfma_f32_16x16x32_bf16 a[28:31], a[228:231], v[156:159], a[28:31]\n"
    "s_add_u32 m0, s68, 0x10000\n"
    "v_mov_b32_e32 v246, v250\n"
    "v_mov_b32_e32 v245, v249\n"
    "v_mfma_f32_16x16x32_bf16 a[24:27], a[228:231], v[152:155], a[24:27]\n"
    "s_add_u32 s92, s8, 0\n"
    "v_exp_f32_e32 v68, v68\n"
    "v_mfma_f32_16x16x32_bf16 a[20:23], a[228:231], v[148:151], a[20:23]\n"
    "buffer_load_dwordx4 v255, s[48:51], s92 offen sc1 lds\n"
    "ds_read_b64 a[192:193], v246 offset:0\n"
    "v_mfma_f32_16x16x32_bf16 a[16:19], a[228:231], v[144:147], a[16:19]\n"
    "v_exp_f32_e64 v69, v69\n"
    "v_mfma_f32_16x16x32_bf16 a[32:35], a[232:235], v[144:147], a[32:35]\n"
    "ds_read_b64 a[194:195], v245 offset:0\n"
    "v_cvt_pk_bf16_f32 v130, v68, v69\n"
    "v_mfma_f32_16x16x32_bf16 a[36:39], a[232:235], v[148:151], a[36:39]\n"
    "ds_read_b64 a[196:197], v246 offset:512\n"
    "buffer_load_dwordx4 v255, s[48:51], s92 offen offset:1024 sc1 lds\n"
    "v_mfma_f32_16x16x32_bf16 a[40:43], a[232:235], v[152:155], a[40:43]\n"
    "s_add_u32 m0, s68, 0x12000\n"
    "v_exp_f32_e64 v70, v70\n"
    "v_mfma_f32_16x16x32_bf16 a[44:47], a[232:235], v[156:159], a[44:47]\n"
    "s_add_u32 s92, s8, 0x1000\n"
    "ds_read_b64 a[198:199], v245 offset:512\n"
    "buffer_load_dwordx4 v255, s[48:51], s92 offen sc1 lds\n"
    "v_mfma_f32_16x16x32_bf16 a[60:63], a[236:239], v[156:159], a[60:63]\n"
    "v_exp_f32_e64 v71, v71\n"
    "v_mfma_f32_16x16x32_bf16 a[56:59], a[236:239], v[152:155], a[56:59]\n"
    "ds_read_b64 a[200:201], v246 offset:1024\n"
    "v_cvt_pk_bf16_f32 v131, v70, v71\n"
    "v_mfma_f32_16x16x32_bf16 a[52:55], a[236:239], v[148:151], a[52:55]\n"
    "v_exp_f32_e64 v72, v72\n"
    "v_mfma_f32_16x16x32_bf16 a[48:51], a[236:239], v[144:147], a[48:51]\n"
    "ds_read_b64 a[202:203], v245 offset:1024\n"
    "ds_read_b64 a[204:205], v246 offset:1536\n"
    "v_mfma_f32_16x16x32_bf16 a[64:67], a[240:243], v[144:147], a[64:67]\n"
    "PG5_%=:\n"
    "s_cbranch_execz PG9_%=\n"
    "v_exp_f32_e64 v73, v73\n"
    "v_mfma_f32_16x16x32_bf16 a[68:71], a[240:243], v[148:151], a[68:71]\n"
    "buffer_load_dwordx4 v255, s[48:51], s92 offen offset:1024 sc1 lds\n"
    "v_cvt_pk_bf16_f32 v132, v72, v73\n"
    "v_mfma_f32_16x16x32_bf16 a[72:75], a[240:243], v[152:155], a[72:75]\n"
    "s_add_u32 m0, s68, 0x14000\n"
    "s_add_u32 s92, s8, 0x2000\n"
    "ds_read_b64 a[206:207], v245 offset:1536\n"
    "v_mfma_f32_16x16x32_bf16 a[76:79], a[240:243], v[156:159], a[76:79]\n"
    "v_exp_f32_e64 v74, v74\n"
    "v_mfma_f32_16x16x32_bf16 a[92:95], a[244:247], v[156:159], a[92:95]\n"
    "ds_read_b64 a[208:209], v246 offset:4096\n"
    "ds_read_b64 a[210:211], v245 offset:4096\n"
    "v_mfma_f32_16x16x32_bf16 a[88:91], a[244:247], v[152:155], a[88:91]\n"
    "v_exp_f32_e64 v75, v75\n"
    "v_mfma_f32_16x16x32_bf16 a[84:87], a[244:247], v[148:151], a[84:87]\n"
    "buffer_load_dwordx4 v255, s[48:51], s92 offen sc1 lds\n"
    "v_cvt_pk_bf16_f32 v133, v74, v75\n"
    "v_mfma_f32_16x16x32_bf16 a[80:83], a[244:247], v[144:147], a[80:83]\n"
    "ds_read_b64 a[212:213], v246 offset:4608\n"
    "ds_read_b64 a[214:215], v245 offset:4608\n"
    "v_mfma_f32_16x16x32_bf16 a[96:99], a[248:251], v[144:147], a[96:99]\n"
    "v_exp_f32_e64 v76, v76\n"
    "v_mfma_f32_16x16x32_bf16 a[100:103], a[248:251], v[148:151], a[100:103]\n"
    "buffer_load_dwordx4 v255, s[48:51], s92 offen offset:1024 sc1 lds\n"
    "ds_read_b64 a[216:217], v246 offset:5120\n"
    "v_mfma_f32_16x16x32_bf16 a[104:107], a[248:251], v[152:155], a[104:107]\n"
    "s_add_u32 m0, s68, 0x16000\n"
    "v_exp_f32_e64 v77, v77\n"
    "v_mfma_f32_16x16x32_bf16 a[108:111], a[248:251], v[156:159], a[108:111]\n"
    "s_add_u32 s92, s8, 0x3000\n"
    "ds_read_b64 a[218:219], v245 offset:5120\n"
    "v_cvt_pk_bf16_f32 v134, v76, v77\n"
    "v_mfma_f32_16x16x32_bf16 a[124:127], a[252:255], v[156:159], a[124:127]\n"
    "v_exp_f32_e64 v78, v78\n"
    "v_mfma_f32_16x16x32_bf16 a[120:123], a[252:255], v[152:155], a[120:123]\n"
    "buffer_load_dwordx4 v255, s[48:51], s92 offen sc1 lds\n"
    "ds_read_b64 a[220:221], v246 offset:5632\n"
    "v_mfma_f32_16x16x32_bf16 a[116:119], a[252:255], v[148:151], a[116:119]\n"
    "v_exp_f32_e64 v79, v79\n"
    "v_mfma_f32_16x16x32_bf16 a[112:115], a[252:255], v[144:147], a[112:115]\n"
    "s_waitcnt lgkmcnt(13)\n"
    "ds_read_b64 a[222:223], v245 offset:5632\n"
    "v_cvt_pk_bf16_f32 v135, v78, v79\n"
    "v_mfma_f32_16x16x32_bf16 v[96:99], a[192:195], v[0:3], v[192:195]\n"
    "s_waitcnt lgkmcnt(12)\n"
    "v_exp_f32_e64 v80, v80\n"
    "v_mfma_f32_16x16x32_bf16 v[96:99], a[196:199], v[4:7], v[96:99]\n"
    "s_waitcnt lgkmcnt(6)\n"
    "buffer_load_dwordx4 v255, s[48:51], s92 offen offset:1024 sc1 lds\n"
    "v_mov_b32_e32 v248, v252\n"
    "v_mfma_f32_16x16x32_bf16 v[96:99], a[200:203], v[8:11], v[96:99]\n"
    "s_add_u32 m0, s68, 0x18000\n"
    "v_exp_f32_e64 v81, v81\n"
    "v_mfma_f32_16x16x32_bf16 v[96:99], a[204:207], v[12:15], v[96:99]\n"
    "s_add_u32 s92, s8, 0x4000\n"
    "buffer_load_dwordx4 v255, s[48:51], s92 offen sc1 lds\n"
    "v_cvt_pk_bf16_f32 v136, v80, v81\n"
    "v_mfma_f32_16x16x32_bf16 v[100:103], a[208:211], v[0:3], v[192:195]\n"
    "s_waitcnt lgkmcnt(4)\n"
    "v_exp_f32_e32 v82, v82\n"
    "v_mfma_f32_16x16x32_bf16 v[100:103], a[212:215], v[4:7], v[100:103]\n"
    "s_waitcnt lgkmcnt(0)\n"
    "buffer_load_dwordx4 v255, s[48:51], s92 offen offset:1024 sc1 lds\n"
    "v_mov_b32_e32 v247, v251\n"
    "v_mfma_f32_16x16x32_bf16 v[100:103], a[216:219], v[8:11], v[100:103]\n"
    "s_add_u32 m0, s68, 0x1a000\n"
    "v_exp_f32_e64 v83, v83\n"
    "v_mfma_f32_16x16x32_bf16 v[100:103], a[220:223], v[12:15], v[100:103]\n"
    "s_add_u32 s92, s9, 0\n"
    "ds_read_b64_tr_b16 a[224:225], v248 offset:8192\n"
    "v_cvt_pk_bf16_f32 v137, v82, v83\n"
    "v_mfma_f32_16x16x32_bf16 v[104:107], a[192:195], v[16:19], v[196:199]\n"
    "v_exp_f32_e32 v84, v84\n"
    "v_mfma_f32_16x16x32_bf16 v[104:107], a[196:199], v[20:23], v[104:107]\n"
    "ds_read_b64_tr_b16 a[226:227], v248 offset:12288\n"
    "ds_read_b64_tr_b16 a[228:229], v247 offset:8192\n"
    "v_mfma_f32_16x16x32_bf16 v[104:107], a[200:203], v[24:27], v[104:107]\n"
    "v_exp_f32_e64 v85, v85\n"
    "v_mfma_f32_16x16x32_bf16 v[104:107], a[204:207], v[28:31], v[104:107]\n"
    "buffer_load_dwordx4 v255, s[48:51], s92 offen sc1 lds\n"
    "v_cvt_pk_bf16_f32 v138, v84, v85\n"
    "v_mfma_f32_16x16x32_bf16 v[108:111], a[208:211], v[16:19], v[196:199]\n"
    "ds_read_b64_tr_b16 a[230:231], v247 offset:12288\n"
    "ds_read_b64_tr_b16 a[232:233], v248 offset:8704\n"
    "v_mfma_f32_16x16x32_bf16 v[108:111], a[212:215], v[20:23], v[108:111]\n"
    "v_exp_f32_e64 v86, v86\n"
    "v_mfma_f32_16x16x32_bf16 v[108:111], a[216:219], v[24:27], v[108:111]\n"
    "v_exp_f32_e64 v87, v87\n"
    "v_mfma_f32_16x16x32_bf16 v[108:111], a[220:223], v[28:31], v[108:111]\n"
    "buffer_load_dwordx4 v255, s[48:51], s92 offen offset:1024 sc1 lds\n"
    "v_cvt_pk_bf16_f32 v139, v86, v87\n"
    "v_mfma_f32_16x16x32_bf16 v[112:115], a[192:195], v[32:35], v[200:203]\n"
    "s_add_u32 m0, s68, 0x1c000\n"
    "ds_read_b64_tr_b16 a[234:235], v248 offset:12800\n"
    "ds_read_b64_tr_b16 a[236:237], v247 offset:8704\n"
    "v_mfma_f32_16x16x32_bf16 v[112:115], a[196:199], v[36:39], v[112:115]\n"
    "s_add_u32 s92, s9, 0x1000\n"
    "v_exp_f32_e64 v88, v88\n"
    "v_mfma_f32_16x16x32_bf16 v[112:115], a[200:203], v[40:43], v[112:115]\n"
    "ds_read_b64_tr_b16 a[238:239], v247 offset:12800\n"
    "buffer_load_dwordx4 v255, s[48:51], s92 offen sc1 lds\n"
    "v_mfma_f32_16x16x32_bf16 v[112:115], a[204:207], v[44:47], v[112:115]\n"
    "v_exp_f32_e64 v89, v89\n"
    "v_mfma_f32_16x16x32_bf16 v[116:119], a[208:211], v[32:35], v[200:203]\n"
    "ds_read_b64_tr_b16 a[240:241], v248 offset:9216\n"
    "v_cvt_pk_bf16_f32 v140, v88, v89\n"
    "v_mfma_f32_16x16x32_bf16 v[116:119], a[212:215], v[36:39], v[116:119]\n"
    "ds_read_b64_tr_b16 a[242:243], v248 offset:13312\n"
    "ds_read_b64_tr_b16 a[244:245], v247 offset:9216\n"
    "v_mfma_f32_16x16x32_bf16 v[116:119], a[216:219], v[40:43], v[116:119]\n"
    "v_exp_f32_e64 v90, v90\n"
    "v_mfma_f32_16x16x32_bf16 v[116:119], a[220:223], v[44:47], v[116:119]\n"
    "buffer_load_dwordx4 v255, s[48:51], s92 offen offset:1024 sc1 lds\n"
    "ds_read_b64_tr_b16 a[246:247], v247 offset:13312\n"
    "v_mfma_f32_16x16x32_bf16 v[120:123], a[192:195], v[48:51], v[204:207]\n"
    "s_add_u32 m0, s68, 0x1e000\n"
    "v_exp_f32_e64 v91, v91\n"
    "v_mfma_f32_16x16x32_bf16 v[120:123], a[196:199], v[52:55], v[120:123]\n"
    "s_add_u32 s92, s9, 0x2000\n"
    "buffer_load_dwordx4 v255, s[48:51], s92 offen sc1 lds\n"
    "v_cvt_pk_bf16_f32 v141, v90, v91\n"
    "v_mfma_f32_16x16x32_bf16 v[120:123], a[200:203], v[56:59], v[120:123]\n"
    "v_exp_f32_e64 v92, v92\n"
    "v_mfma_f32_16x16x32_bf16 v[120:123], a[204:207], v[60:63], v[120:123]\n"
    "ds_read_b64_tr_b16 a[248:249], v248 offset:9728\n"
    "ds_read_b64_tr_b16 a[250:251], v248 offset:13824\n"
    "v_mfma_f32_16x16x32_bf16 v[124:127], a[208:211], v[48:51], v[204:207]\n"
    "v_exp_f32_e64 v93, v93\n"
    "v_mfma_f32_16x16x32_bf16 v[124:127], a[212:215], v[52:55], v[124:127]\n"
    "ds_read_b64_tr_b16 a[252:253], v247 offset:9728\n"
    "v_cvt_pk_bf16_f32 v142, v92, v93\n"
    "v_mfma_f32_16x16x32_bf16 v[124:127], a[216:219], v[56:59], v[124:127]\n"
    "v_exp_f32_e64 v94, v94\n"
    "v_mfma_f32_16x16x32_bf16 v[124:127], a[220:223], v[60:63], v[124:127]\n"
    "buffer_load_dwordx4 v255, s[48:51], s92 offen offset:1024 sc1 lds\n"
    "ds_read_b64_tr_b16 a[254:255], v247 offset:13824\n"
    "v_exp_f32_e32 v95, v95\n"
    "s_nop 0\n"
    "v_cvt_pk_bf16_f32 v143, v94, v95\n"
    "v_mfma_f32_16x16x32_bf16 v[208:211], v[224:227], v[128:131], v[208:211]\n"
    "v_exp_f32_e64 v96, v96\n"
    "v_mfma_f32_16x16x32_bf16 v[212:215], v[224:227], v[132:135], v[212:215]\n"
    "v_mfma_f32_16x16x32_bf16 v[216:219], v[224:227], v[136:139], v[216:219]\n"
    "v_exp_f32_e64 v97, v97\n"
    "v_mfma_f32_16x16x32_bf16 v[220:223], v[224:227], v[140:143], v[220:223]\n"
    "s_waitcnt lgkmcnt(14)\n"
    "v_cvt_pk_bf16_f32 v144, v96, v97\n"
    "v_mfma_f32_16x16x32_bf16 a[0:3], a[224:227], v[128:131], a[0:3]\n"
    "v_exp_f32_e32 v98, v98\n"
    "v_mfma_f32_16x16x32_bf16 a[4:7], a[224:227], v[132:135], a[4:7]\n"
    "v_mfma_f32_16x16x32_bf16 a[8:11], a[224:227], v[136:139], a[8:11]\n"
    "v_exp_f32_e64 v99, v99\n"
    "v_mfma_f32_16x16x32_bf16 a[12:15], a[224:227], v[140:143], a[12:15]\n"
    "s_waitcnt lgkmcnt(12)\n"
    "ds_read_b64 a[192:193], v246 offset:16384\n"
    "v_cvt_pk_bf16_f32 v145, v98, v99\n"
    "v_mfma_f32_16x16x32_bf16 a[28:31], a[228:231], v[140:143], a[28:31]\n"
    "s_add_u32 m0, s68, 0x20000\n"
    "ds_read_b64 a[194:195], v245 offset:16384\n"
    "v_mfma_f32_16x16x32_bf16 a[24:27], a[228:231], v[136:139], a[24:27]\n"
    "s_add_u32 s92, s9, 0x3000\n"
    "v_exp_f32_e32 v100, v100\n"
    "v_mfma_f32_16x16x32_bf16 a[20:23], a[228:231], v[132:135], a[20:23]\n"
    "buffer_load_dwordx4 v255, s[48:51], s92 offen sc1 lds\n"
    "ds_read_b64 a[196:197], v246 offset:16896\n"
    "v_mfma_f32_16x16x32_bf16 a[16:19], a[228:231], v[128:131], a[16:19]\n"
    "s_waitcnt lgkmcnt(13)\n"
    "v_exp_f32_e32 v101, v101\n"
    "v_mfma_f32_16x16x32_bf16 a[32:35], a[232:235], v[128:131], a[32:35]\n"
    "ds_read_b64 a[198:199], v245 offset:16896\n"
    "v_cvt_pk_bf16_f32 v146, v100, v101\n"
    "v_mfma_f32_16x16x32_bf16 a[36:39], a[232:235], v[132:135], a[36:39]\n"
    "s_waitcnt lgkmcnt(12)\n"
    "buffer_load_dwordx4 v255, s[48:51], s92 offen offset:1024 sc1 lds\n"
    "ds_read_b64 a[200:201], v246 offset:17408\n"
    "v_mfma_f32_16x16x32_bf16 a[40:43], a[232:235], v[136:139], a[40:43]\n"
    "s_add_u32 m0, s68, 0x22000\n"
    "v_exp_f32_e32 v102, v102\n"
    "v_mfma_f32_16x16x32_bf16 a[44:47], a[232:235], v[140:143], a[44:47]\n"
    "s_add_u32 s92, s9, 0x4000\n"
    "v_exp_f32_e64 v103, v103\n"
    "v_mfma_f32_16x16x32_bf16 a[60:63], a[236:239], v[140:143], a[60:63]\n"
    "buffer_load_dwordx4 v255, s[48:51], s92 offen sc1 lds\n"
    "v_cvt_pk_bf16_f32 v147, v102, v103\n"
    "v_mfma_f32_16x16x32_bf16 a[56:59], a[236:239], v[136:139], a[56:59]\n"
    "ds_read_b64 a[202:203], v245 offset:17408\n"
    "ds_read_b64 a[204:205], v246 offset:17920\n"
    "v_mfma_f32_16x16x32_bf16 a[52:55], a[236:239], v[132:135], a[52:55]\n"
    "v_exp_f32_e64 v104, v104\n"
    "v_mfma_f32_16x16x32_bf16 a[48:51], a[236:239], v[128:131], a[48:51]\n"
    "s_waitcnt lgkmcnt(13)\n"
    "ds_read_b64 a[206:207], v245 offset:17920\n"
    "buffer_load_dwordx4 v255, s[48:51], s92 offen offset:1024 sc1 lds\n"
    "v_mfma_f32_16x16x32_bf16 a[64:67], a[240:243], v[128:131], a[64:67]\n"
    "v_exp_f32_e32 v105, v105\n"
    "v_mfma_f32_16x16x32_bf16 a[68:71], a[240:243], v[132:135], a[68:71]\n"
    "s_add_u32 m0, s68, 0x24000\n"
    "ds_read_b64 a[208:209], v246 offset:20480\n"
    "v_cvt_pk_bf16_f32 v148, v104, v105\n"
    "v_mfma_f32_16x16x32_bf16 a[72:75], a[240:243], v[136:139], a[72:75]\n"
    "s_add_u32 s92, s10, 0\n"
    "v_exp_f32_e32 v106, v106\n"
    "v_mfma_f32_16x16x32_bf16 a[76:79], a[240:243], v[140:143], a[76:79]\n"
    "s_waitcnt lgkmcnt(13)\n"
    "ds_read_b64 a[210:211], v245 offset:20480\n"
    "buffer_load_dwordx4 v255, s[48:51], s92 offen sc1 lds\n"
    "v_mfma_f32_16x16x32_bf16 a[92:95], a[244:247], v[140:143], a[92:95]\n"
    "v_exp_f32_e32 v107, v107\n"
    "v_mfma_f32_16x16x32_bf16 a[88:91], a[244:247], v[136:139], a[88:91]\n"
    "ds_read_b64 a[212:213], v246 offset:20992\n"
    "v_cvt_pk_bf16_f32 v149, v106, v107\n"
    "v_mfma_f32_16x16x32_bf16 a[84:87], a[244:247], v[132:135], a[84:87]\n"
    "ds_read_b64 a[214:215], v245 offset:20992\n"
    "ds_read_b64 a[216:217], v246 offset:21504\n"
    "v_mfma_f32_16x16x32_bf16 a[80:83], a[244:247], v[128:131], a[80:83]\n"
    "s_waitcnt lgkmcnt(15)\n"
    "v_exp_f32_e32 v108, v108\n"
    "v_mfma_f32_16x16x32_bf16 a[96:99], a[248:251], v[128:131], a[96:99]\n"
    "buffer_load_dwordx4 v255, s[48:51], s92 offen offset:1024 sc1 lds\n"
    "ds_read_b64 a[218:219], v245 offset:21504\n"
    "v_mfma_f32_16x16x32_bf16 a[100:103], a[248:251], v[132:135], a[100:103]\n"
    "s_add_u32 m0, s68, 0x26000\n"
    "v_exp_f32_e64 v109, v109\n"
    "v_mfma_f32_16x16x32_bf16 a[104:107], a[248:251], v[136:139], a[104:107]\n"
    "s_add_u32 s92, s10, 0x1000\n"
    "buffer_load_dwordx4 v255, s[48:51], s92 offen sc1 lds\n"
    "v_cvt_pk_bf16_f32 v150, v108, v109\n"
    "v_mfma_f32_16x16x32_bf16 a[108:111], a[248:251], v[140:143], a[108:111]\n"
    "s_waitcnt lgkmcnt(14)\n"
    "v_exp_f32_e32 v110, v110\n"
    "v_mfma_f32_16x16x32_bf16 a[124:127], a[252:255], v[140:143], a[124:127]\n"
    "ds_read_b64 a[220:221], v246 offset:22016\n"
    "ds_read_b64 a[222:223], v245 offset:22016\n"
    "v_mfma_f32_16x16x32_bf16 a[120:123], a[252:255], v[136:139], a[120:123]\n"
    "v_exp_f32_e64 v111, v111\n"
    "v_mfma_f32_16x16x32_bf16 a[116:119], a[252:255], v[132:135], a[116:119]\n"
    "s_waitcnt lgkmcnt(14)\n"
    "buffer_load_dwordx4 v255, s[48:51], s92 offen offset:1024 sc1 lds\n"
    "v_cvt_pk_bf16_f32 v151, v110, v111\n"
    "v_mfma_f32_16x16x32_bf16 a[112:115], a[252:255], v[128:131], a[112:115]\n"
    "s_add_u32 s91, s16, 0x2000\n"
    "v_exp_f32_e32 v112, v112\n"
    "v_mfma_f32_16x16x32_bf16 v[64:67], a[192:195], v[0:3], v[192:195]\n"
    "s_waitcnt lgkmcnt(12)\n"
    "v_exp_f32_e32 v113, v113\n"
    "v_mfma_f32_16x16x32_bf16 v[64:67], a[196:199], v[4:7], v[64:67]\n"
    "s_waitcnt lgkmcnt(10)\n"
    "buffer_load_dwordx4 v[164:167], v255, s[48:51], s91 offen sc1\n"
    "v_cvt_pk_bf16_f32 v152, v112, v113\n"
    "v_mfma_f32_16x16x32_bf16 v[64:67], a[200:203], v[8:11], v[64:67]\n"
    "s_waitcnt lgkmcnt(8)\n"
    "v_exp_f32_e64 v114, v114\n"
    "v_mfma_f32_16x16x32_bf16 v[64:67], a[204:207], v[12:15], v[64:67]\n"
    "s_waitcnt lgkmcnt(6)\n"
    "ds_read_b64_tr_b16 a[224:225], v248 offset:24576\n"
    "ds_read_b64_tr_b16 a[226:227], v248 offset:28672\n"
    "v_mfma_f32_16x16x32_bf16 v[68:71], a[208:211], v[0:3], v[192:195]\n"
    "s_waitcnt lgkmcnt(6)\n"
    "v_exp_f32_e64 v115, v115\n"
    "v_mfma_f32_16x16x32_bf16 v[68:71], a[212:215], v[4:7], v[68:71]\n"
    "s_waitcnt lgkmcnt(4)\n"
    "ds_read_b64_tr_b16 a[228:229], v247 offset:24576\n"
    "v_cvt_pk_bf16_f32 v153, v114, v115\n"
    "v_mfma_f32_16x16x32_bf16 v[68:71], a[216:219], v[8:11], v[68:71]\n"
    "s_waitcnt lgkmcnt(3)\n"
    "buffer_load_dwordx4 v[168:171], v255, s[48:51], s91 offen offset:1024 sc1\n"
    "ds_read_b64_tr_b16 a[230:231], v247 offset:28672\n"
    "v_mfma_f32_16x16x32_bf16 v[68:71], a[220:223], v[12:15], v[68:71]\n"
    "v_exp_f32_e64 v116, v116\n"
    "v_mfma_f32_16x16x32_bf16 v[72:75], a[192:195], v[16:19], v[196:199]\n"
    "v_exp_f32_e64 v117, v117\n"
    "v_mfma_f32_16x16x32_bf16 v[72:75], a[196:199], v[20:23], v[72:75]\n"
    "buffer_load_dwordx4 v[172:175], v255, s[48:51], s91 offen offset:2048 sc1\n"
    "v_cvt_pk_bf16_f32 v154, v116, v117\n"
    "v_mfma_f32_16x16x32_bf16 v[72:75], a[200:203], v[24:27], v[72:75]\n"
    "ds_read_b64_tr_b16 a[232:233], v248 offset:25088\n"
    "ds_read_b64_tr_b16 a[234:235], v248 offset:29184\n"
    "v_mfma_f32_16x16x32_bf16 v[72:75], a[204:207], v[28:31], v[72:75]\n"
    "v_exp_f32_e64 v118, v118\n"
    "v_mfma_f32_16x16x32_bf16 v[76:79], a[208:211], v[16:19], v[196:199]\n"
    "ds_read_b64_tr_b16 a[236:237], v247 offset:25088\n"
    "buffer_load_dwordx4 v[176:179], v255, s[48:51], s91 offen offset:3072 sc1\n"
    "v_mfma_f32_16x16x32_bf16 v[76:79], a[212:215], v[20:23], v[76:79]\n"
    "s_add_u32 s91, s16, 0x3000\n"
    "v_exp_f32_e64 v119, v119\n"
    "v_mfma_f32_16x16x32_bf16 v[76:79], a[216:219], v[24:27], v[76:79]\n"
    "ds_read_b64_tr_b16 a[238:239], v247 offset:29184\n"
    "v_cvt_pk_bf16_f32 v155, v118, v119\n"
    "v_mfma_f32_16x16x32_bf16 v[76:79], a[220:223], v[28:31], v[76:79]\n"
    "v_exp_f32_e64 v120, v120\n"
    "v_mfma_f32_16x16x32_bf16 v[80:83], a[192:195], v[32:35], v[200:203]\n"
    "ds_read_b64_tr_b16 a[240:241], v248 offset:25600\n"
    "buffer_load_dwordx4 v[180:183], v255, s[48:51], s91 offen sc1\n"
    "v_mfma_f32_16x16x32_bf16 v[80:83], a[196:199], v[36:39], v[80:83]\n"
    "v_exp_f32_e64 v121, v121\n"
    "v_mfma_f32_16x16x32_bf16 v[80:83], a[200:203], v[40:43], v[80:83]\n"
    "ds_read_b64_tr_b16 a[242:243], v248 offset:29696\n"
    "v_cvt_pk_bf16_f32 v156, v120, v121\n"
    "v_mfma_f32_16x16x32_bf16 v[80:83], a[204:207], v[44:47], v[80:83]\n"
    "v_exp_f32_e64 v122, v122\n"
    "v_mfma_f32_16x16x32_bf16 v[84:87], a[208:211], v[32:35], v[200:203]\n"
    "ds_read_b64_tr_b16 a[244:245], v247 offset:25600\n"
    "ds_read_b64_tr_b16 a[246:247], v247 offset:29696\n"
    "v_mfma_f32_16x16x32_bf16 v[84:87], a[212:215], v[36:39], v[84:87]\n"
    "buffer_load_dwordx4 v[184:187], v255, s[48:51], s91 offen offset:1024 sc1\n"
    "ds_read_b64_tr_b16 a[248:249], v248 offset:26112\n"
    "v_mfma_f32_16x16x32_bf16 v[84:87], a[216:219], v[40:43], v[84:87]\n"
    "v_exp_f32_e64 v123, v123\n"
    "v_mfma_f32_16x16x32_bf16 v[84:87], a[220:223], v[44:47], v[84:87]\n"
    "buffer_load_dwordx4 v[188:191], v255, s[48:51], s91 offen offset:2048 sc1\n"
    "v_cvt_pk_bf16_f32 v157, v122, v123\n"
    "v_mfma_f32_16x16x32_bf16 v[88:91], a[192:195], v[48:51], v[204:207]\n"
    "v_exp_f32_e64 v124, v124\n"
    "v_mfma_f32_16x16x32_bf16 v[88:91], a[196:199], v[52:55], v[88:91]\n"
    "ds_read_b64_tr_b16 a[250:251], v248 offset:30208\n"
    "ds_read_b64_tr_b16 a[252:253], v247 offset:26112\n"
    "v_mfma_f32_16x16x32_bf16 v[88:91], a[200:203], v[56:59], v[88:91]\n"
    "v_exp_f32_e64 v125, v125\n"
    "v_mfma_f32_16x16x32_bf16 v[88:91], a[204:207], v[60:63], v[88:91]\n"
    "buffer_load_dwordx4 a[128:131], v255, s[48:51], s91 offen offset:3072 sc1\n"
    "v_cvt_pk_bf16_f32 v158, v124, v125\n"
    "v_mfma_f32_16x16x32_bf16 v[92:95], a[208:211], v[48:51], v[204:207]\n"
    "v_exp_f32_e64 v126, v126\n"
    "v_mfma_f32_16x16x32_bf16 v[92:95], a[212:215], v[52:55], v[92:95]\n"
    "ds_read_b64_tr_b16 a[254:255], v247 offset:30208\n"
    "v_mfma_f32_16x16x32_bf16 v[92:95], a[216:219], v[56:59], v[92:95]\n"
    "v_exp_f32_e64 v127, v127\n"
    "v_mfma_f32_16x16x32_bf16 v[92:95], a[220:223], v[60:63], v[92:95]\n"
    "v_cvt_pk_bf16_f32 v159, v126, v127\n"
    "v_mfma_f32_16x16x32_bf16 v[208:211], v[224:227], v[144:147], v[208:211]\n"
    "v_exp_f32_e64 v64, v64\n"
    "v_mfma_f32_16x16x32_bf16 v[212:215], v[224:227], v[148:151], v[212:215]\n"
    "v_mfma_f32_16x16x32_bf16 v[216:219], v[224:227], v[152:155], v[216:219]\n"
    "v_exp_f32_e64 v65, v65\n"
    "v_mfma_f32_16x16x32_bf16 v[220:223], v[224:227], v[156:159], v[220:223]\n"
    "s_waitcnt lgkmcnt(14)\n"
    "v_cvt_pk_bf16_f32 v128, v64, v65\n"
    "v_mfma_f32_16x16x32_bf16 a[0:3], a[224:227], v[144:147], a[0:3]\n"
    "v_exp_f32_e32 v66, v66\n"
    "v_mfma_f32_16x16x32_bf16 a[4:7], a[224:227], v[148:151], a[4:7]\n"
    "s_waitcnt vmcnt(35)\n"
    "s_waitcnt lgkmcnt(0)\n"
    "s_barrier\n"
    "v_mfma_f32_16x16x32_bf16 a[8:11], a[224:227], v[152:155], a[8:11]\n"
    "v_exp_f32_e32 v67, v67\n"
    "v_mfma_f32_16x16x32_bf16 a[12:15], a[224:227], v[156:159], a[12:15]\n"
    "v_cvt_pk_bf16_f32 v129, v66, v67\n"
    "v_add_u32_e32 v246, 0x8000, v250\n"
    "v_mfma_f32_16x16x32_bf16 a[28:31], a[228:231], v[156:159], a[28:31]\n"
    "s_add_u32 m0, s68, 0\n"
    "v_add_u32_e32 v245, 0x8000, v249\n"
    "v_mfma_f32_16x16x32_bf16 a[24:27], a[228:231], v[152:155], a[24:27]\n"
    "s_add_u32 s92, s10, 0x2000\n"
    "v_exp_f32_e32 v68, v68\n"
    "v_mfma_f32_16x16x32_bf16 a[20:23], a[228:231], v[148:151], a[20:23]\n"
    "buffer_load_dwordx4 v255, s[48:51], s92 offen sc1 lds\n"
    "ds_read_b64 a[192:193], v246 offset:0\n"
    "v_mfma_f32_16x16x32_bf16 a[16:19], a[228:231], v[144:147], a[16:19]\n"
    "v_exp_f32_e64 v69, v69\n"
    "v_mfma_f32_16x16x32_bf16 a[32:35], a[232:235], v[144:147], a[32:35]\n"
    "ds_read_b64 a[194:195], v245 offset:0\n"
    "v_cvt_pk_bf16_f32 v130, v68, v69\n"
    "v_mfma_f32_16x16x32_bf16 a[36:39], a[232:235], v[148:151], a[36:39]\n"
    "ds_read_b64 a[196:197], v246 offset:512\n"
    "buffer_load_dwordx4 v255, s[48:51], s92 offen offset:1024 sc1 lds\n"
    "v_mfma_f32_16x16x32_bf16 a[40:43], a[232:235], v[152:155], a[40:43]\n"
    "s_add_u32 m0, s68, 0x2000\n"
    "v_exp_f32_e64 v70, v70\n"
    "v_mfma_f32_16x16x32_bf16 a[44:47], a[232:235], v[156:159], a[44:47]\n"
    "s_add_u32 s92, s10, 0x3000\n"
    "ds_read_b64 a[198:199], v245 offset:512\n"
    "buffer_load_dwordx4 v255, s[48:51], s92 offen sc1 lds\n"
    "v_mfma_f32_16x16x32_bf16 a[60:63], a[236:239], v[156:159], a[60:63]\n"
    "v_exp_f32_e64 v71, v71\n"
    "v_mfma_f32_16x16x32_bf16 a[56:59], a[236:239], v[152:155], a[56:59]\n"
    "ds_read_b64 a[200:201], v246 offset:1024\n"
    "v_cvt_pk_bf16_f32 v131, v70, v71\n"
    "v_mfma_f32_16x16x32_bf16 a[52:55], a[236:239], v[148:151], a[52:55]\n"
    "v_exp_f32_e64 v72, v72\n"
    "v_mfma_f32_16x16x32_bf16 a[48:51], a[236:239], v[144:147], a[48:51]\n"
    "ds_read_b64 a[202:203], v245 offset:1024\n"
    "ds_read_b64 a[204:205], v246 offset:1536\n"
    "v_mfma_f32_16x16x32_bf16 a[64:67], a[240:243], v[144:147], a[64:67]\n"
    "v_exp_f32_e64 v73, v73\n"
    "v_mfma_f32_16x16x32_bf16 a[68:71], a[240:243], v[148:151], a[68:71]\n"
    "buffer_load_dwordx4 v255, s[48:51], s92 offen offset:1024 sc1 lds\n"
    "v_cvt_pk_bf16_f32 v132, v72, v73\n"
    "v_mfma_f32_16x16x32_bf16 a[72:75], a[240:243], v[152:155], a[72:75]\n"
    "s_add_u32 m0, s68, 0x4000\n"
    "s_add_u32 s92, s10, 0x4000\n"
    "ds_read_b64 a[206:207], v245 offset:1536\n"
    "v_mfma_f32_16x16x32_bf16 a[76:79], a[240:243], v[156:159], a[76:79]\n"
    "v_exp_f32_e64 v74, v74\n"
    "v_mfma_f32_16x16x32_bf16 a[92:95], a[244:247], v[156:159], a[92:95]\n"
    "ds_read_b64 a[208:209], v246 offset:4096\n"
    "ds_read_b64 a[210:211], v245 offset:4096\n"
    "v_mfma_f32_16x16x32_bf16 a[88:91], a[244:247], v[152:155], a[88:91]\n"
    "v_exp_f32_e64 v75, v75\n"
    "v_mfma_f32_16x16x32_bf16 a[84:87], a[244:247], v[148:151], a[84:87]\n"
    "buffer_load_dwordx4 v255, s[48:51], s92 offen sc1 lds\n"
    "v_cvt_pk_bf16_f32 v133, v74, v75\n"
    "v_mfma_f32_16x16x32_bf16 a[80:83], a[244:247], v[144:147], a[80:83]\n"
    "ds_read_b64 a[212:213], v246 offset:4608\n"
    "ds_read_b64 a[214:215], v245 offset:4608\n"
    "v_mfma_f32_16x16x32_bf16 a[96:99], a[248:251], v[144:147], a[96:99]\n"
    "v_exp_f32_e64 v76, v76\n"
    "v_mfma_f32_16x16x32_bf16 a[100:103], a[248:251], v[148:151], a[100:103]\n"
    "buffer_load_dwordx4 v255, s[48:51], s92 offen offset:1024 sc1 lds\n"
    "ds_read_b64 a[216:217], v246 offset:5120\n"
    "v_mfma_f32_16x16x32_bf16 a[104:107], a[248:251], v[152:155], a[104:107]\n"
    "s_add_u32 m0, s68, 0x6000\n"
    "v_exp_f32_e64 v77, v77\n"
    "v_mfma_f32_16x16x32_bf16 a[108:111], a[248:251], v[156:159], a[108:111]\n"
    "s_add_u32 s92, s11, 0\n"
    "ds_read_b64 a[218:219], v245 offset:5120\n"
    "v_cvt_pk_bf16_f32 v134, v76, v77\n"
    "v_mfma_f32_16x16x32_bf16 a[124:127], a[252:255], v[156:159], a[124:127]\n"
    "v_exp_f32_e32 v78, v78\n"
    "v_mfma_f32_16x16x32_bf16 a[120:123], a[252:255], v[152:155], a[120:123]\n"
    "buffer_load_dwordx4 v255, s[48:51], s92 offen sc1 lds\n"
    "ds_read_b64 a[220:221], v246 offset:5632\n"
    "v_mfma_f32_16x16x32_bf16 a[116:119], a[252:255], v[148:151], a[116:119]\n"
    "v_exp_f32_e64 v79, v79\n"
    "v_mfma_f32_16x16x32_bf16 a[112:115], a[252:255], v[144:147], a[112:115]\n"
    "s_waitcnt lgkmcnt(13)\n"
    "ds_read_b64 a[222:223], v245 offset:5632\n"
    "v_cvt_pk_bf16_f32 v135, v78, v79\n"
    "v_mfma_f32_16x16x32_bf16 v[96:99], a[192:195], v[0:3], v[192:195]\n"
    "s_waitcnt lgkmcnt(12)\n"
    "v_exp_f32_e64 v80, v80\n"
    "v_mfma_f32_16x16x32_bf16 v[96:99], a[196:199], v[4:7], v[96:99]\n"
    "s_waitcnt lgkmcnt(8)\n"
    "buffer_load_dwordx4 v255, s[48:51], s92 offen offset:1024 sc1 lds\n"
    "v_add_u32_e32 v248, 0x8000, v252\n"
    "v_mfma_f32_16x16x32_bf16 v[96:99], a[200:203], v[8:11], v[96:99]\n"
    "s_add_u32 s91, s16, 0x4000\n"
    "v_exp_f32_e32 v81, v81\n"
    "v_mfma_f32_16x16x32_bf16 v[96:99], a[204:207], v[12:15], v[96:99]\n"
    "s_waitcnt lgkmcnt(6)\n"
    "buffer_load_dwordx4 a[132:135], v255, s[48:51], s91 offen sc1\n"
    "v_cvt_pk_bf16_f32 v136, v80, v81\n"
    "v_mfma_f32_16x16x32_bf16 v[100:103], a[208:211], v[0:3], v[192:195]\n"
    "s_waitcnt lgkmcnt(4)\n"
    "v_exp_f32_e64 v82, v82\n"
    "v_mfma_f32_16x16x32_bf16 v[100:103], a[212:215], v[4:7], v[100:103]\n"
    "s_waitcnt lgkmcnt(2)\n"
    "buffer_load_dwordx4 a[136:139], v255, s[48:51], s91 offen offset:1024 sc1\n"
    "v_add_u32_e32 v247, 0x8000, v251\n"
    "v_mfma_f32_16x16x32_bf16 v[100:103], a[216:219], v[8:11], v[100:103]\n"
    "s_waitcnt lgkmcnt(0)\n"
    "v_exp_f32_e64 v83, v83\n"
    "v_mfma_f32_16x16x32_bf16 v[100:103], a[220:223], v[12:15], v[100:103]\n"
    "ds_read_b64_tr_b16 a[224:225], v248 offset:8192\n"
    "v_cvt_pk_bf16_f32 v137, v82, v83\n"
    "v_mfma_f32_16x16x32_bf16 v[104:107], a[192:195], v[16:19], v[196:199]\n"
    "v_exp_f32_e64 v84, v84\n"
    "v_mfma_f32_16x16x32_bf16 v[104:107], a[196:199], v[20:23], v[104:107]\n"
    "ds_read_b64_tr_b16 a[226:227], v248 offset:12288\n"
    "ds_read_b64_tr_b16 a[228:229], v247 offset:8192\n"
    "v_mfma_f32_16x16x32_bf16 v[104:107], a[200:203], v[24:27], v[104:107]\n"
    "v_exp_f32_e64 v85, v85\n"
    "v_mfma_f32_16x16x32_bf16 v[104:107], a[204:207], v[28:31], v[104:107]\n"
    "buffer_load_dwordx4 a[140:143], v255, s[48:51], s91 offen offset:2048 sc1\n"
    "v_cvt_pk_bf16_f32 v138, v84, v85\n"
    "v_mfma_f32_16x16x32_bf16 v[108:111], a[208:211], v[16:19], v[196:199]\n"
    "ds_read_b64_tr_b16 a[230:231], v247 offset:12288\n"
    "ds_read_b64_tr_b16 a[232:233], v248 offset:8704\n"
    "v_mfma_f32_16x16x32_bf16 v[108:111], a[212:215], v[20:23], v[108:111]\n"
    "v_exp_f32_e64 v86, v86\n"
    "v_mfma_f32_16x16x32_bf16 v[108:111], a[216:219], v[24:27], v[108:111]\n"
    "v_exp_f32_e64 v87, v87\n"
    "v_mfma_f32_16x16x32_bf16 v[108:111], a[220:223], v[28:31], v[108:111]\n"
    "buffer_load_dwordx4 a[144:147], v255, s[48:51], s91 offen offset:3072 sc1\n"
    "v_cvt_pk_bf16_f32 v139, v86, v87\n"
    "v_mfma_f32_16x16x32_bf16 v[112:115], a[192:195], v[32:35], v[200:203]\n"
    "s_add_u32 s91, s17, 0\n"
    "ds_read_b64_tr_b16 a[234:235], v248 offset:12800\n"
    "ds_read_b64_tr_b16 a[236:237], v247 offset:8704\n"
    "v_mfma_f32_16x16x32_bf16 v[112:115], a[196:199], v[36:39], v[112:115]\n"
    "v_exp_f32_e32 v88, v88\n"
    "v_mfma_f32_16x16x32_bf16 v[112:115], a[200:203], v[40:43], v[112:115]\n"
    "ds_read_b64_tr_b16 a[238:239], v247 offset:12800\n"
    "buffer_load_dwordx4 a[148:151], v255, s[48:51], s91 offen sc1\n"
    "v_mfma_f32_16x16x32_bf16 v[112:115], a[204:207], v[44:47], v[112:115]\n"
    "v_exp_f32_e64 v89, v89\n"
    "v_mfma_f32_16x16x32_bf16 v[116:119], a[208:211], v[32:35], v[200:203]\n"
    "ds_read_b64_tr_b16 a[240:241], v248 offset:9216\n"
    "v_cvt_pk_bf16_f32 v140, v88, v89\n"
    "v_mfma_f32_16x16x32_bf16 v[116:119], a[212:215], v[36:39], v[116:119]\n"
    "ds_read_b64_tr_b16 a[242:243], v248 offset:13312\n"
    "ds_read_b64_tr_b16 a[244:245], v247 offset:9216\n"
    "v_mfma_f32_16x16x32_bf16 v[116:119], a[216:219], v[40:43], v[116:119]\n"
    "v_exp_f32_e64 v90, v90\n"
    "v_mfma_f32_16x16x32_bf16 v[116:119], a[220:223], v[44:47], v[116:119]\n"
    "buffer_load_dwordx4 a[152:155], v255, s[48:51], s91 offen offset:1024 sc1\n"
    "ds_read_b64_tr_b16 a[246:247], v247 offset:13312\n"
    "v_mfma_f32_16x16x32_bf16 v[120:123], a[192:195], v[48:51], v[204:207]\n"
    "v_exp_f32_e64 v91, v91\n"
    "v_mfma_f32_16x16x32_bf16 v[120:123], a[196:199], v[52:55], v[120:123]\n"
    "buffer_load_dwordx4 a[156:159], v255, s[48:51], s91 offen offset:2048 sc1\n"
    "v_cvt_pk_bf16_f32 v141, v90, v91\n"
    "v_mfma_f32_16x16x32_bf16 v[120:123], a[200:203], v[56:59], v[120:123]\n"
    "v_exp_f32_e64 v92, v92\n"
    "v_mfma_f32_16x16x32_bf16 v[120:123], a[204:207], v[60:63], v[120:123]\n"
    "ds_read_b64_tr_b16 a[248:249], v248 offset:9728\n"
    "ds_read_b64_tr_b16 a[250:251], v248 offset:13824\n"
    "v_mfma_f32_16x16x32_bf16 v[124:127], a[208:211], v[48:51], v[204:207]\n"
    "v_exp_f32_e64 v93, v93\n"
    "v_mfma_f32_16x16x32_bf16 v[124:127], a[212:215], v[52:55], v[124:127]\n"
    "ds_read_b64_tr_b16 a[252:253], v247 offset:9728\n"
    "v_cvt_pk_bf16_f32 v142, v92, v93\n"
    "v_mfma_f32_16x16x32_bf16 v[124:127], a[216:219], v[56:59], v[124:127]\n"
    "v_exp_f32_e64 v94, v94\n"
    "v_mfma_f32_16x16x32_bf16 v[124:127], a[220:223], v[60:63], v[124:127]\n"
    "buffer_load_dwordx4 a[160:163], v255, s[48:51], s91 offen offset:3072 sc1\n"
    "ds_read_b64_tr_b16 a[254:255], v247 offset:13824\n"
    "v_exp_f32_e32 v95, v95\n"
    "s_nop 0\n"
    "v_cvt_pk_bf16_f32 v143, v94, v95\n"
    "v_mfma_f32_16x16x32_bf16 v[208:211], v[224:227], v[128:131], v[208:211]\n"
    "s_mov_b32 s85, 0x7000\n"
    "v_exp_f32_e64 v96, v96\n"
    "v_mfma_f32_16x16x32_bf16 v[212:215], v[224:227], v[132:135], v[212:215]\n"
    "buffer_load_dwordx4 v[160:163], v255, s[48:51], s85 offen sc1\n"
    "v_mfma_f32_16x16x32_bf16 v[216:219], v[224:227], v[136:139], v[216:219]\n"
    "v_exp_f32_e64 v97, v97\n"
    "v_mfma_f32_16x16x32_bf16 v[220:223], v[224:227], v[140:143], v[220:223]\n"
    "s_waitcnt lgkmcnt(14)\n"
    "v_cvt_pk_bf16_f32 v144, v96, v97\n"
    "v_mfma_f32_16x16x32_bf16 a[0:3], a[224:227], v[128:131], a[0:3]\n"
    "s_add_u32 s91, s17, 0x1000\n"
    "v_exp_f32_e32 v98, v98\n"
    "v_mfma_f32_16x16x32_bf16 a[4:7], a[224:227], v[132:135], a[4:7]\n"
    "v_exp_f32_e64 v99, v99\n"
    "v_mfma_f32_16x16x32_bf16 a[8:11], a[224:227], v[136:139], a[8:11]\n"
    "buffer_load_dwordx4 a[164:167], v255, s[48:51], s91 offen sc1\n"
    "v_cvt_pk_bf16_f32 v145, v98, v99\n"
    "v_mfma_f32_16x16x32_bf16 a[12:15], a[224:227], v[140:143], a[12:15]\n"
    "s_waitcnt lgkmcnt(12)\n"
    "v_exp_f32_e32 v100, v100\n"
    "v_mfma_f32_16x16x32_bf16 a[28:31], a[228:231], v[140:143], a[28:31]\n"
    "v_exp_f32_e64 v101, v101\n"
    "v_mfma_f32_16x16x32_bf16 a[24:27], a[228:231], v[136:139], a[24:27]\n"
    "ds_read_b64_tr_b16 a[224:225], v248 offset:24576\n"
    "v_cvt_pk_bf16_f32 v146, v100, v101\n"
    "v_mfma_f32_16x16x32_bf16 a[20:23], a[228:231], v[132:135], a[20:23]\n"
    "PG6_%=:\n"
    "s_cbranch_execz PG10_%=\n"
    "v_exp_f32_e64 v102, v102\n"
    "v_mfma_f32_16x16x32_bf16 a[16:19], a[228:231], v[128:131], a[16:19]\n"
    "s_waitcnt lgkmcnt(11)\n"
    "ds_read_b64_tr_b16 a[226:227], v248 offset:28672\n"
    "buffer_load_dwordx4 a[168:171], v255, s[48:51], s91 offen offset:1024 sc1\n"
    "v_mfma_f32_16x16x32_bf16 a[32:35], a[232:235], v[128:131], a[32:35]\n"
    "v_exp_f32_e32 v103, v103\n"
    "v_mfma_f32_16x16x32_bf16 a[36:39], a[232:235], v[132:135], a[36:39]\n"
    "ds_read_b64_tr_b16 a[228:229], v247 offset:24576\n"
    "v_cvt_pk_bf16_f32 v147, v102, v103\n"
    "v_mfma_f32_16x16x32_bf16 a[40:43], a[232:235], v[136:139], a[40:43]\n"
    "v_exp_f32_e64 v104, v104\n"
    "v_mfma_f32_16x16x32_bf16 a[44:47], a[232:235], v[140:143], a[44:47]\n"
    "s_waitcnt lgkmcnt(11)\n"
    "v_exp_f32_e32 v105, v105\n"
    "v_mfma_f32_16x16x32_bf16 a[60:63], a[236:239], v[140:143], a[60:63]\n"
    "ds_read_b64_tr_b16 a[230:231], v247 offset:28672\n"
    "v_cvt_pk_bf16_f32 v148, v104, v105\n"
    "v_mfma_f32_16x16x32_bf16 a[56:59], a[236:239], v[136:139], a[56:59]\n"
    "v_exp_f32_e64 v106, v106\n"
    "v_mfma_f32_16x16x32_bf16 a[52:55], a[236:239], v[132:135], a[52:55]\n"
    "v_exp_f32_e64 v107, v107\n"
    "v_mfma_f32_16x16x32_bf16 a[48:51], a[236:239], v[128:131], a[48:51]\n"
    "s_waitcnt lgkmcnt(10)\n"
    "buffer_load_dwordx4 a[172:175], v255, s[48:51], s91 offen offset:2048 sc1\n"
    "v_cvt_pk_bf16_f32 v149, v106, v107\n"
    "v_mfma_f32_16x16x32_bf16 a[64:67], a[240:243], v[128:131], a[64:67]\n"
    "v_exp_f32_e32 v108, v108\n"
    "v_mfma_f32_16x16x32_bf16 a[68:71], a[240:243], v[132:135], a[68:71]\n"
    "ds_read_b64_tr_b16 a[232:233], v248 offset:25088\n"
    "ds_read_b64_tr_b16 a[234:235], v248 offset:29184\n"
    "v_mfma_f32_16x16x32_bf16 a[72:75], a[240:243], v[136:139], a[72:75]\n"
    "v_exp_f32_e64 v109, v109\n"
    "v_mfma_f32_16x16x32_bf16 a[76:79], a[240:243], v[140:143], a[76:79]\n"
    "s_waitcnt lgkmcnt(10)\n"
    "buffer_load_dwordx4 a[176:179], v255, s[48:51], s91 offen offset:3072 sc1\n"
    "v_cvt_pk_bf16_f32 v150, v108, v109\n"
    "v_mfma_f32_16x16x32_bf16 a[92:95], a[244:247], v[140:143], a[92:95]\n"
    "s_add_u32 s91, s17, 0x2000\n"
    "v_exp_f32_e32 v110, v110\n"
    "v_mfma_f32_16x16x32_bf16 a[88:91], a[244:247], v[136:139], a[88:91]\n"
    "v_exp_f32_e64 v111, v111\n"
    "v_mfma_f32_16x16x32_bf16 a[84:87], a[244:247], v[132:135], a[84:87]\n"
    "ds_read_b64_tr_b16 a[236:237], v247 offset:25088\n"
    "v_cvt_pk_bf16_f32 v151, v110, v111\n"
    "v_mfma_f32_16x16x32_bf16 a[80:83], a[244:247], v[128:131], a[80:83]\n"
    "s_waitcnt lgkmcnt(9)\n"
    "v_exp_f32_e32 v112, v112\n"
    "v_mfma_f32_16x16x32_bf16 a[96:99], a[248:251], v[128:131], a[96:99]\n"
    "ds_read_b64_tr_b16 a[238:239], v247 offset:29184\n"
    "ds_read_b64_tr_b16 a[240:241], v248 offset:25600\n"
    "v_mfma_f32_16x16x32_bf16 a[100:103], a[248:251], v[132:135], a[100:103]\n"
    "v_exp_f32_e64 v113, v113\n"
    "v_mfma_f32_16x16x32_bf16 a[104:107], a[248:251], v[136:139], a[104:107]\n"
    "ds_read_b64_tr_b16 a[242:243], v248 offset:29696\n"
    "v_cvt_pk_bf16_f32 v152, v112, v113\n"
    "v_mfma_f32_16x16x32_bf16 a[108:111], a[248:251], v[140:143], a[108:111]\n"
    "s_waitcnt lgkmcnt(10)\n"
    "v_exp_f32_e32 v114, v114\n"
    "v_mfma_f32_16x16x32_bf16 a[124:127], a[252:255], v[140:143], a[124:127]\n"
    "v_exp_f32_e64 v115, v115\n"
    "v_mfma_f32_16x16x32_bf16 a[120:123], a[252:255], v[136:139], a[120:123]\n"
    "buffer_load_dwordx4 a[180:183], v255, s[48:51], s91 offen sc1\n"
    "v_cvt_pk_bf16_f32 v153, v114, v115\n"
    "v_mfma_f32_16x16x32_bf16 a[116:119], a[252:255], v[132:135], a[116:119]\n"
    "v_exp_f32_e64 v116, v116\n"
    "v_mfma_f32_16x16x32_bf16 a[112:115], a[252:255], v[128:131], a[112:115]\n"
    "v_exp_f32_e32 v117, v117\n"
    "s_nop 0\n"
    "v_cvt_pk_bf16_f32 v154, v116, v117\n"
    "v_exp_f32_e64 v118, v118\n"
    "ds_read_b64_tr_b16 a[244:245], v247 offset:25600\n"
    "ds_read_b64_tr_b16 a[246:247], v247 offset:29696\n"
    "v_exp_f32_e32 v119, v119\n"
    "s_nop 0\n"
    "v_cvt_pk_bf16_f32 v155, v118, v119\n"
    "v_exp_f32_e64 v120, v120\n"
    "buffer_load_dwordx4 a[184:187], v255, s[48:51], s91 offen offset:1024 sc1\n"
    "v_exp_f32_e32 v121, v121\n"
    "s_nop 0\n"
    "v_cvt_pk_bf16_f32 v156, v120, v121\n"
    "ds_read_b64_tr_b16 a[248:249], v248 offset:26112\n"
    "ds_read_b64_tr_b16 a[250:251], v248 offset:30208\n"
    "v_exp_f32_e32 v122, v122\n"
    "v_exp_f32_e32 v123, v123\n"
    "s_nop 0\n"
    "v_cvt_pk_bf16_f32 v157, v122, v123\n"
    "v_exp_f32_e32 v124, v124\n"
    "buffer_load_dwordx4 a[188:191], v255, s[48:51], s91 offen offset:2048 sc1\n"
    "ds_read_b64_tr_b16 a[252:253], v247 offset:26112\n"
    "ds_read_b64_tr_b16 a[254:255], v247 offset:30208\n"
    "v_exp_f32_e32 v125, v125\n"
    "s_nop 0\n"
    "v_cvt_pk_bf16_f32 v158, v124, v125\n"
    "v_exp_f32_e32 v126, v126\n"
    "v_exp_f32_e32 v127, v127\n"
    "s_nop 0\n"
    "v_cvt_pk_bf16_f32 v159, v126, v127\n"
    "s_waitcnt vmcnt(8)\n"
    "v_mfma_f32_16x16x32_bf16 v[208:211], v[224:227], v[144:147], v[208:211]\n"
    "s_lshr_b32 s38, s36, 1\n"
    "buffer_load_dwordx4 v[80:83], v255, s[48:51], s91 offen offset:3072 sc1\n"
    "v_mfma_f32_16x16x32_bf16 v[212:215], v[224:227], v[148:151], v[212:215]\n"
    "s_and_b32 s39, s89, 1\n"
    "v_mfma_f32_16x16x32_bf16 v[216:219], v[224:227], v[152:155], v[216:219]\n"
    "s_add_u32 s91, s17, 0x3000\n"
    "buffer_load_dwordx4 v[84:87], v255, s[48:51], s91 offen sc1\n"
    "v_mfma_f32_16x16x32_bf16 v[220:223], v[224:227], v[156:159], v[220:223]\n"
    "s_waitcnt lgkmcnt(14)\n"
    "s_and_b32 s60, s36, 1\n"
    "v_mfma_f32_16x16x32_bf16 a[0:3], a[224:227], v[144:147], a[0:3]\n"
    "s_lshl_b32 s60, s60, 12\n"
    "buffer_load_dwordx4 v[88:91], v255, s[48:51], s91 offen offset:1024 sc1\n"
    "v_add_u32_e32 v242, s60, v255\n"
    "v_mfma_f32_16x16x32_bf16 a[4:7], a[224:227], v[148:151], a[4:7]\n"
    "v_mfma_f32_16x16x32_bf16 a[8:11], a[224:227], v[152:155], a[8:11]\n"
    "v_mfma_f32_16x16x32_bf16 a[12:15], a[224:227], v[156:159], a[12:15]\n"
    "s_waitcnt lgkmcnt(12)\n"
    "buffer_load_dwordx4 v[92:95], v255, s[48:51], s91 offen offset:2048 sc1\n"
    "v_add_f32_e32 v232, v208, v212\n"
    "v_mfma_f32_16x16x32_bf16 a[28:31], a[228:231], v[156:159], a[28:31]\n"
    "v_add_f32_e32 v233, v216, v220\n"
    "v_add_f32_e32 v232, v232, v233\n"
    "v_mfma_f32_16x16x32_bf16 a[24:27], a[228:231], v[152:155], a[24:27]\n"
    "s_waitcnt lgkmcnt(10)\n"
    "buffer_load_dwordx4 v[96:99], v255, s[48:51], s91 offen offset:3072 sc1\n"
    "v_cmp_ngt_f32_e32 vcc, 0x5f800000, v232\n"
    "v_mfma_f32_16x16x32_bf16 a[20:23], a[228:231], v[148:151], a[20:23]\n"
    "v_mfma_f32_16x16x32_bf16 a[16:19], a[228:231], v[144:147], a[16:19]\n"
    "s_cmp_eq_u64 vcc, 0\n"
    "s_cbranch_scc0 SLOW%=\n"
    "BACK%=:\n"
    "v_rcp_f32_e32 v192, v208\n"
    "v_mfma_f32_16x16x32_bf16 a[32:35], a[232:235], v[144:147], a[32:35]\n"
    "s_add_u32 s91, s17, 0x4000\n"
    "buffer_load_dwordx4 v[100:103], v255, s[48:51], s91 offen sc1\n"
    "v_mfma_f32_16x16x32_bf16 a[36:39], a[232:235], v[148:151], a[36:39]\n"
    "v_rcp_f32_e64 v196, v212\n"
    "v_mfma_f32_16x16x32_bf16 a[40:43], a[232:235], v[152:155], a[40:43]\n"
    "v_rcp_f32_e64 v200, v216\n"
    "v_mfma_f32_16x16x32_bf16 a[44:47], a[232:235], v[156:159], a[44:47]\n"
    "s_waitcnt lgkmcnt(8)\n"
    "v_rcp_f32_e32 v204, v220\n"
    "v_mfma_f32_16x16x32_bf16 a[60:63], a[236:239], v[156:159], a[60:63]\n"
    "v_mov_b32_e32 v193, v192\n"
    "v_mov_b32_e32 v197, v196\n"
    "v_mfma_f32_16x16x32_bf16 a[56:59], a[236:239], v[152:155], a[56:59]\n"
    "v_mov_b32_e32 v201, v200\n"
    "v_mov_b32_e32 v205, v204\n"
    "v_mfma_f32_16x16x32_bf16 a[52:55], a[236:239], v[148:151], a[52:55]\n"
    "s_cmp_eq_u32 s38, s39\n"
    "s_cselect_b64 vcc, -1, 0\n"
    "v_accvgpr_read_b32 v232, a0\n"
    "v_accvgpr_read_b32 v233, a1\n"
    "v_mfma_f32_16x16x32_bf16 a[48:51], a[236:239], v[144:147], a[48:51]\n"
    "s_waitcnt lgkmcnt(6)\n"
    "v_mul_f32_e32 v232, v192, v232\n"
    "v_mul_f32_e64 v233, v192, v233\n"
    "v_mfma_f32_16x16x32_bf16 a[64:67], a[240:243], v[144:147], a[64:67]\n"
    "v_cvt_pk_bf16_f32 v64, v232, v233\n"
    "v_accvgpr_read_b32 v234, a2\n"
    "v_mfma_f32_16x16x32_bf16 a[68:71], a[240:243], v[148:151], a[68:71]\n"
    "v_accvgpr_read_b32 v235, a3\n"
    "v_mul_f32_e64 v234, v192, v234\n"
    "v_mfma_f32_16x16x32_bf16 a[72:75], a[240:243], v[152:155], a[72:75]\n"
    "v_mul_f32_e64 v235, v192, v235\n"
    "v_cvt_pk_bf16_f32 v65, v234, v235\n"
    "v_mfma_f32_16x16x32_bf16 a[76:79], a[240:243], v[156:159], a[76:79]\n"
    "s_waitcnt lgkmcnt(4)\n"
    "v_accvgpr_read_b32 v236, a16\n"
    "v_accvgpr_read_b32 v237, a17\n"
    "v_mfma_f32_16x16x32_bf16 a[92:95], a[244:247], v[156:159], a[92:95]\n"
    "v_mul_f32_e32 v236, v192, v236\n"
    "v_mul_f32_e64 v237, v192, v237\n"
    "v_mfma_f32_16x16x32_bf16 a[88:91], a[244:247], v[152:155], a[88:91]\n"
    "v_cvt_pk_bf16_f32 v66, v236, v237\n"
    "v_accvgpr_read_b32 v238, a18\n"
    "v_mfma_f32_16x16x32_bf16 a[84:87], a[244:247], v[148:151], a[84:87]\n"
    "v_accvgpr_read_b32 v239, a19\n"
    "v_mul_f32_e64 v238, v192, v238\n"
    "v_mfma_f32_16x16x32_bf16 a[80:83], a[244:247], v[144:147], a[80:83]\n"
    "s_waitcnt lgkmcnt(2)\n"
    "v_mul_f32_e32 v239, v192, v239\n"
    "v_cvt_pk_bf16_f32 v67, v238, v239\n"
    "v_mfma_f32_16x16x32_bf16 a[96:99], a[248:251], v[144:147], a[96:99]\n"
    "buffer_store_dwordx4 v[64:67], v255, s[48:51], s72 offen sc1\n"
    "s_cbranch_vccz OW0_%=\n"
    "ds_write_b128 v242, v[64:67] offset:49152\n"
    "OW0_%=:\n"
    "v_mfma_f32_16x16x32_bf16 a[100:103], a[248:251], v[148:151], a[100:103]\n"
    "v_accvgpr_read_b32 v232, a4\n"
    "v_accvgpr_read_b32 v233, a5\n"
    "v_mfma_f32_16x16x32_bf16 a[104:107], a[248:251], v[152:155], a[104:107]\n"
    "v_mul_f32_e32 v232, v196, v232\n"
    "v_mul_f32_e64 v233, v196, v233\n"
    "v_mfma_f32_16x16x32_bf16 a[108:111], a[248:251], v[156:159], a[108:111]\n"
    "s_waitcnt lgkmcnt(0)\n"
    "v_cvt_pk_bf16_f32 v68, v232, v233\n"
    "v_accvgpr_read_b32 v234, a6\n"
    "v_mfma_f32_16x16x32_bf16 a[124:127], a[252:255], v[156:159], a[124:127]\n"
    "v_accvgpr_read_b32 v235, a7\n"
    "v_mul_f32_e32 v234, v196, v234\n"
    "v_mfma_f32_16x16x32_bf16 a[120:123], a[252:255], v[152:155], a[120:123]\n"
    "s_waitcnt lgkmcnt(0)\n"
    "s_barrier\n"
    "v_mul_f32_e64 v235, v196, v235\n"
    "v_cvt_pk_bf16_f32 v69, v234, v235\n"
    "v_mfma_f32_16x16x32_bf16 a[116:119], a[252:255], v[148:151], a[116:119]\n"
    "v_accvgpr_read_b32 v236, a20\n"
    "v_accvgpr_read_b32 v237, a21\n"
    "v_mfma_f32_16x16x32_bf16 a[112:115], a[252:255], v[144:147], a[112:115]\n"
    "v_mul_f32_e32 v236, v196, v236\n"
    "v_mul_f32_e32 v237, v196, v237\n"
    "v_cvt_pk_bf16_f32 v70, v236, v237\n"
    "v_accvgpr_read_b32 v238, a22\n"
    "v_accvgpr_read_b32 v239, a23\n"
    "v_mul_f32_e32 v238, v196, v238\n"
    "v_mul_f32_e32 v239, v196, v239\n"
    "v_cvt_pk_bf16_f32 v71, v238, v239\n"
    "buffer_store_dwordx4 v[68:71], v255, s[48:51], s72 offen offset:1024 sc1\n"
    "s_cbranch_vccz OW1_%=\n"
    "ds_write_b128 v242, v[68:71] offset:50176\n"
    "OW1_%=:\n"
    "v_accvgpr_read_b32 v232, a8\n"
    "v_accvgpr_read_b32 v233, a9\n"
    "v_mul_f32_e32 v232, v200, v232\n"
    "v_mul_f32_e64 v233, v200, v233\n"
    "v_cvt_pk_bf16_f32 v72, v232, v233\n"
    "v_accvgpr_read_b32 v234, a10\n"
    "v_accvgpr_read_b32 v235, a11\n"
    "v_mul_f32_e32 v234, v200, v234\n"
    "v_mul_f32_e32 v235, v200, v235\n"
    "v_cvt_pk_bf16_f32 v73, v234, v235\n"
    "v_accvgpr_read_b32 v236, a24\n"
    "v_accvgpr_read_b32 v237, a25\n"
    "v_mul_f32_e32 v236, v200, v236\n"
    "v_mul_f32_e32 v237, v200, v237\n"
    "v_cvt_pk_bf16_f32 v74, v236, v237\n"
    "v_accvgpr_read_b32 v238, a26\n"
    "v_accvgpr_read_b32 v239, a27\n"
    "v_mul_f32_e32 v238, v200, v238\n"
    "v_mul_f32_e32 v239, v200, v239\n"
    "v_cvt_pk_bf16_f32 v75, v238, v239\n"
    "buffer_store_dwordx4 v[72:75], v255, s[48:51], s72 offen offset:2048 sc1\n"
    "s_cbranch_vccz OW2_%=\n"
    "ds_write_b128 v242, v[72:75] offset:51200\n"
    "OW2_%=:\n"
    "v_accvgpr_read_b32 v232, a12\n"
    "v_accvgpr_read_b32 v233, a13\n"
    "v_mul_f32_e32 v232, v204, v232\n"
    "v_mul_f32_e64 v233, v204, v233\n"
    "v_cvt_pk_bf16_f32 v76, v232, v233\n"
    "v_accvgpr_read_b32 v234, a14\n"
    "v_accvgpr_read_b32 v235, a15\n"
    "v_mul_f32_e32 v234, v204, v234\n"
    "v_mul_f32_e32 v235, v204, v235\n"
    "v_cvt_pk_bf16_f32 v77, v234, v235\n"
    "v_accvgpr_read_b32 v236, a28\n"
    "v_accvgpr_read_b32 v237, a29\n"
    "v_mul_f32_e32 v236, v204, v236\n"
    "v_mul_f32_e32 v237, v204, v237\n"
    "v_cvt_pk_bf16_f32 v78, v236, v237\n"
    "v_accvgpr_read_b32 v238, a30\n"
    "v_accvgpr_read_b32 v239, a31\n"
    "v_mul_f32_e32 v238, v204, v238\n"
    "v_mul_f32_e32 v239, v204, v239\n"
    "v_cvt_pk_bf16_f32 v79, v238, v239\n"
    "buffer_store_dwordx4 v[76:79], v255, s[48:51], s72 offen offset:3072 sc1\n"
    "s_cbranch_vccz OW3_%=\n"
    "ds_write_b128 v242, v[76:79] offset:52224\n"
    "OW3_%=:\n"
    "s_add_u32 s72, s72, 0x1000\n"
    "v_accvgpr_read_b32 v232, a32\n"
    "v_accvgpr_read_b32 v233, a33\n"
    "v_pk_mul_f32 v[232:233], v[232:233], v[192:193]\n"
    "v_cvt_pk_bf16_f32 v64, v232, v233\n"
    "v_accvgpr_read_b32 v234, a34\n"
    "v_accvgpr_read_b32 v235, a35\n"
    "v_pk_mul_f32 v[234:235], v[234:235], v[192:193]\n"
    "v_cvt_pk_bf16_f32 v65, v234, v235\n"
    "v_accvgpr_read_b32 v236, a48\n"
    "v_accvgpr_read_b32 v237, a49\n"
    "v_pk_mul_f32 v[236:237], v[236:237], v[192:193]\n"
    "v_cvt_pk_bf16_f32 v66, v236, v237\n"
    "v_accvgpr_read_b32 v238, a50\n"
    "v_accvgpr_read_b32 v239, a51\n"
    "v_pk_mul_f32 v[238:239], v[238:239], v[192:193]\n"
    "v_cvt_pk_bf16_f32 v67, v238, v239\n"
    "buffer_store_dwordx4 v[64:67], v255, s[48:51], s72 offen sc1\n"
    "s_cbranch_vccz OW4_%=\n"
    "ds_write_b128 v242, v[64:67] offset:32768\n"
    "OW4_%=:\n"
    "v_accvgpr_read_b32 v232, a36\n"
    "v_accvgpr_read_b32 v233, a37\n"
    "v_pk_mul_f32 v[232:233], v[232:233], v[196:197]\n"
    "v_cvt_pk_bf16_f32 v68, v232, v233\n"
    "v_accvgpr_read_b32 v234, a38\n"
    "v_accvgpr_read_b32 v235, a39\n"
    "v_pk_mul_f32 v[234:235], v[234:235], v[196:197]\n"
    "v_cvt_pk_bf16_f32 v69, v234, v235\n"
    "v_accvgpr_read_b32 v236, a52\n"
    "v_accvgpr_read_b32 v237, a53\n"
    "v_pk_mul_f32 v[236:237], v[236:237], v[196:197]\n"
    "v_cvt_pk_bf16_f32 v70, v236, v237\n"
    "v_accvgpr_read_b32 v238, a54\n"
    "v_accvgpr_read_b32 v239, a55\n"
    "v_pk_mul_f32 v[238:239], v[238:239], v[196:197]\n"
    "v_cvt_pk_bf16_f32 v71, v238, v239\n"
    "buffer_store_dwordx4 v[68:71], v255, s[48:51], s72 offen offset:1024 sc1\n"
    "s_cbranch_vccz OW5_%=\n"
    "ds_write_b128 v242, v[68:71] offset:33792\n"
    "OW5_%=:\n"
    "v_accvgpr_read_b32 v232, a40\n"
    "v_accvgpr_read_b32 v233, a41\n"
    "v_pk_mul_f32 v[232:233], v[232:233], v[200:201]\n"
    "v_cvt_pk_bf16_f32 v72, v232, v233\n"
    "v_accvgpr_read_b32 v234, a42\n"
    "v_accvgpr_read_b32 v235, a43\n"
    "v_pk_mul_f32 v[234:235], v[234:235], v[200:201]\n"
    "v_cvt_pk_bf16_f32 v73, v234, v235\n"
    "v_accvgpr_read_b32 v236, a56\n"
    "v_accvgpr_read_b32 v237, a57\n"
    "v_pk_mul_f32 v[236:237], v[236:237], v[200:201]\n"
    "v_cvt_pk_bf16_f32 v74, v236, v237\n"
    "v_accvgpr_read_b32 v238, a58\n"
    "v_accvgpr_read_b32 v239, a59\n"
    "v_pk_mul_f32 v[238:239], v[238:239], v[200:201]\n"
    "v_cvt_pk_bf16_f32 v75, v238, v239\n"
    "buffer_store_dwordx4 v[72:75], v255, s[48:51], s72 offen offset:2048 sc1\n"
    "s_cbranch_vccz OW6_%=\n"
    "ds_write_b128 v242, v[72:75] offset:34816\n"
    "OW6_%=:\n"
    "v_accvgpr_read_b32 v232, a44\n"
    "v_accvgpr_read_b32 v233, a45\n"
    "v_pk_mul_f32 v[232:233], v[232:233], v[204:205]\n"
    "v_cvt_pk_bf16_f32 v76, v232, v233\n"
    "v_accvgpr_read_b32 v234, a46\n"
    "v_accvgpr_read_b32 v235, a47\n"
    "v_pk_mul_f32 v[234:235], v[234:235], v[204:205]\n"
    "v_cvt_pk_bf16_f32 v77, v234, v235\n"
    "v_accvgpr_read_b32 v236, a60\n"
    "v_accvgpr_read_b32 v237, a61\n"
    "v_pk_mul_f32 v[236:237], v[236:237], v[204:205]\n"
    "v_cvt_pk_bf16_f32 v78, v236, v237\n"
    "v_accvgpr_read_b32 v238, a62\n"
    "v_accvgpr_read_b32 v239, a63\n"
    "v_pk_mul_f32 v[238:239], v[238:239], v[204:205]\n"
    "v_cvt_pk_bf16_f32 v79, v238, v239\n"
    "buffer_store_dwordx4 v[76:79], v255, s[48:51], s72 offen offset:3072 sc1\n"
    "s_cbranch_vccz OW7_%=\n"
    "ds_write_b128 v242, v[76:79] offset:35840\n"
    "OW7_%=:\n"
    "s_add_u32 s72, s72, 0x1000\n"
    "v_accvgpr_read_b32 v232, a64\n"
    "v_accvgpr_read_b32 v233, a65\n"
    "v_pk_mul_f32 v[232:233], v[232:233], v[192:193]\n"
    "v_cvt_pk_bf16_f32 v64, v232, v233\n"
    "v_accvgpr_read_b32 v234, a66\n"
    "v_accvgpr_read_b32 v235, a67\n"
    "v_pk_mul_f32 v[234:235], v[234:235], v[192:193]\n"
    "v_cvt_pk_bf16_f32 v65, v234, v235\n"
    "v_accvgpr_read_b32 v236, a80\n"
    "v_accvgpr_read_b32 v237, a81\n"
    "v_pk_mul_f32 v[236:237], v[236:237], v[192:193]\n"
    "v_cvt_pk_bf16_f32 v66, v236, v237\n"
    "v_accvgpr_read_b32 v238, a82\n"
    "v_accvgpr_read_b32 v239, a83\n"
    "v_pk_mul_f32 v[238:239], v[238:239], v[192:193]\n"
    "v_cvt_pk_bf16_f32 v67, v238, v239\n"
    "buffer_store_dwordx4 v[64:67], v255, s[48:51], s72 offen sc1\n"
    "s_cbranch_vccz OW8_%=\n"
    "ds_write_b128 v242, v[64:67] offset:40960\n"
    "OW8_%=:\n"
    "v_accvgpr_read_b32 v232, a68\n"
    "v_accvgpr_read_b32 v233, a69\n"
    "v_pk_mul_f32 v[232:233], v[232:233], v[196:197]\n"
    "v_cvt_pk_bf16_f32 v68, v232, v233\n"
    "v_accvgpr_read_b32 v234, a70\n"
    "v_accvgpr_read_b32 v235, a71\n"
    "v_pk_mul_f32 v[234:235], v[234:235], v[196:197]\n"
    "v_cvt_pk_bf16_f32 v69, v234, v235\n"
    "v_accvgpr_read_b32 v236, a84\n"
    "v_accvgpr_read_b32 v237, a85\n"
    "v_pk_mul_f32 v[236:237], v[236:237], v[196:197]\n"
    "v_cvt_pk_bf16_f32 v70, v236, v237\n"
    "v_accvgpr_read_b32 v238, a86\n"
    "v_accvgpr_read_b32 v239, a87\n"
    "v_pk_mul_f32 v[238:239], v[238:239], v[196:197]\n"
    "v_cvt_pk_bf16_f32 v71, v238, v239\n"
    "buffer_store_dwordx4 v[68:71], v255, s[48:51], s72 offen offset:1024 sc1\n"
    "s_cbranch_vccz OW9_%=\n"
    "ds_write_b128 v242, v[68:71] offset:41984\n"
    "OW9_%=:\n"
    "v_accvgpr_read_b32 v232, a72\n"
    "v_accvgpr_read_b32 v233, a73\n"
    "v_pk_mul_f32 v[232:233], v[232:233], v[200:201]\n"
    "v_cvt_pk_bf16_f32 v72, v232, v233\n"
    "v_accvgpr_read_b32 v234, a74\n"
    "v_accvgpr_read_b32 v235, a75\n"
    "v_pk_mul_f32 v[234:235], v[234:235], v[200:201]\n"
    "v_cvt_pk_bf16_f32 v73, v234, v235\n"
    "v_accvgpr_read_b32 v236, a88\n"
    "v_accvgpr_read_b32 v237, a89\n"
    "v_pk_mul_f32 v[236:237], v[236:237], v[200:201]\n"
    "v_cvt_pk_bf16_f32 v74, v236, v237\n"
    "v_accvgpr_read_b32 v238, a90\n"
    "v_accvgpr_read_b32 v239, a91\n"
    "v_pk_mul_f32 v[238:239], v[238:239], v[200:201]\n"
    "v_cvt_pk_bf16_f32 v75, v238, v239\n"
    "buffer_store_dwordx4 v[72:75], v255, s[48:51], s72 offen offset:2048 sc1\n"
    "s_cbranch_vccz OW10_%=\n"
    "ds_write_b128 v242, v[72:75] offset:43008\n"
    "OW10_%=:\n"
    "v_accvgpr_read_b32 v232, a76\n"
    "v_accvgpr_read_b32 v233, a77\n"
    "v_pk_mul_f32 v[232:233], v[232:233], v[204:205]\n"
    "v_cvt_pk_bf16_f32 v76, v232, v233\n"
    "v_accvgpr_read_b32 v234, a78\n"
    "v_accvgpr_read_b32 v235, a79\n"
    "v_pk_mul_f32 v[234:235], v[234:235], v[204:205]\n"
    "v_cvt_pk_bf16_f32 v77, v234, v235\n"
    "v_accvgpr_read_b32 v236, a92\n"
    "v_accvgpr_read_b32 v237, a93\n"
    "v_pk_mul_f32 v[236:237], v[236:237], v[204:205]\n"
    "v_cvt_pk_bf16_f32 v78, v236, v237\n"
    "v_accvgpr_read_b32 v238, a94\n"
    "v_accvgpr_read_b32 v239, a95\n"
    "v_pk_mul_f32 v[238:239], v[238:239], v[204:205]\n"
    "v_cvt_pk_bf16_f32 v79, v238, v239\n"
    "buffer_store_dwordx4 v[76:79], v255, s[48:51], s72 offen offset:3072 sc1\n"
    "s_cbranch_vccz OW11_%=\n"
    "ds_write_b128 v242, v[76:79] offset:44032\n"
    "OW11_%=:\n"
    "s_add_u32 s72, s72, 0x1000\n"
    "v_accvgpr_read_b32 v232, a96\n"
    "v_accvgpr_read_b32 v233, a97\n"
    "v_pk_mul_f32 v[232:233], v[232:233], v[192:193]\n"
    "v_cvt_pk_bf16_f32 v64, v232, v233\n"
    "v_accvgpr_read_b32 v234, a98\n"
    "v_accvgpr_read_b32 v235, a99\n"
    "v_pk_mul_f32 v[234:235], v[234:235], v[192:193]\n"
    "v_cvt_pk_bf16_f32 v65, v234, v235\n"
    "v_accvgpr_read_b32 v236, a112\n"
    "v_accvgpr_read_b32 v237, a113\n"
    "v_pk_mul_f32 v[236:237], v[236:237], v[192:193]\n"
    "v_cvt_pk_bf16_f32 v66, v236, v237\n"
    "v_accvgpr_read_b32 v238, a114\n"
    "v_accvgpr_read_b32 v239, a115\n"
    "v_pk_mul_f32 v[238:239], v[238:239], v[192:193]\n"
    "v_cvt_pk_bf16_f32 v67, v238, v239\n"
    "buffer_store_dwordx4 v[64:67], v255, s[48:51], s72 offen sc1\n"
    "s_cbranch_vccz OW12_%=\n"
    "ds_write_b128 v242, v[64:67] offset:57344\n"
    "OW12_%=:\n"
    "v_accvgpr_read_b32 v232, a100\n"
    "v_accvgpr_read_b32 v233, a101\n"
    "v_pk_mul_f32 v[232:233], v[232:233], v[196:197]\n"
    "v_cvt_pk_bf16_f32 v68, v232, v233\n"
    "v_accvgpr_read_b32 v234, a102\n"
    "v_accvgpr_read_b32 v235, a103\n"
    "v_pk_mul_f32 v[234:235], v[234:235], v[196:197]\n"
    "v_cvt_pk_bf16_f32 v69, v234, v235\n"
    "v_accvgpr_read_b32 v236, a116\n"
    "v_accvgpr_read_b32 v237, a117\n"
    "v_pk_mul_f32 v[236:237], v[236:237], v[196:197]\n"
    "v_cvt_pk_bf16_f32 v70, v236, v237\n"
    "v_accvgpr_read_b32 v238, a118\n"
    "v_accvgpr_read_b32 v239, a119\n"
    "v_pk_mul_f32 v[238:239], v[238:239], v[196:197]\n"
    "v_cvt_pk_bf16_f32 v71, v238, v239\n"
    "buffer_store_dwordx4 v[68:71], v255, s[48:51], s72 offen offset:1024 sc1\n"
    "s_cbranch_vccz OW13_%=\n"
    "ds_write_b128 v242, v[68:71] offset:58368\n"
    "OW13_%=:\n"
    "v_accvgpr_read_b32 v232, a104\n"
    "v_accvgpr_read_b32 v233, a105\n"
    "v_pk_mul_f32 v[232:233], v[232:233], v[200:201]\n"
    "v_cvt_pk_bf16_f32 v72, v232, v233\n"
    "v_accvgpr_read_b32 v234, a106\n"
    "v_accvgpr_read_b32 v235, a107\n"
    "v_pk_mul_f32 v[234:235], v[234:235], v[200:201]\n"
    "v_cvt_pk_bf16_f32 v73, v234, v235\n"
    "v_accvgpr_read_b32 v236, a120\n"
    "v_accvgpr_read_b32 v237, a121\n"
    "v_pk_mul_f32 v[236:237], v[236:237], v[200:201]\n"
    "v_cvt_pk_bf16_f32 v74, v236, v237\n"
    "v_accvgpr_read_b32 v238, a122\n"
    "v_accvgpr_read_b32 v239, a123\n"
    "v_pk_mul_f32 v[238:239], v[238:239], v[200:201]\n"
    "v_cvt_pk_bf16_f32 v75, v238, v239\n"
    "buffer_store_dwordx4 v[72:75], v255, s[48:51], s72 offen offset:2048 sc1\n"
    "s_cbranch_vccz OW14_%=\n"
    "ds_write_b128 v242, v[72:75] offset:59392\n"
    "OW14_%=:\n"
    "v_accvgpr_read_b32 v232, a108\n"
    "v_accvgpr_read_b32 v233, a109\n"
    "v_pk_mul_f32 v[232:233], v[232:233], v[204:205]\n"
    "v_cvt_pk_bf16_f32 v76, v232, v233\n"
    "v_accvgpr_read_b32 v234, a110\n"
    "v_accvgpr_read_b32 v235, a111\n"
    "v_pk_mul_f32 v[234:235], v[234:235], v[204:205]\n"
    "v_cvt_pk_bf16_f32 v77, v234, v235\n"
    "v_accvgpr_read_b32 v236, a124\n"
    "v_accvgpr_read_b32 v237, a125\n"
    "v_pk_mul_f32 v[236:237], v[236:237], v[204:205]\n"
    "v_cvt_pk_bf16_f32 v78, v236, v237\n"
    "v_accvgpr_read_b32 v238, a126\n"
    "v_accvgpr_read_b32 v239, a127\n"
    "v_pk_mul_f32 v[238:239], v[238:239], v[204:205]\n"
    "v_cvt_pk_bf16_f32 v79, v238, v239\n"
    "buffer_store_dwordx4 v[76:79], v255, s[48:51], s72 offen offset:3072 sc1\n"
    "s_cbranch_vccz OW15_%=\n"
    "ds_write_b128 v242, v[76:79] offset:60416\n"
    "OW15_%=:\n"
    "s_add_u32 s72, s72, 0x1000\n"
    "s_lshr_b32 s38, s36, 1\n"
    "s_lshl_b32 s38, s38, 12\n"
    "v_add_u32_e32 v244, s38, v255\n"
    "s_and_b32 s38, s36, 1\n"
    "s_lshl_b32 s38, s38, 12\n"
    "v_add_u32_e64 v252, s38, v255\n"
    "v_add_u32_e32 v245, 0x10000, v244\n"
    "v_add_u32_e32 v246, 0x20000, v244\n"
    "v_add_u32_e32 v253, 0x10000, v252\n"
    "v_add_u32_e32 v254, 0x20000, v252\n"
    "s_waitcnt vmcnt(29)\n"
    "v_xor_b32_e32 v161, v160, v161\n"
    "v_xor_b32_e32 v162, v160, v162\n"
    "v_xor_b32_e32 v163, v160, v163\n"
    "v_xor_b32_e64 v160, s78, v160\n"
    "v_or3_b32 v160, v160, v161, v162\n"
    "v_or_b32_e32 v160, v160, v163\n"
    "v_cmp_eq_u32_e32 vcc, 0, v160\n"
    "s_nop 1\n"
    "s_cmp_eq_u64 vcc, -1\n"
    "s_cbranch_scc1 POK3_%=\n"
    "s_mov_b32 s61, 20000\n"
    "PSP3_%=:\n"
    "s_sleep 4\n"
    "buffer_load_dwordx4 v[160:163], v255, s[48:51], s85 offen sc1\n"
    "s_waitcnt vmcnt(0)\n"
    "v_xor_b32_e64 v161, v160, v161\n"
    "v_xor_b32_e64 v162, v160, v162\n"
    "v_xor_b32_e64 v163, v160, v163\n"
    "v_xor_b32_e64 v160, s78, v160\n"
    "v_or3_b32 v160, v160, v161, v162\n"
    "v_or_b32_e64 v160, v160, v163\n"
    "v_cmp_eq_u32_e32 vcc, 0, v160\n"
    "s_nop 1\n"
    "s_cmp_eq_u64 vcc, -1\n"
    "s_cbranch_scc1 POK3_%=\n"
    "s_sub_u32 s61, s61, 1\n"
    "s_cmp_lg_u32 s61, 0\n"
    "s_cbranch_scc1 PSP3_%=\n"
    "POK3_%=:\n"
    "ds_read_b128 v[20:23], v245 offset:0\n"
    "ds_read_b128 v[24:27], v245 offset:1024\n"
    "ds_read_b128 v[28:31], v245 offset:2048\n"
    "ds_read_b128 v[32:35], v245 offset:3072\n"
    "ds_read_b128 v[36:39], v245 offset:8192\n"
    "ds_read_b128 v[40:43], v245 offset:9216\n"
    "ds_read_b128 v[44:47], v245 offset:10240\n"
    "ds_read_b128 v[48:51], v245 offset:11264\n"
    "ds_read_b128 v[52:55], v245 offset:16384\n"
    "ds_read_b128 v[56:59], v245 offset:17408\n"
    "s_waitcnt lgkmcnt(0)\n"
    "s_barrier\n"
    "ds_read_b128 v[136:139], v252 offset:49152\n"
    "ds_read_b128 v[152:155], v252 offset:32768\n"
    "ds_read_b128 v[140:143], v252 offset:50176\n"
    "ds_read_b128 v[156:159], v252 offset:33792\n"
    "ds_read_b128 v[144:147], v252 offset:51200\n"
    "ds_read_b128 v[160:163], v252 offset:34816\n"
    "ds_read_b128 v[148:151], v252 offset:52224\n"
    "ds_read_b128 v[192:195], v252 offset:35840\n"
    "s_waitcnt lgkmcnt(7)\n"
    "v_mfma_f32_16x16x32_bf16 a[0:3], v[20:23], v[136:139], 0\n"
    "s_waitcnt lgkmcnt(6)\n"
    "s_lshl_b32 s75, s89, 1\n"
    "ds_read_b128 v[60:63], v245 offset:18432\n"
    "ds_read_b128 v[64:67], v245 offset:19456\n"
    "v_mfma_f32_16x16x32_bf16 a[0:3], v[40:43], v[152:155], a[0:3]\n"
    "s_lshr_b32 s38, s90, 2\n"
    "ds_read_b128 v[68:71], v245 offset:24576\n"
    "v_mfma_f32_16x16x32_bf16 a[4:7], v[24:27], v[136:139], 0\n"
    "s_and_b32 s39, s38, 1\n"
    "ds_read_b128 v[72:75], v245 offset:25600\n"
    "v_mfma_f32_16x16x32_bf16 a[4:7], v[44:47], v[152:155], a[4:7]\n"
    "s_add_u32 s75, s75, s39\n"
    "ds_read_b128 v[76:79], v245 offset:26624\n"
    "ds_read_b128 v[116:119], v245 offset:27648\n"
    "v_mfma_f32_16x16x32_bf16 a[8:11], v[28:31], v[136:139], 0\n"
    "s_lshr_b32 s38, s38, 1\n"
    "ds_read_b128 v[120:123], v245 offset:32768\n"
    "v_mfma_f32_16x16x32_bf16 a[8:11], v[48:51], v[152:155], a[8:11]\n"
    "s_lshl_b32 s38, s38, 2\n"
    "ds_read_b128 v[124:127], v245 offset:33792\n"
    "PG7_%=:\n"
    "s_cbranch_execz PG11_%=\n"
    "v_mfma_f32_16x16x32_bf16 a[12:15], v[32:35], v[136:139], 0\n"
    "s_and_b32 s39, s90, 3\n"
    "ds_read_b128 v[128:131], v245 offset:34816\n"
    "v_mfma_f32_16x16x32_bf16 a[12:15], v[52:55], v[152:155], a[12:15]\n"
    "s_add_u32 s86, s38, s39\n"
    "ds_read_b128 v[132:135], v245 offset:35840\n"
    "ds_read_b128 v[196:199], v252 offset:40960\n"
    "v_mfma_f32_16x16x32_bf16 a[16:19], v[36:39], v[136:139], 0\n"
    "s_lshl_b32 s38, s86, 2\n"
    "ds_read_b128 v[212:215], v252 offset:57344\n"
    "v_mfma_f32_16x16x32_bf16 a[16:19], v[56:59], v[152:155], a[16:19]\n"
    "s_waitcnt lgkmcnt(15)\n"
    "s_add_u32 s38, s38, s36\n"
    "ds_read_b128 v[200:203], v252 offset:41984\n"
    "v_mfma_f32_16x16x32_bf16 a[20:23], v[20:23], v[140:143], 0\n"
    "s_mul_i32 s94, s38, 0x11800\n"
    "s_add_u32 s94, s94, 0x2c00000\n"
    "ds_read_b128 a[60:63], v252 offset:58368\n"
    "v_mfma_f32_16x16x32_bf16 a[20:23], v[40:43], v[156:159], a[20:23]\n"
    "s_mul_i32 s62, s90, 0x46000\n"
    "ds_read_b128 v[204:207], v252 offset:43008\n"
    "ds_read_b128 a[64:67], v252 offset:59392\n"
    "v_mfma_f32_16x16x32_bf16 a[24:27], v[24:27], v[140:143], 0\n"
    "s_add_u32 s62, s62, s68\n"
    "ds_read_b128 v[208:211], v252 offset:44032\n"
    "v_mfma_f32_16x16x32_bf16 a[24:27], v[44:47], v[156:159], a[24:27]\n"
    "s_add_u32 s62, s62, 0x1f00000\n"
    "ds_read_b128 a[68:71], v252 offset:60416\n"
    "v_mfma_f32_16x16x32_bf16 a[28:31], v[28:31], v[140:143], 0\n"
    "s_mov_b32 s38, 0x8000\n"
    "s_cmp_eq_u32 s37, 0\n"
    "s_cselect_b32 s38, s38, 0x40000000\n"
    "v_add_u32_e32 v231, 4, v230\n"
    "v_mfma_f32_16x16x32_bf16 a[28:31], v[48:51], v[156:159], a[28:31]\n"
    "ds_read_b128 a[192:195], v245 offset:40960\n"
    "buffer_store_dword v231, v241, s[48:51], s38 offen sc1\n"
    "v_mfma_f32_16x16x32_bf16 a[32:35], v[32:35], v[140:143], 0\n"
    "s_lshl_b32 s38, s89, 1\n"
    "ds_read_b128 a[196:199], v245 offset:41984\n"
    "ds_read_b128 a[200:203], v245 offset:43008\n"
    "v_mfma_f32_16x16x32_bf16 a[32:35], v[52:55], v[156:159], a[32:35]\n"
    "s_lshr_b32 s39, s36, 1\n"
    "ds_read_b128 a[204:207], v245 offset:44032\n"
    "ds_read_b128 a[208:211], v245 offset:49152\n"
    "v_mfma_f32_16x16x32_bf16 a[36:39], v[36:39], v[140:143], 0\n"
    "s_add_u32 s38, s38, s39\n"
    "ds_read_b128 a[212:215], v245 offset:50176\n"
    "v_mfma_f32_16x16x32_bf16 a[36:39], v[56:59], v[156:159], a[36:39]\n"
    "s_lshl_b32 s38, s38, 2\n"
    "ds_read_b128 a[216:219], v245 offset:51200\n"
    "ds_read_b128 a[220:223], v245 offset:52224\n"
    "v_mfma_f32_16x16x32_bf16 a[40:43], v[20:23], v[144:147], 0\n"
    "s_waitcnt lgkmcnt(15)\n"
    "s_lshr_b32 s39, s90, 2\n"
    "ds_read_b128 a[224:227], v245 offset:57344\n"
    "v_mfma_f32_16x16x32_bf16 a[40:43], v[40:43], v[160:163], a[40:43]\n"
    "s_add_u32 s38, s38, s39\n"
    "ds_read_b128 a[228:231], v245 offset:58368\n"
    "v_mfma_f32_16x16x32_bf16 a[44:47], v[24:27], v[144:147], 0\n"
    "s_lshr_b32 s63, s89, 1\n"
    "s_lshl_b32 s63, s63, 2\n"
    "ds_read_b128 a[232:235], v245 offset:59392\n"
    "v_mfma_f32_16x16x32_bf16 a[44:47], v[44:47], v[160:163], a[44:47]\n"
    "s_add_u32 s63, s63, s39\n"
    "ds_read_b128 a[236:239], v245 offset:60416\n"
    "ds_read_b128 a[240:243], v246 offset:0\n"
    "v_mfma_f32_16x16x32_bf16 a[48:51], v[28:31], v[144:147], 0\n"
    "s_lshl_b32 s63, s63, 11\n"
    "ds_read_b128 a[244:247], v246 offset:1024\n"
    "v_mfma_f32_16x16x32_bf16 a[48:51], v[48:51], v[160:163], a[48:51]\n"
    "s_add_u32 s63, s63, 0x30000\n"
    "ds_read_b128 a[248:251], v246 offset:2048\n"
    "v_mfma_f32_16x16x32_bf16 a[52:55], v[32:35], v[144:147], 0\n"
    "s_lshl_b32 s88, s38, 17\n"
    "ds_read_b128 a[252:255], v246 offset:3072\n"
    "ds_read_b128 a[72:75], v246 offset:8192\n"
    "v_mfma_f32_16x16x32_bf16 a[52:55], v[52:55], v[160:163], a[52:55]\n"
    "s_and_b32 s39, s36, 1\n"
    "s_and_b32 s67, s73, 7\n"
    "ds_read_b128 a[76:79], v246 offset:9216\n"
    "v_mfma_f32_16x16x32_bf16 a[56:59], v[36:39], v[144:147], 0\n"
    "s_lshl_b32 s39, s39, 11\n"
    "ds_read_b128 a[80:83], v246 offset:10240\n"
    "v_mfma_f32_16x16x32_bf16 a[56:59], v[56:59], v[160:163], a[56:59]\n"
    "s_add_u32 s88, s88, s39\n"
    "ds_read_b128 a[84:87], v246 offset:11264\n"
    "v_mfma_f32_16x16x32_bf16 v[0:3], v[20:23], v[148:151], 0\n"
    "s_add_u32 s88, s88, 0x400000\n"
    "v_bfe_u32 v221, v255, 4, 2\n"
    "v_mul_u32_u24_e32 v220, 0x2800, v221\n"
    "v_mfma_f32_16x16x32_bf16 v[0:3], v[40:43], v[192:195], v[0:3]\n"
    "ds_read_b128 a[88:91], v246 offset:16384\n"
    "v_lshrrev_b32_e64 v222, 6, v255\n"
    "v_mfma_f32_16x16x32_bf16 v[4:7], v[24:27], v[148:151], 0\n"
    "ds_read_b128 a[92:95], v246 offset:17408\n"
    "v_lshl_add_u32 v220, v222, 2, v220\n"
    "v_mfma_f32_16x16x32_bf16 v[4:7], v[44:47], v[192:195], v[4:7]\n"
    "s_mul_i32 s82, s37, 40\n"
    "ds_read_b128 a[96:99], v246 offset:18432\n"
    "ds_read_b128 a[100:103], v246 offset:19456\n"
    "v_mfma_f32_16x16x32_bf16 v[8:11], v[28:31], v[148:151], 0\n"
    "s_add_u32 s83, s82, 0x18000\n"
    "ds_read_b128 a[104:107], v246 offset:24576\n"
    "v_or_b32_e32 v221, s36, v221\n"
    "v_mfma_f32_16x16x32_bf16 v[8:11], v[48:51], v[192:195], v[8:11]\n"
    "ds_read_b128 a[108:111], v246 offset:25600\n"
    "v_lshl_add_u32 v221, v221, 4, v222\n"
    "v_mfma_f32_16x16x32_bf16 v[12:15], v[32:35], v[148:151], 0\n"
    "ds_read_b128 a[112:115], v246 offset:26624\n"
    "v_cmp_gt_u32_e32 vcc, 10, v221\n"
    "v_mfma_f32_16x16x32_bf16 v[12:15], v[52:55], v[192:195], v[12:15]\n"
    "ds_read_b128 a[116:119], v246 offset:27648\n"
    "v_lshlrev_b32_e32 v221, 2, v222\n"
    "v_mfma_f32_16x16x32_bf16 v[16:19], v[36:39], v[148:151], 0\n"
    "ds_read_b128 a[120:123], v244 offset:0\n"
    "buffer_load_dword v222, v220, s[48:51], s83 offen sc1\n"
    "v_mfma_f32_16x16x32_bf16 v[16:19], v[56:59], v[192:195], v[16:19]\n"
    "ds_read_b128 a[124:127], v244 offset:1024\n"
    "v_mov_b32_e32 v220, 0x40000000\n"
    "v_mfma_f32_16x16x32_bf16 a[0:3], v[60:63], v[196:199], a[0:3]\n"
    "s_waitcnt lgkmcnt(15)\n"
    "v_cndmask_b32_e64 v221, v220, v221, vcc\n"
    "v_mfma_f32_16x16x32_bf16 a[0:3], v[116:119], v[212:215], a[0:3]\n"
    "buffer_load_dwordx4 v[104:107], v255, s[48:51], s91 offen offset:1024 sc1\n"
    "buffer_load_dwordx4 v[108:111], v255, s[48:51], s91 offen offset:2048 sc1\n"
    "v_mfma_f32_16x16x32_bf16 a[4:7], v[64:67], v[196:199], a[4:7]\n"
    "buffer_load_dwordx4 v[112:115], v255, s[48:51], s91 offen offset:3072 sc1\n"
    "v_mfma_f32_16x16x32_bf16 a[4:7], v[120:123], v[212:215], a[4:7]\n"
    "v_mfma_f32_16x16x32_bf16 a[8:11], v[68:71], v[196:199], a[8:11]\n"
    "v_add_u32_e32 v231, 1, v230\n"
    "v_mfma_f32_16x16x32_bf16 a[8:11], v[124:127], v[212:215], a[8:11]\n"
    "s_waitcnt vmcnt(5)\n"
    "s_waitcnt lgkmcnt(0)\n"
    "s_barrier\n"
    "buffer_store_dword v231, v241, s[48:51], s81 offen sc1\n"
    "v_mfma_f32_16x16x32_bf16 a[12:15], v[72:75], v[196:199], a[12:15]\n"
    "v_mfma_f32_16x16x32_bf16 a[12:15], v[128:131], v[212:215], a[12:15]\n"
    "s_waitcnt vmcnt(4)\n"
    "v_add_f32_dpp v222, v222, v222 quad_perm:[1,0,3,2] row_mask:0xf bank_mask:0xf\n"
    "s_nop 1\n"
    "v_add_f32_dpp v222, v222, v222 quad_perm:[2,3,0,1] row_mask:0xf bank_mask:0xf\n"
    "v_mfma_f32_16x16x32_bf16 a[16:19], v[76:79], v[196:199], a[16:19]\n"
    "s_lshr_b32 s83, s37, 6\n"
    "s_cmp_eq_u32 s83, 2\n"
    "s_cselect_b32 s83, 1.0, 0\n"
    "v_cvt_pk_bf16_f32 v222, 0, v222\n"
    "v_mfma_f32_16x16x32_bf16 a[16:19], v[132:135], v[212:215], a[16:19]\n"
    "v_add_f32_e64 v222, s83, v222\n"
    "v_cvt_pk_bf16_f32 v222, 0, v222\n"
    "v_mfma_f32_16x16x32_bf16 a[20:23], v[60:63], v[200:203], a[20:23]\n"
    "s_add_u32 s82, s82, 0x13400\n"
    "buffer_store_dword v222, v221, s[48:51], s82 offen sc1\n"
    "v_mfma_f32_16x16x32_bf16 a[20:23], v[116:119], a[60:63], a[20:23]\n"
    "s_add_u32 m0, s68, 0x10000\n"
    "v_mfma_f32_16x16x32_bf16 a[24:27], v[64:67], v[200:203], a[24:27]\n"
    "s_add_u32 s92, s11, 0x1000\n"
    "buffer_load_dwordx4 v255, s[48:51], s92 offen sc1 lds\n"
    "v_mfma_f32_16x16x32_bf16 a[24:27], v[120:123], a[60:63], a[24:27]\n"
    "v_mfma_f32_16x16x32_bf16 a[28:31], v[68:71], v[200:203], a[28:31]\n"
    "v_mfma_f32_16x16x32_bf16 a[28:31], v[124:127], a[60:63], a[28:31]\n"
    "buffer_load_dwordx4 v255, s[48:51], s92 offen offset:1024 sc1 lds\n"
    "v_mfma_f32_16x16x32_bf16 a[32:35], v[72:75], v[200:203], a[32:35]\n"
    "v_mfma_f32_16x16x32_bf16 a[32:35], v[128:131], a[60:63], a[32:35]\n"
    "v_mfma_f32_16x16x32_bf16 a[36:39], v[76:79], v[200:203], a[36:39]\n"
    "s_add_u32 m0, s68, 0x12000\n"
    "v_mfma_f32_16x16x32_bf16 a[36:39], v[132:135], a[60:63], a[36:39]\n"
    "s_add_u32 s92, s11, 0x2000\n"
    "buffer_load_dwordx4 v255, s[48:51], s92 offen sc1 lds\n"
    "v_mfma_f32_16x16x32_bf16 a[40:43], v[60:63], v[204:207], a[40:43]\n"
    "v_mfma_f32_16x16x32_bf16 a[40:43], v[116:119], a[64:67], a[40:43]\n"
    "v_mfma_f32_16x16x32_bf16 a[44:47], v[64:67], v[204:207], a[44:47]\n"
    "v_mfma_f32_16x16x32_bf16 a[44:47], v[120:123], a[64:67], a[44:47]\n"
    "v_mfma_f32_16x16x32_bf16 a[48:51], v[68:71], v[204:207], a[48:51]\n"
    "buffer_load_dwordx4 v255, s[48:51], s92 offen offset:1024 sc1 lds\n"
    "v_mfma_f32_16x16x32_bf16 a[48:51], v[124:127], a[64:67], a[48:51]\n"
    "v_mfma_f32_16x16x32_bf16 a[52:55], v[72:75], v[204:207], a[52:55]\n"
    "v_mfma_f32_16x16x32_bf16 a[52:55], v[128:131], a[64:67], a[52:55]\n"
    "s_add_u32 m0, s68, 0x14000\n"
    "v_mfma_f32_16x16x32_bf16 a[56:59], v[76:79], v[204:207], a[56:59]\n"
    "s_add_u32 s92, s11, 0x3000\n"
    "buffer_load_dwordx4 v255, s[48:51], s92 offen sc1 lds\n"
    "v_mfma_f32_16x16x32_bf16 a[56:59], v[132:135], a[64:67], a[56:59]\n"
    "v_mfma_f32_16x16x32_bf16 v[0:3], v[60:63], v[208:211], v[0:3]\n"
    "v_mfma_f32_16x16x32_bf16 v[0:3], v[116:119], a[68:71], v[0:3]\n"
    "v_mfma_f32_16x16x32_bf16 v[4:7], v[64:67], v[208:211], v[4:7]\n"
    "buffer_load_dwordx4 v255, s[48:51], s92 offen offset:1024 sc1 lds\n"
    "v_mfma_f32_16x16x32_bf16 v[4:7], v[120:123], a[68:71], v[4:7]\n"
    "v_mfma_f32_16x16x32_bf16 v[8:11], v[68:71], v[208:211], v[8:11]\n"
    "v_mfma_f32_16x16x32_bf16 v[8:11], v[124:127], a[68:71], v[8:11]\n"
    "s_add_u32 m0, s68, 0x16000\n"
    "v_mfma_f32_16x16x32_bf16 v[12:15], v[72:75], v[208:211], v[12:15]\n"
    "s_add_u32 s92, s11, 0x4000\n"
    "buffer_load_dwordx4 v255, s[48:51], s92 offen sc1 lds\n"
    "v_mfma_f32_16x16x32_bf16 v[12:15], v[128:131], a[68:71], v[12:15]\n"
    "v_mfma_f32_16x16x32_bf16 v[16:19], v[76:79], v[208:211], v[16:19]\n"
    "v_mfma_f32_16x16x32_bf16 v[16:19], v[132:135], a[68:71], v[16:19]\n"
    "buffer_load_dwordx4 v255, s[48:51], s92 offen offset:1024 sc1 lds\n"
    "s_add_u32 m0, s68, 0x18000\n"
    "s_add_u32 s92, s12, 0\n"
    "buffer_load_dwordx4 v255, s[48:51], s92 offen sc1 lds\n"
    "buffer_load_dwordx4 v255, s[48:51], s92 offen offset:1024 sc1 lds\n"
    "s_add_u32 m0, s68, 0x1a000\n"
    "s_add_u32 s92, s12, 0x1000\n"
    "buffer_load_dwordx4 v255, s[48:51], s92 offen sc1 lds\n"
    "buffer_load_dwordx4 v255, s[48:51], s92 offen offset:1024 sc1 lds\n"
    "s_add_u32 m0, s68, 0x1c000\n"
    "s_add_u32 s92, s12, 0x2000\n"
    "buffer_load_dwordx4 v255, s[48:51], s92 offen sc1 lds\n"
    "buffer_load_dwordx4 v255, s[48:51], s92 offen offset:1024 sc1 lds\n"
    "s_add_u32 m0, s68, 0x1e000\n"
    "s_add_u32 s92, s12, 0x3000\n"
    "buffer_load_dwordx4 v255, s[48:51], s92 offen sc1 lds\n"
    "buffer_load_dwordx4 v255, s[48:51], s92 offen offset:1024 sc1 lds\n"
    "s_add_u32 m0, s68, 0x20000\n"
    "s_add_u32 s92, s12, 0x4000\n"
    "buffer_load_dwordx4 v255, s[48:51], s92 offen sc1 lds\n"
    "buffer_load_dwordx4 v255, s[48:51], s92 offen offset:1024 sc1 lds\n"
    "ds_read_b128 v[60:63], v244 offset:2048\n"
    "ds_read_b128 v[64:67], v244 offset:3072\n"
    "ds_read_b128 v[68:71], v244 offset:8192\n"
    "ds_read_b128 v[72:75], v244 offset:9216\n"
    "ds_read_b128 v[76:79], v244 offset:10240\n"
    "ds_read_b128 v[116:119], v244 offset:11264\n"
    "ds_read_b128 v[120:123], v244 offset:16384\n"
    "ds_read_b128 v[124:127], v244 offset:17408\n"
    "ds_read_b128 v[128:131], v244 offset:18432\n"
    "ds_read_b128 v[132:135], v244 offset:19456\n"
    "v_bfe_u32 v240, v255, 4, 3\n"
    "v_lshlrev_b32_e32 v240, 2, v240\n"
    "buffer_load_dword v229, v240, s[48:51], s63 offen sc1\n"
    "s_add_u32 m0, s68, 0x22000\n"
    "s_add_u32 s92, s13, 0\n"
    "buffer_load_dwordx4 v255, s[48:51], s92 offen sc1 lds\n"
    "buffer_load_dwordx4 v255, s[48:51], s92 offen offset:1024 sc1 lds\n"
    "s_waitcnt lgkmcnt(0)\n"
    "s_barrier\n"
    "s_waitcnt vmcnt(2)\n"
    "v_sub_u32_e32 v229, v229, v230\n"
    "v_subrev_u32_e32 v229, 1, v229\n"
    "v_cmp_gt_u32_e32 vcc, 3, v229\n"
    "s_nop 1\n"
    "s_cmp_eq_u64 vcc, -1\n"
    "s_cbranch_scc1 POK4_%=\n"
    "s_mov_b32 s61, 20000\n"
    "PSP4_%=:\n"
    "s_sleep 4\n"
    "buffer_load_dword v229, v240, s[48:51], s63 offen sc1\n"
    "s_waitcnt vmcnt(0)\n"
    "v_sub_u32_e32 v229, v229, v230\n"
    "v_subrev_u32_e32 v229, 1, v229\n"
    "v_cmp_gt_u32_e32 vcc, 3, v229\n"
    "s_nop 1\n"
    "s_cmp_eq_u64 vcc, -1\n"
    "s_cbranch_scc1 POK4_%=\n"
    "s_sub_u32 s61, s61, 1\n"
    "s_cmp_lg_u32 s61, 0\n"
    "s_cbranch_scc1 PSP4_%=\n"
    "POK4_%=:\n"
    "s_cmp_eq_u32 s67, 0\n"
    "s_cselect_b32 s66, 0x4000, 0\n"
    "s_add_u32 s88, s88, s66\n"
    "s_add_u32 m0, s68, 0x24000\n"
    "s_nop 0\n"
    "buffer_load_dwordx4 v255, s[48:51], s88 offen sc1 lds\n"
    "buffer_load_dwordx4 v255, s[48:51], s88 offen offset:1024 sc1 lds\n"
    "s_add_u32 s88, s88, 0x1000\n"
    "s_add_u32 m0, s68, 0x26000\n"
    "s_nop 0\n"
    "buffer_load_dwordx4 v255, s[48:51], s88 offen sc1 lds\n"
    "buffer_load_dwordx4 v255, s[48:51], s88 offen offset:1024 sc1 lds\n"
    "s_add_u32 s88, s88, 0x1000\n"
    "s_add_u32 m0, s68, 0\n"
    "s_nop 0\n"
    "buffer_load_dwordx4 v255, s[48:51], s88 offen sc1 lds\n"
    "buffer_load_dwordx4 v255, s[48:51], s88 offen offset:1024 sc1 lds\n"
    "s_add_u32 s88, s88, 0x1000\n"
    "s_add_u32 m0, s68, 0x2000\n"
    "s_nop 0\n"
    "buffer_load_dwordx4 v255, s[48:51], s88 offen sc1 lds\n"
    "buffer_load_dwordx4 v255, s[48:51], s88 offen offset:1024 sc1 lds\n"
    "s_add_u32 s88, s88, 0x1000\n"
    "s_cmp_eq_u32 s67, 1\n"
    "s_cselect_b32 s66, 0x4000, 0\n"
    "s_add_u32 s88, s88, s66\n"
    "s_add_u32 m0, s68, 0x4000\n"
    "s_nop 0\n"
    "buffer_load_dwordx4 v255, s[48:51], s88 offen sc1 lds\n"
    "buffer_load_dwordx4 v255, s[48:51], s88 offen offset:1024 sc1 lds\n"
    "s_add_u32 s88, s88, 0x1000\n"
    "s_add_u32 m0, s68, 0xc000\n"
    "s_nop 0\n"
    "buffer_load_dwordx4 v255, s[48:51], s88 offen sc1 lds\n"
    "buffer_load_dwordx4 v255, s[48:51], s88 offen offset:1024 sc1 lds\n"
    "s_add_u32 s88, s88, 0x1000\n"
    "s_lshl_b32 s38, s89, 1\n"
    "s_and_b32 s39, s36, 1\n"
    "s_add_u32 s38, s38, s39\n"
    "s_lshl_b32 s39, s90, 1\n"
    "s_lshr_b32 s60, s36, 1\n"
    "s_add_u32 s39, s39, s60\n"
    "s_lshl_b32 s63, s89, 12\n"
    "s_add_u32 s63, s63, 0x40000\n"
    "s_lshl_b32 s93, s90, 2\n"
    "s_add_u32 s93, s93, s63\n"
    "s_lshl_b32 s38, s38, 5\n"
    "s_and_b32 s60, s39, 7\n"
    "s_add_u32 s38, s38, s60\n"
    "s_mul_i32 s64, s38, 0xa00\n"
    "s_lshr_b32 s39, s39, 3\n"
    "s_mul_i32 s39, s39, 0x280000\n"
    "s_add_u32 s64, s64, s39\n"
    "s_add_u32 s64, s64, 0x2f00000\n"
    "v_lshrrev_b32_e32 v242, 1, v255\n"
    "s_barrier\n"
    "ds_read_b128 v[20:23], v244 offset:24576\n"
    "ds_read_b128 v[24:27], v244 offset:25600\n"
    "ds_read_b128 v[28:31], v244 offset:26624\n"
    "ds_read_b128 v[32:35], v244 offset:27648\n"
    "ds_read_b128 v[36:39], v245 offset:0\n"
    "ds_read_b128 v[40:43], v245 offset:1024\n"
    "ds_read_b128 v[44:47], v245 offset:2048\n"
    "ds_read_b128 v[48:51], v245 offset:3072\n"
    "ds_read_b128 v[52:55], v245 offset:8192\n"
    "ds_read_b128 v[56:59], v245 offset:9216\n"
    "s_waitcnt vmcnt(8)\n"
    "s_barrier\n"
    "ds_read_b128 v[136:139], v254 offset:16384\n"
    "ds_read_b128 v[152:155], v254 offset:24576\n"
    "ds_read_b128 v[140:143], v254 offset:17408\n"
    "ds_read_b128 v[156:159], v254 offset:25600\n"
    "ds_read_b128 v[144:147], v254 offset:18432\n"
    "ds_read_b128 v[160:163], v254 offset:26624\n"
    "ds_read_b128 v[148:151], v254 offset:19456\n"
    "ds_read_b128 v[192:195], v254 offset:27648\n"
    "s_waitcnt lgkmcnt(7)\n"
    "v_mfma_f32_16x16x32_bf16 a[0:3], a[192:195], v[136:139], a[0:3]\n"
    "s_waitcnt lgkmcnt(6)\n"
    "v_mfma_f32_16x16x32_bf16 a[0:3], a[212:215], v[152:155], a[0:3]\n"
    "v_mfma_f32_16x16x32_bf16 a[4:7], a[196:199], v[136:139], a[4:7]\n"
    "v_mfma_f32_16x16x32_bf16 a[4:7], a[216:219], v[152:155], a[4:7]\n"
    "s_waitcnt vmcnt(4)\n"
    "s_waitcnt lgkmcnt(0)\n"
    "s_barrier\n"
    "v_mfma_f32_16x16x32_bf16 a[8:11], a[200:203], v[136:139], a[8:11]\n"
    "ds_read_b128 v[196:199], v252 offset:0\n"
    "v_mfma_f32_16x16x32_bf16 a[8:11], a[220:223], v[152:155], a[8:11]\n"
    "s_add_u32 m0, s68, 0x8000\n"
    "v_mfma_f32_16x16x32_bf16 a[12:15], a[204:207], v[136:139], a[12:15]\n"
    "buffer_load_dwordx4 v255, s[48:51], s88 offen sc1 lds\n"
    "v_mfma_f32_16x16x32_bf16 a[12:15], a[224:227], v[152:155], a[12:15]\n"
    "ds_read_b128 v[212:215], v252 offset:8192\n"
    "v_mfma_f32_16x16x32_bf16 a[16:19], a[208:211], v[136:139], a[16:19]\n"
    "v_mfma_f32_16x16x32_bf16 a[16:19], a[228:231], v[152:155], a[16:19]\n"
    "v_mfma_f32_16x16x32_bf16 a[20:23], a[192:195], v[140:143], a[20:23]\n"
    "s_add_u32 s82, s84, 0x1400\n"
    "ds_read_b128 v[200:203], v252 offset:1024\n"
    "v_mfma_f32_16x16x32_bf16 a[20:23], a[212:215], v[156:159], a[20:23]\n"
    "v_mfma_f32_16x16x32_bf16 a[24:27], a[196:199], v[140:143], a[24:27]\n"
    "buffer_store_dword v231, v241, s[48:51], s82 offen sc1\n"
    "v_mfma_f32_16x16x32_bf16 a[24:27], a[216:219], v[156:159], a[24:27]\n"
    "buffer_load_dwordx4 v255, s[48:51], s88 offen offset:1024 sc1 lds\n"
    "ds_read_b128 a[60:63], v252 offset:9216\n"
    "v_mfma_f32_16x16x32_bf16 a[28:31], a[200:203], v[140:143], a[28:31]\n"
    "v_mfma_f32_16x16x32_bf16 a[28:31], a[220:223], v[156:159], a[28:31]\n"
    "v_mfma_f32_16x16x32_bf16 a[32:35], a[204:207], v[140:143], a[32:35]\n"
    "v_mfma_f32_16x16x32_bf16 a[32:35], a[224:227], v[156:159], a[32:35]\n"
    "ds_read_b128 v[204:207], v252 offset:2048\n"
    "v_mfma_f32_16x16x32_bf16 a[36:39], a[208:211], v[140:143], a[36:39]\n"
    "v_mfma_f32_16x16x32_bf16 a[36:39], a[228:231], v[156:159], a[36:39]\n"
    "v_mfma_f32_16x16x32_bf16 a[40:43], a[192:195], v[144:147], a[40:43]\n"
    "ds_read_b128 a[64:67], v252 offset:10240\n"
    "v_mfma_f32_16x16x32_bf16 a[40:43], a[212:215], v[160:163], a[40:43]\n"
    "s_add_u32 s88, s88, 0x1000\n"
    "v_mfma_f32_16x16x32_bf16 a[44:47], a[196:199], v[144:147], a[44:47]\n"
    "s_add_u32 m0, s68, 0xa000\n"
    "v_mfma_f32_16x16x32_bf16 a[44:47], a[216:219], v[160:163], a[44:47]\n"
    "buffer_load_dwordx4 v255, s[48:51], s88 offen sc1 lds\n"
    "ds_read_b128 v[208:211], v252 offset:3072\n"
    "v_mfma_f32_16x16x32_bf16 a[48:51], a[200:203], v[144:147], a[48:51]\n"
    "v_mfma_f32_16x16x32_bf16 a[48:51], a[220:223], v[160:163], a[48:51]\n"
    "v_mfma_f32_16x16x32_bf16 a[52:55], a[204:207], v[144:147], a[52:55]\n"
    "ds_read_b128 a[68:71], v252 offset:11264\n"
    "v_mfma_f32_16x16x32_bf16 a[52:55], a[224:227], v[160:163], a[52:55]\n"
    "v_mfma_f32_16x16x32_bf16 a[56:59], a[208:211], v[144:147], a[56:59]\n"
    "v_mfma_f32_16x16x32_bf16 a[56:59], a[228:231], v[160:163], a[56:59]\n"
    "v_mfma_f32_16x16x32_bf16 v[0:3], a[192:195], v[148:151], v[0:3]\n"
    "buffer_load_dwordx4 v255, s[48:51], s88 offen offset:1024 sc1 lds\n"
    "v_mfma_f32_16x16x32_bf16 v[0:3], a[212:215], v[192:195], v[0:3]\n"
    "v_mfma_f32_16x16x32_bf16 v[4:7], a[196:199], v[148:151], v[4:7]\n"
    "v_mfma_f32_16x16x32_bf16 v[4:7], a[216:219], v[192:195], v[4:7]\n"
    "v_mfma_f32_16x16x32_bf16 v[8:11], a[200:203], v[148:151], v[8:11]\n"
    "v_mfma_f32_16x16x32_bf16 v[8:11], a[220:223], v[192:195], v[8:11]\n"
    "v_mfma_f32_16x16x32_bf16 v[12:15], a[204:207], v[148:151], v[12:15]\n"
    "v_mfma_f32_16x16x32_bf16 v[12:15], a[224:227], v[192:195], v[12:15]\n"
    "v_mfma_f32_16x16x32_bf16 v[16:19], a[208:211], v[148:151], v[16:19]\n"
    "s_add_u32 s88, s88, 0x1000\n"
    "v_mfma_f32_16x16x32_bf16 v[16:19], a[228:231], v[192:195], v[16:19]\n"
    "s_waitcnt lgkmcnt(7)\n"
    "v_mfma_f32_16x16x32_bf16 a[0:3], a[232:235], v[196:199], a[0:3]\n"
    "s_waitcnt lgkmcnt(6)\n"
    "v_mfma_f32_16x16x32_bf16 a[0:3], a[252:255], v[212:215], a[0:3]\n"
    "v_mfma_f32_16x16x32_bf16 a[4:7], a[236:239], v[196:199], a[4:7]\n"
    "v_mfma_f32_16x16x32_bf16 a[4:7], a[72:75], v[212:215], a[4:7]\n"
    "s_waitcnt vmcnt(5)\n"
    "s_waitcnt lgkmcnt(0)\n"
    "s_barrier\n"
    "v_mfma_f32_16x16x32_bf16 a[8:11], a[240:243], v[196:199], a[8:11]\n"
    "ds_read_b128 v[136:139], v252 offset:16384\n"
    "v_mfma_f32_16x16x32_bf16 a[8:11], a[76:79], v[212:215], a[8:11]\n"
    "s_cmp_eq_u32 s67, 2\n"
    "s_cselect_b32 s66, 0x4000, 0\n"
    "v_mfma_f32_16x16x32_bf16 a[12:15], a[244:247], v[196:199], a[12:15]\n"
    "s_add_u32 s88, s88, s66\n"
    "s_add_u32 m0, s68, 0xe000\n"
    "v_mfma_f32_16x16x32_bf16 a[12:15], a[80:83], v[212:215], a[12:15]\n"
    "buffer_load_dwordx4 v255, s[48:51], s88 offen sc1 lds\n"
    "ds_read_b128 v[152:155], v252 offset:49152\n"
    "v_mfma_f32_16x16x32_bf16 a[16:19], a[248:251], v[196:199], a[16:19]\n"
    "v_mfma_f32_16x16x32_bf16 a[16:19], a[84:87], v[212:215], a[16:19]\n"
    "buffer_load_dwordx4 v255, s[48:51], s88 offen offset:1024 sc1 lds\n"
    "v_mfma_f32_16x16x32_bf16 a[20:23], a[232:235], v[200:203], a[20:23]\n"
    "ds_read_b128 v[140:143], v252 offset:17408\n"
    "v_mfma_f32_16x16x32_bf16 a[20:23], a[252:255], a[60:63], a[20:23]\n"
    "v_mfma_f32_16x16x32_bf16 a[24:27], a[236:239], v[200:203], a[24:27]\n"
    "s_add_u32 s88, s88, 0x1000\n"
    "v_mfma_f32_16x16x32_bf16 a[24:27], a[72:75], a[60:63], a[24:27]\n"
    "s_add_u32 m0, s68, 0x24000\n"
    "ds_read_b128 v[156:159], v252 offset:50176\n"
    "v_mfma_f32_16x16x32_bf16 a[28:31], a[240:243], v[200:203], a[28:31]\n"
    "buffer_load_dwordx4 v255, s[48:51], s88 offen sc1 lds\n"
    "v_mfma_f32_16x16x32_bf16 a[28:31], a[76:79], a[60:63], a[28:31]\n"
    "v_mfma_f32_16x16x32_bf16 a[32:35], a[244:247], v[200:203], a[32:35]\n"
    "v_mfma_f32_16x16x32_bf16 a[32:35], a[80:83], a[60:63], a[32:35]\n"
    "ds_read_b128 v[144:147], v252 offset:18432\n"
    "buffer_load_dwordx4 v255, s[48:51], s88 offen offset:1024 sc1 lds\n"
    "v_mfma_f32_16x16x32_bf16 a[36:39], a[248:251], v[200:203], a[36:39]\n"
    "v_mfma_f32_16x16x32_bf16 a[36:39], a[84:87], a[60:63], a[36:39]\n"
    "v_mfma_f32_16x16x32_bf16 a[40:43], a[232:235], v[204:207], a[40:43]\n"
    "ds_read_b128 v[160:163], v252 offset:51200\n"
    "v_mfma_f32_16x16x32_bf16 a[40:43], a[252:255], a[64:67], a[40:43]\n"
    "s_add_u32 s88, s88, 0x1000\n"
    "v_mfma_f32_16x16x32_bf16 a[44:47], a[236:239], v[204:207], a[44:47]\n"
    "s_add_u32 m0, s68, 0x26000\n"
    "s_add_u32 s92, s13, 0x1000\n"
    "v_mfma_f32_16x16x32_bf16 a[44:47], a[72:75], a[64:67], a[44:47]\n"
    "buffer_load_dwordx4 v255, s[48:51], s92 offen sc1 lds\n"
    "ds_read_b128 v[148:151], v252 offset:19456\n"
    "v_mfma_f32_16x16x32_bf16 a[48:51], a[240:243], v[204:207], a[48:51]\n"
    "v_mfma_f32_16x16x32_bf16 a[48:51], a[76:79], a[64:67], a[48:51]\n"
    "v_mfma_f32_16x16x32_bf16 a[52:55], a[244:247], v[204:207], a[52:55]\n"
    "buffer_load_dwordx4 v255, s[48:51], s92 offen offset:1024 sc1 lds\n"
    "ds_read_b128 v[192:195], v252 offset:52224\n"
    "v_mfma_f32_16x16x32_bf16 a[52:55], a[80:83], a[64:67], a[52:55]\n"
    "v_mfma_f32_16x16x32_bf16 a[56:59], a[248:251], v[204:207], a[56:59]\n"
    "v_mfma_f32_16x16x32_bf16 a[56:59], a[84:87], a[64:67], a[56:59]\n"
    "s_add_u32 s95, s94, 0x8000\n"
    "v_mfma_f32_16x16x32_bf16 v[0:3], a[232:235], v[208:211], v[0:3]\n"
    "buffer_load_dwordx4 a[192:195], v255, s[48:51], s95 offen sc1\n"
    "v_mfma_f32_16x16x32_bf16 v[0:3], a[252:255], a[68:71], v[0:3]\n"
    "v_mfma_f32_16x16x32_bf16 v[4:7], a[236:239], v[208:211], v[4:7]\n"
    "v_mfma_f32_16x16x32_bf16 v[4:7], a[72:75], a[68:71], v[4:7]\n"
    "v_mfma_f32_16x16x32_bf16 v[8:11], a[240:243], v[208:211], v[8:11]\n"
    "buffer_load_dwordx4 a[196:199], v255, s[48:51], s95 offen offset:1024 sc1\n"
    "v_mfma_f32_16x16x32_bf16 v[8:11], a[76:79], a[68:71], v[8:11]\n"
    "v_mfma_f32_16x16x32_bf16 v[12:15], a[244:247], v[208:211], v[12:15]\n"
    "v_mfma_f32_16x16x32_bf16 v[12:15], a[80:83], a[68:71], v[12:15]\n"
    "v_mfma_f32_16x16x32_bf16 v[16:19], a[248:251], v[208:211], v[16:19]\n"
    "v_mfma_f32_16x16x32_bf16 v[16:19], a[84:87], a[68:71], v[16:19]\n"
    "s_waitcnt lgkmcnt(7)\n"
    "v_mfma_f32_16x16x32_bf16 a[0:3], a[88:91], v[136:139], a[0:3]\n"
    "s_waitcnt lgkmcnt(6)\n"
    "v_mfma_f32_16x16x32_bf16 a[0:3], a[108:111], v[152:155], a[0:3]\n"
    "v_mfma_f32_16x16x32_bf16 a[4:7], a[92:95], v[136:139], a[4:7]\n"
    "v_mfma_f32_16x16x32_bf16 a[4:7], a[112:115], v[152:155], a[4:7]\n"
    "s_waitcnt vmcnt(8)\n"
    "s_waitcnt lgkmcnt(0)\n"
    "s_barrier\n"
    "v_mfma_f32_16x16x32_bf16 a[8:11], a[96:99], v[136:139], a[8:11]\n"
    "ds_read_b128 v[196:199], v252 offset:32768\n"
    "v_mfma_f32_16x16x32_bf16 a[8:11], a[116:119], v[152:155], a[8:11]\n"
    "s_add_u32 m0, s68, 0x6000\n"
    "v_mfma_f32_16x16x32_bf16 a[12:15], a[100:103], v[136:139], a[12:15]\n"
    "buffer_load_dwordx4 v255, s[48:51], s88 offen sc1 lds\n"
    "v_mfma_f32_16x16x32_bf16 a[12:15], a[120:123], v[152:155], a[12:15]\n"
    "ds_read_b128 v[212:215], v252 offset:40960\n"
    "v_mfma_f32_16x16x32_bf16 a[16:19], a[104:107], v[136:139], a[16:19]\n"
    "v_mfma_f32_16x16x32_bf16 a[16:19], a[124:127], v[152:155], a[16:19]\n"
    "buffer_load_dwordx4 v255, s[48:51], s88 offen offset:1024 sc1 lds\n"
    "v_mfma_f32_16x16x32_bf16 a[20:23], a[88:91], v[140:143], a[20:23]\n"
    "ds_read_b128 v[200:203], v252 offset:33792\n"
    "v_mfma_f32_16x16x32_bf16 a[20:23], a[108:111], v[156:159], a[20:23]\n"
    "v_mfma_f32_16x16x32_bf16 a[24:27], a[92:95], v[140:143], a[24:27]\n"
    "s_add_u32 s88, s88, 0x1000\n"
    "v_mfma_f32_16x16x32_bf16 a[24:27], a[112:115], v[156:159], a[24:27]\n"
    "s_add_u32 m0, s68, 0x10000\n"
    "ds_read_b128 a[60:63], v252 offset:41984\n"
    "v_mfma_f32_16x16x32_bf16 a[28:31], a[96:99], v[140:143], a[28:31]\n"
    "buffer_load_dwordx4 v255, s[48:51], s88 offen sc1 lds\n"
    "v_mfma_f32_16x16x32_bf16 a[28:31], a[116:119], v[156:159], a[28:31]\n"
    "v_mfma_f32_16x16x32_bf16 a[32:35], a[100:103], v[140:143], a[32:35]\n"
    "v_mfma_f32_16x16x32_bf16 a[32:35], a[120:123], v[156:159], a[32:35]\n"
    "ds_read_b128 v[204:207], v252 offset:34816\n"
    "buffer_load_dwordx4 v255, s[48:51], s88 offen offset:1024 sc1 lds\n"
    "v_mfma_f32_16x16x32_bf16 a[36:39], a[104:107], v[140:143], a[36:39]\n"
    "v_mfma_f32_16x16x32_bf16 a[36:39], a[124:127], v[156:159], a[36:39]\n"
    "v_mfma_f32_16x16x32_bf16 a[40:43], a[88:91], v[144:147], a[40:43]\n"
    "ds_read_b128 a[64:67], v252 offset:43008\n"
    "v_mfma_f32_16x16x32_bf16 a[40:43], a[108:111], v[160:163], a[40:43]\n"
    "s_add_u32 s88, s88, 0x1000\n"
    "v_mfma_f32_16x16x32_bf16 a[44:47], a[92:95], v[144:147], a[44:47]\n"
    "s_add_u32 m0, s68, 0\n"
    "s_add_u32 s92, s13, 0x2000\n"
    "v_mfma_f32_16x16x32_bf16 a[44:47], a[112:115], v[160:163], a[44:47]\n"
    "buffer_load_dwordx4 v255, s[48:51], s92 offen sc1 lds\n"
    "ds_read_b128 v[208:211], v252 offset:35840\n"
    "v_mfma_f32_16x16x32_bf16 a[48:51], a[96:99], v[144:147], a[48:51]\n"
    "v_mfma_f32_16x16x32_bf16 a[48:51], a[116:119], v[160:163], a[48:51]\n"
    "v_mfma_f32_16x16x32_bf16 a[52:55], a[100:103], v[144:147], a[52:55]\n"
    "buffer_load_dwordx4 v255, s[48:51], s92 offen offset:1024 sc1 lds\n"
    "ds_read_b128 a[68:71], v252 offset:44032\n"
    "v_mfma_f32_16x16x32_bf16 a[52:55], a[120:123], v[160:163], a[52:55]\n"
    "v_mfma_f32_16x16x32_bf16 a[56:59], a[104:107], v[144:147], a[56:59]\n"
    "v_mfma_f32_16x16x32_bf16 a[56:59], a[124:127], v[160:163], a[56:59]\n"
    "v_mfma_f32_16x16x32_bf16 v[0:3], a[88:91], v[148:151], v[0:3]\n"
    "buffer_load_dwordx4 a[200:203], v255, s[48:51], s95 offen offset:2048 sc1\n"
    "v_mfma_f32_16x16x32_bf16 v[0:3], a[108:111], v[192:195], v[0:3]\n"
    "v_mfma_f32_16x16x32_bf16 v[4:7], a[92:95], v[148:151], v[4:7]\n"
    "v_mfma_f32_16x16x32_bf16 v[4:7], a[112:115], v[192:195], v[4:7]\n"
    "v_mfma_f32_16x16x32_bf16 v[8:11], a[96:99], v[148:151], v[8:11]\n"
    "buffer_load_dwordx4 a[204:207], v255, s[48:51], s95 offen offset:3072 sc1\n"
    "v_mfma_f32_16x16x32_bf16 v[8:11], a[116:119], v[192:195], v[8:11]\n"
    "v_mfma_f32_16x16x32_bf16 v[12:15], a[100:103], v[148:151], v[12:15]\n"
    "v_mfma_f32_16x16x32_bf16 v[12:15], a[120:123], v[192:195], v[12:15]\n"
    "v_mfma_f32_16x16x32_bf16 v[16:19], a[104:107], v[148:151], v[16:19]\n"
    "v_mfma_f32_16x16x32_bf16 v[16:19], a[124:127], v[192:195], v[16:19]\n"
    "s_waitcnt lgkmcnt(7)\n"
    "v_mfma_f32_16x16x32_bf16 a[0:3], v[60:63], v[196:199], a[0:3]\n"
    "s_waitcnt lgkmcnt(6)\n"
    "v_mfma_f32_16x16x32_bf16 a[0:3], v[116:119], v[212:215], a[0:3]\n"
    "v_mfma_f32_16x16x32_bf16 a[4:7], v[64:67], v[196:199], a[4:7]\n"
    "v_mfma_f32_16x16x32_bf16 a[4:7], v[120:123], v[212:215], a[4:7]\n"
    "s_waitcnt vmcnt(12)\n"
    "s_waitcnt lgkmcnt(0)\n"
    "s_barrier\n"
    "v_mfma_f32_16x16x32_bf16 a[8:11], v[68:71], v[196:199], a[8:11]\n"
    "ds_read_b128 v[136:139], v252 offset:57344\n"
    "v_mfma_f32_16x16x32_bf16 a[8:11], v[124:127], v[212:215], a[8:11]\n"
    "s_cmp_eq_u32 s67, 3\n"
    "s_cselect_b32 s66, 0x4000, 0\n"
    "v_mfma_f32_16x16x32_bf16 a[12:15], v[72:75], v[196:199], a[12:15]\n"
    "s_add_u32 s88, s88, s66\n"
    "s_add_u32 m0, s68, 0x2000\n"
    "v_mfma_f32_16x16x32_bf16 a[12:15], v[128:131], v[212:215], a[12:15]\n"
    "buffer_load_dwordx4 v255, s[48:51], s88 offen sc1 lds\n"
    "ds_read_b128 v[152:155], v254 offset:16384\n"
    "v_mfma_f32_16x16x32_bf16 a[16:19], v[76:79], v[196:199], a[16:19]\n"
    "v_mfma_f32_16x16x32_bf16 a[16:19], v[132:135], v[212:215], a[16:19]\n"
    "v_mfma_f32_16x16x32_bf16 a[20:23], v[60:63], v[200:203], a[20:23]\n"
    "ds_read_b128 v[140:143], v252 offset:58368\n"
    "buffer_load_dwordx4 v255, s[48:51], s88 offen offset:1024 sc1 lds\n"
    "v_mfma_f32_16x16x32_bf16 a[20:23], v[116:119], a[60:63], a[20:23]\n"
    "v_mfma_f32_16x16x32_bf16 a[24:27], v[64:67], v[200:203], a[24:27]\n"
    "v_mfma_f32_16x16x32_bf16 a[24:27], v[120:123], a[60:63], a[24:27]\n"
    "ds_read_b128 v[156:159], v254 offset:17408\n"
    "v_mfma_f32_16x16x32_bf16 a[28:31], v[68:71], v[200:203], a[28:31]\n"
    "v_mfma_f32_16x16x32_bf16 a[28:31], v[124:127], a[60:63], a[28:31]\n"
    "s_add_u32 s88, s88, 0x1000\n"
    "v_mfma_f32_16x16x32_bf16 a[32:35], v[72:75], v[200:203], a[32:35]\n"
    "s_add_u32 m0, s68, 0x4000\n"
    "v_mfma_f32_16x16x32_bf16 a[32:35], v[128:131], a[60:63], a[32:35]\n"
    "PG8_%=:\n"
    "s_cbranch_execz PG12_%=\n"
    "buffer_load_dwordx4 v255, s[48:51], s88 offen sc1 lds\n"
    "ds_read_b128 v[144:147], v252 offset:59392\n"
    "v_mfma_f32_16x16x32_bf16 a[36:39], v[76:79], v[200:203], a[36:39]\n"
    "v_mfma_f32_16x16x32_bf16 a[36:39], v[132:135], a[60:63], a[36:39]\n"
    "v_mfma_f32_16x16x32_bf16 a[40:43], v[60:63], v[204:207], a[40:43]\n"
    "ds_read_b128 v[160:163], v254 offset:18432\n"
    "v_mfma_f32_16x16x32_bf16 a[40:43], v[116:119], a[64:67], a[40:43]\n"
    "v_mfma_f32_16x16x32_bf16 a[44:47], v[64:67], v[204:207], a[44:47]\n"
    "buffer_load_dwordx4 v255, s[48:51], s88 offen offset:1024 sc1 lds\n"
    "v_mfma_f32_16x16x32_bf16 a[44:47], v[120:123], a[64:67], a[44:47]\n"
    "ds_read_b128 v[148:151], v252 offset:60416\n"
    "v_mfma_f32_16x16x32_bf16 a[48:51], v[68:71], v[204:207], a[48:51]\n"
    "v_mfma_f32_16x16x32_bf16 a[48:51], v[124:127], a[64:67], a[48:51]\n"
    "v_mfma_f32_16x16x32_bf16 a[52:55], v[72:75], v[204:207], a[52:55]\n"
    "s_add_u32 s88, s88, 0x1000\n"
    "ds_read_b128 v[192:195], v254 offset:19456\n"
    "v_mfma_f32_16x16x32_bf16 a[52:55], v[128:131], a[64:67], a[52:55]\n"
    "s_add_u32 m0, s68, 0xc000\n"
    "v_mfma_f32_16x16x32_bf16 a[56:59], v[76:79], v[204:207], a[56:59]\n"
    "s_add_u32 s92, s13, 0x3000\n"
    "buffer_load_dwordx4 v255, s[48:51], s92 offen sc1 lds\n"
    "v_mfma_f32_16x16x32_bf16 a[56:59], v[132:135], a[64:67], a[56:59]\n"
    "v_mfma_f32_16x16x32_bf16 v[0:3], v[60:63], v[208:211], v[0:3]\n"
    "v_mfma_f32_16x16x32_bf16 v[0:3], v[116:119], a[68:71], v[0:3]\n"
    "v_mfma_f32_16x16x32_bf16 v[4:7], v[64:67], v[208:211], v[4:7]\n"
    "v_mfma_f32_16x16x32_bf16 v[4:7], v[120:123], a[68:71], v[4:7]\n"
    "buffer_load_dwordx4 v255, s[48:51], s92 offen offset:1024 sc1 lds\n"
    "v_mfma_f32_16x16x32_bf16 v[8:11], v[68:71], v[208:211], v[8:11]\n"
    "v_mfma_f32_16x16x32_bf16 v[8:11], v[124:127], a[68:71], v[8:11]\n"
    "v_mfma_f32_16x16x32_bf16 v[12:15], v[72:75], v[208:211], v[12:15]\n"
    "v_mfma_f32_16x16x32_bf16 v[12:15], v[128:131], a[68:71], v[12:15]\n"
    "v_mfma_f32_16x16x32_bf16 v[16:19], v[76:79], v[208:211], v[16:19]\n"
    "v_mfma_f32_16x16x32_bf16 v[16:19], v[132:135], a[68:71], v[16:19]\n"
    "s_waitcnt lgkmcnt(7)\n"
    "v_mfma_f32_16x16x32_bf16 a[0:3], v[20:23], v[136:139], a[0:3]\n"
    "s_waitcnt lgkmcnt(6)\n"
    "v_mfma_f32_16x16x32_bf16 a[0:3], v[40:43], v[152:155], a[0:3]\n"
    "v_mfma_f32_16x16x32_bf16 a[4:7], v[24:27], v[136:139], a[4:7]\n"
    "s_nop 3\n"
    "v_mfma_f32_16x16x32_bf16 a[4:7], v[44:47], v[152:155], a[4:7]\n"
    "s_waitcnt vmcnt(10)\n"
    "s_waitcnt lgkmcnt(0)\n"
    "s_barrier\n"
    "v_mfma_f32_16x16x32_bf16 a[8:11], v[28:31], v[136:139], a[8:11]\n"
    "ds_read_b128 v[60:63], v245 offset:10240\n"
    "v_mfma_f32_16x16x32_bf16 a[8:11], v[48:51], v[152:155], a[8:11]\n"
    "s_add_u32 m0, s68, 0x8000\n"
    "ds_read_b128 v[64:67], v245 offset:11264\n"
    "v_mfma_f32_16x16x32_bf16 a[12:15], v[32:35], v[136:139], a[12:15]\n"
    "buffer_load_dwordx4 v255, s[48:51], s88 offen sc1 lds\n"
    "ds_read_b128 v[68:71], v245 offset:16384\n"
    "s_nop 3\n"
    "v_mfma_f32_16x16x32_bf16 a[12:15], v[52:55], v[152:155], a[12:15]\n"
    "v_mfma_f32_16x16x32_bf16 a[16:19], v[36:39], v[136:139], a[16:19]\n"
    "ds_read_b128 v[72:75], v245 offset:17408\n"
    "v_mfma_f32_16x16x32_bf16 a[16:19], v[56:59], v[152:155], a[16:19]\n"
    "ds_read_b128 v[76:79], v245 offset:18432\n"
    "v_mfma_f32_16x16x32_bf16 a[20:23], v[20:23], v[140:143], a[20:23]\n"
    "buffer_load_dwordx4 v255, s[48:51], s88 offen offset:1024 sc1 lds\n"
    "s_nop 3\n"
    "v_mfma_f32_16x16x32_bf16 a[20:23], v[40:43], v[156:159], a[20:23]\n"
    "ds_read_b128 v[116:119], v245 offset:19456\n"
    "v_mfma_f32_16x16x32_bf16 a[24:27], v[24:27], v[140:143], a[24:27]\n"
    "ds_read_b128 v[120:123], v245 offset:24576\n"
    "v_mfma_f32_16x16x32_bf16 a[24:27], v[44:47], v[156:159], a[24:27]\n"
    "v_mfma_f32_16x16x32_bf16 a[28:31], v[28:31], v[140:143], a[28:31]\n"
    "ds_read_b128 v[124:127], v245 offset:25600\n"
    "s_nop 3\n"
    "v_mfma_f32_16x16x32_bf16 a[28:31], v[48:51], v[156:159], a[28:31]\n"
    "s_add_u32 s88, s88, 0x1000\n"
    "ds_read_b128 v[128:131], v245 offset:26624\n"
    "v_mfma_f32_16x16x32_bf16 a[32:35], v[32:35], v[140:143], a[32:35]\n"
    "s_add_u32 m0, s68, 0xa000\n"
    "v_mfma_f32_16x16x32_bf16 a[32:35], v[52:55], v[156:159], a[32:35]\n"
    "buffer_load_dwordx4 v255, s[48:51], s88 offen sc1 lds\n"
    "ds_read_b128 v[132:135], v245 offset:27648\n"
    "v_mfma_f32_16x16x32_bf16 a[36:39], v[36:39], v[140:143], a[36:39]\n"
    "ds_read_b128 v[196:199], v252 offset:24576\n"
    "s_nop 3\n"
    "v_mfma_f32_16x16x32_bf16 a[36:39], v[56:59], v[156:159], a[36:39]\n"
    "ds_read_b128 v[212:215], v253 offset:0\n"
    "v_mfma_f32_16x16x32_bf16 a[40:43], v[20:23], v[144:147], a[40:43]\n"
    "v_mfma_f32_16x16x32_bf16 a[40:43], v[40:43], v[160:163], a[40:43]\n"
    "ds_read_b128 v[200:203], v252 offset:25600\n"
    "v_mfma_f32_16x16x32_bf16 a[44:47], v[24:27], v[144:147], a[44:47]\n"
    "buffer_load_dwordx4 v255, s[48:51], s88 offen offset:1024 sc1 lds\n"
    "ds_read_b128 a[60:63], v253 offset:1024\n"
    "s_nop 3\n"
    "v_mfma_f32_16x16x32_bf16 a[44:47], v[44:47], v[160:163], a[44:47]\n"
    "v_mfma_f32_16x16x32_bf16 a[48:51], v[28:31], v[144:147], a[48:51]\n"
    "ds_read_b128 v[204:207], v252 offset:26624\n"
    "v_mfma_f32_16x16x32_bf16 a[48:51], v[48:51], v[160:163], a[48:51]\n"
    "ds_read_b128 a[64:67], v253 offset:2048\n"
    "v_mfma_f32_16x16x32_bf16 a[52:55], v[32:35], v[144:147], a[52:55]\n"
    "s_add_u32 s88, s88, 0x1000\n"
    "s_nop 3\n"
    "v_mfma_f32_16x16x32_bf16 a[52:55], v[52:55], v[160:163], a[52:55]\n"
    "s_add_u32 m0, s68, 0xe000\n"
    "ds_read_b128 v[208:211], v252 offset:27648\n"
    "v_mfma_f32_16x16x32_bf16 a[56:59], v[36:39], v[144:147], a[56:59]\n"
    "s_add_u32 s92, s13, 0x4000\n"
    "buffer_load_dwordx4 v255, s[48:51], s92 offen sc1 lds\n"
    "ds_read_b128 a[68:71], v253 offset:3072\n"
    "v_mfma_f32_16x16x32_bf16 a[56:59], v[56:59], v[160:163], a[56:59]\n"
    "v_mfma_f32_16x16x32_bf16 v[0:3], v[20:23], v[148:151], v[0:3]\n"
    "s_nop 3\n"
    "v_mfma_f32_16x16x32_bf16 v[0:3], v[40:43], v[192:195], v[0:3]\n"
    "v_mfma_f32_16x16x32_bf16 v[4:7], v[24:27], v[148:151], v[4:7]\n"
    "v_mfma_f32_16x16x32_bf16 v[4:7], v[44:47], v[192:195], v[4:7]\n"
    "buffer_load_dwordx4 v255, s[48:51], s92 offen offset:1024 sc1 lds\n"
    "v_mfma_f32_16x16x32_bf16 v[8:11], v[28:31], v[148:151], v[8:11]\n"
    "s_nop 3\n"
    "v_mfma_f32_16x16x32_bf16 v[8:11], v[48:51], v[192:195], v[8:11]\n"
    "v_mfma_f32_16x16x32_bf16 v[12:15], v[32:35], v[148:151], v[12:15]\n"
    "v_mfma_f32_16x16x32_bf16 v[12:15], v[52:55], v[192:195], v[12:15]\n"
    "v_mfma_f32_16x16x32_bf16 v[16:19], v[36:39], v[148:151], v[16:19]\n"
    "s_nop 3\n"
    "v_mfma_f32_16x16x32_bf16 v[16:19], v[56:59], v[192:195], v[16:19]\n"
    "s_waitcnt lgkmcnt(7)\n"
    "v_mfma_f32_16x16x32_bf16 a[0:3], v[60:63], v[196:199], a[0:3]\n"
    "s_waitcnt lgkmcnt(6)\n"
    "v_mfma_f32_16x16x32_bf16 a[0:3], v[116:119], v[212:215], a[0:3]\n"
    "v_mfma_f32_16x16x32_bf16 a[4:7], v[64:67], v[196:199], a[4:7]\n"
    "s_nop 3\n"
    "v_mfma_f32_16x16x32_bf16 a[4:7], v[120:123], v[212:215], a[4:7]\n"
    "s_waitcnt vmcnt(8)\n"
    "s_waitcnt lgkmcnt(0)\n"
    "s_barrier\n"
    "v_mfma_f32_16x16x32_bf16 a[8:11], v[68:71], v[196:199], a[8:11]\n"
    "ds_read_b128 v[20:23], v245 offset:32768\n"
    "v_mfma_f32_16x16x32_bf16 a[8:11], v[124:127], v[212:215], a[8:11]\n"
    "s_cmp_eq_u32 s67, 4\n"
    "s_cselect_b32 s66, 0x4000, 0\n"
    "ds_read_b128 v[24:27], v245 offset:33792\n"
    "v_mfma_f32_16x16x32_bf16 a[12:15], v[72:75], v[196:199], a[12:15]\n"
    "s_add_u32 s88, s88, s66\n"
    "s_add_u32 m0, s68, 0x24000\n"
    "ds_read_b128 v[28:31], v245 offset:34816\n"
    "s_nop 3\n"
    "v_mfma_f32_16x16x32_bf16 a[12:15], v[128:131], v[212:215], a[12:15]\n"
    "buffer_load_dwordx4 v255, s[48:51], s88 offen sc1 lds\n"
    "v_mfma_f32_16x16x32_bf16 a[16:19], v[76:79], v[196:199], a[16:19]\n"
    "ds_read_b128 v[32:35], v245 offset:35840\n"
    "v_mfma_f32_16x16x32_bf16 a[16:19], v[132:135], v[212:215], a[16:19]\n"
    "buffer_load_dwordx4 v255, s[48:51], s88 offen offset:1024 sc1 lds\n"
    "ds_read_b128 v[36:39], v245 offset:40960\n"
    "v_mfma_f32_16x16x32_bf16 a[20:23], v[60:63], v[200:203], a[20:23]\n"
    "s_nop 3\n"
    "v_mfma_f32_16x16x32_bf16 a[20:23], v[116:119], a[60:63], a[20:23]\n"
    "ds_read_b128 v[40:43], v245 offset:41984\n"
    "v_mfma_f32_16x16x32_bf16 a[24:27], v[64:67], v[200:203], a[24:27]\n"
    "s_add_u32 s88, s88, 0x1000\n"
    "ds_read_b128 v[44:47], v245 offset:43008\n"
    "v_mfma_f32_16x16x32_bf16 a[24:27], v[120:123], a[60:63], a[24:27]\n"
    "s_add_u32 m0, s68, 0x6000\n"
    "v_mfma_f32_16x16x32_bf16 a[28:31], v[68:71], v[200:203], a[28:31]\n"
    "buffer_load_dwordx4 v255, s[48:51], s88 offen sc1 lds\n"
    "ds_read_b128 v[48:51], v245 offset:44032\n"
    "s_nop 3\n"
    "v_mfma_f32_16x16x32_bf16 a[28:31], v[124:127], a[60:63], a[28:31]\n"
    "ds_read_b128 v[52:55], v245 offset:49152\n"
    "v_mfma_f32_16x16x32_bf16 a[32:35], v[72:75], v[200:203], a[32:35]\n"
    "v_mfma_f32_16x16x32_bf16 a[32:35], v[128:131], a[60:63], a[32:35]\n"
    "ds_read_b128 v[56:59], v245 offset:50176\n"
    "buffer_load_dwordx4 v255, s[48:51], s88 offen offset:1024 sc1 lds\n"
    "v_mfma_f32_16x16x32_bf16 a[36:39], v[76:79], v[200:203], a[36:39]\n"
    "ds_read_b128 v[136:139], v252 offset:8192\n"
    "s_nop 3\n"
    "v_mfma_f32_16x16x32_bf16 a[36:39], v[132:135], a[60:63], a[36:39]\n"
    "ds_read_b128 v[152:155], v252 offset:16384\n"
    "v_mfma_f32_16x16x32_bf16 a[40:43], v[60:63], v[204:207], a[40:43]\n"
    "v_mfma_f32_16x16x32_bf16 a[40:43], v[116:119], a[64:67], a[40:43]\n"
    "s_add_u32 s88, s88, 0x1000\n"
    "ds_read_b128 v[140:143], v252 offset:9216\n"
    "v_mfma_f32_16x16x32_bf16 a[44:47], v[64:67], v[204:207], a[44:47]\n"
    "s_add_u32 m0, s68, 0x10000\n"
    "ds_read_b128 v[156:159], v252 offset:17408\n"
    "s_nop 3\n"
    "v_mfma_f32_16x16x32_bf16 a[44:47], v[120:123], a[64:67], a[44:47]\n"
    "buffer_load_dwordx4 v255, s[48:51], s88 offen sc1 lds\n"
    "v_mfma_f32_16x16x32_bf16 a[48:51], v[68:71], v[204:207], a[48:51]\n"
    "ds_read_b128 v[144:147], v252 offset:10240\n"
    "v_mfma_f32_16x16x32_bf16 a[48:51], v[124:127], a[64:67], a[48:51]\n"
    "ds_read_b128 v[160:163], v252 offset:18432\n"
    "v_mfma_f32_16x16x32_bf16 a[52:55], v[72:75], v[204:207], a[52:55]\n"
    "buffer_load_dwordx4 v255, s[48:51], s88 offen offset:1024 sc1 lds\n"
    "s_nop 3\n"
    "v_mfma_f32_16x16x32_bf16 a[52:55], v[128:131], a[64:67], a[52:55]\n"
    "ds_read_b128 v[148:151], v252 offset:11264\n"
    "v_mfma_f32_16x16x32_bf16 a[56:59], v[76:79], v[204:207], a[56:59]\n"
    "ds_read_b128 v[192:195], v252 offset:19456\n"
    "v_mfma_f32_16x16x32_bf16 a[56:59], v[132:135], a[64:67], a[56:59]\n"
    "s_add_u32 s88, s88, 0x1000\n"
    "v_mfma_f32_16x16x32_bf16 v[0:3], v[60:63], v[208:211], v[0:3]\n"
    "s_add_u32 m0, s68, 0x12000\n"
    "s_nop 3\n"
    "v_mfma_f32_16x16x32_bf16 v[0:3], v[116:119], a[68:71], v[0:3]\n"
    "s_add_u32 s92, s14, 0\n"
    "buffer_load_dwordx4 v255, s[48:51], s92 offen sc1 lds\n"
    "v_mfma_f32_16x16x32_bf16 v[4:7], v[64:67], v[208:211], v[4:7]\n"
    "v_mfma_f32_16x16x32_bf16 v[4:7], v[120:123], a[68:71], v[4:7]\n"
    "v_mfma_f32_16x16x32_bf16 v[8:11], v[68:71], v[208:211], v[8:11]\n"
    "buffer_load_dwordx4 v255, s[48:51], s92 offen offset:1024 sc1 lds\n"
    "s_nop 3\n"
    "v_mfma_f32_16x16x32_bf16 v[8:11], v[124:127], a[68:71], v[8:11]\n"
    "v_mfma_f32_16x16x32_bf16 v[12:15], v[72:75], v[208:211], v[12:15]\n"
    "v_mfma_f32_16x16x32_bf16 v[12:15], v[128:131], a[68:71], v[12:15]\n"
    "v_mfma_f32_16x16x32_bf16 v[16:19], v[76:79], v[208:211], v[16:19]\n"
    "s_nop 3\n"
    "v_mfma_f32_16x16x32_bf16 v[16:19], v[132:135], a[68:71], v[16:19]\n"
    "s_waitcnt lgkmcnt(7)\n"
    "v_mfma_f32_16x16x32_bf16 a[0:3], v[20:23], v[136:139], a[0:3]\n"
    "s_waitcnt lgkmcnt(6)\n"
    "v_mfma_f32_16x16x32_bf16 a[0:3], v[40:43], v[152:155], a[0:3]\n"
    "v_mfma_f32_16x16x32_bf16 a[4:7], v[24:27], v[136:139], a[4:7]\n"
    "s_nop 3\n"
    "v_mfma_f32_16x16x32_bf16 a[4:7], v[44:47], v[152:155], a[4:7]\n"
    "s_waitcnt vmcnt(10)\n"
    "s_waitcnt lgkmcnt(0)\n"
    "s_barrier\n"
    "v_mfma_f32_16x16x32_bf16 a[8:11], v[28:31], v[136:139], a[8:11]\n"
    "ds_read_b128 v[60:63], v245 offset:51200\n"
    "v_mfma_f32_16x16x32_bf16 a[8:11], v[48:51], v[152:155], a[8:11]\n"
    "s_add_u32 m0, s68, 0x14000\n"
    "ds_read_b128 v[64:67], v245 offset:52224\n"
    "v_mfma_f32_16x16x32_bf16 a[12:15], v[32:35], v[136:139], a[12:15]\n"
    "buffer_load_dwordx4 v255, s[48:51], s88 offen sc1 lds\n"
    "ds_read_b128 v[68:71], v245 offset:57344\n"
    "s_nop 3\n"
    "v_mfma_f32_16x16x32_bf16 a[12:15], v[52:55], v[152:155], a[12:15]\n"
    "v_mfma_f32_16x16x32_bf16 a[16:19], v[36:39], v[136:139], a[16:19]\n"
    "ds_read_b128 v[72:75], v245 offset:58368\n"
    "v_mfma_f32_16x16x32_bf16 a[16:19], v[56:59], v[152:155], a[16:19]\n"
    "buffer_load_dwordx4 v255, s[48:51], s88 offen offset:1024 sc1 lds\n"
    "ds_read_b128 v[76:79], v245 offset:59392\n"
    "v_mfma_f32_16x16x32_bf16 a[20:23], v[20:23], v[140:143], a[20:23]\n"
    "s_nop 3\n"
    "v_mfma_f32_16x16x32_bf16 a[20:23], v[40:43], v[156:159], a[20:23]\n"
    "ds_read_b128 v[116:119], v245 offset:60416\n"
    "v_mfma_f32_16x16x32_bf16 a[24:27], v[24:27], v[140:143], a[24:27]\n"
    "s_add_u32 s88, s88, 0x1000\n"
    "ds_read_b128 v[120:123], v246 offset:0\n"
    "v_mfma_f32_16x16x32_bf16 a[24:27], v[44:47], v[156:159], a[24:27]\n"
    "s_cmp_eq_u32 s67, 5\n"
    "s_cselect_b32 s66, 0x4000, 0\n"
    "v_mfma_f32_16x16x32_bf16 a[28:31], v[28:31], v[140:143], a[28:31]\n"
    "s_add_u32 s88, s88, s66\n"
    "s_add_u32 m0, s68, 0x16000\n"
    "ds_read_b128 v[124:127], v246 offset:1024\n"
    "s_nop 3\n"
    "v_mfma_f32_16x16x32_bf16 a[28:31], v[48:51], v[156:159], a[28:31]\n"
    "buffer_load_dwordx4 v255, s[48:51], s88 offen sc1 lds\n"
    "ds_read_b128 v[128:131], v246 offset:2048\n"
    "v_mfma_f32_16x16x32_bf16 a[32:35], v[32:35], v[140:143], a[32:35]\n"
    "v_mfma_f32_16x16x32_bf16 a[32:35], v[52:55], v[156:159], a[32:35]\n"
    "ds_read_b128 v[132:135], v246 offset:3072\n"
    "buffer_load_dwordx4 v255, s[48:51], s88 offen offset:1024 sc1 lds\n"
    "v_mfma_f32_16x16x32_bf16 a[36:39], v[36:39], v[140:143], a[36:39]\n"
    "ds_read_b128 v[196:199], v252 offset:32768\n"
    "s_nop 3\n"
    "v_mfma_f32_16x16x32_bf16 a[36:39], v[56:59], v[156:159], a[36:39]\n"
    "ds_read_b128 v[212:215], v252 offset:40960\n"
    "v_mfma_f32_16x16x32_bf16 a[40:43], v[20:23], v[144:147], a[40:43]\n"
    "v_mfma_f32_16x16x32_bf16 a[40:43], v[40:43], v[160:163], a[40:43]\n"
    "s_add_u32 s88, s88, 0x1000\n"
    "ds_read_b128 v[200:203], v252 offset:33792\n"
    "v_mfma_f32_16x16x32_bf16 a[44:47], v[24:27], v[144:147], a[44:47]\n"
    "s_add_u32 m0, s68, 0x2000\n"
    "ds_read_b128 a[60:63], v252 offset:41984\n"
    "s_nop 3\n"
    "v_mfma_f32_16x16x32_bf16 a[44:47], v[44:47], v[160:163], a[44:47]\n"
    "buffer_load_dwordx4 v255, s[48:51], s88 offen sc1 lds\n"
    "v_mfma_f32_16x16x32_bf16 a[48:51], v[28:31], v[144:147], a[48:51]\n"
    "ds_read_b128 v[204:207], v252 offset:34816\n"
    "v_mfma_f32_16x16x32_bf16 a[48:51], v[48:51], v[160:163], a[48:51]\n"
    "ds_read_b128 a[64:67], v252 offset:43008\n"
    "v_mfma_f32_16x16x32_bf16 a[52:55], v[32:35], v[144:147], a[52:55]\n"
    "buffer_load_dwordx4 v255, s[48:51], s88 offen offset:1024 sc1 lds\n"
    "s_nop 3\n"
    "v_mfma_f32_16x16x32_bf16 a[52:55], v[52:55], v[160:163], a[52:55]\n"
    "ds_read_b128 v[208:211], v252 offset:35840\n"
    "v_mfma_f32_16x16x32_bf16 a[56:59], v[36:39], v[144:147], a[56:59]\n"
    "ds_read_b128 a[68:71], v252 offset:44032\n"
    "v_mfma_f32_16x16x32_bf16 a[56:59], v[56:59], v[160:163], a[56:59]\n"
    "s_add_u32 s88, s88, 0x1000\n"
    "v_mfma_f32_16x16x32_bf16 v[0:3], v[20:23], v[148:151], v[0:3]\n"
    "s_add_u32 m0, s68, 0x4000\n"
    "s_nop 3\n"
    "v_mfma_f32_16x16x32_bf16 v[0:3], v[40:43], v[192:195], v[0:3]\n"
    "s_add_u32 s92, s14, 0x1000\n"
    "buffer_load_dwordx4 v255, s[48:51], s92 offen sc1 lds\n"
    "v_mfma_f32_16x16x32_bf16 v[4:7], v[24:27], v[148:151], v[4:7]\n"
    "v_mfma_f32_16x16x32_bf16 v[4:7], v[44:47], v[192:195], v[4:7]\n"
    "v_mfma_f32_16x16x32_bf16 v[8:11], v[28:31], v[148:151], v[8:11]\n"
    "buffer_load_dwordx4 v255, s[48:51], s92 offen offset:1024 sc1 lds\n"
    "s_nop 3\n"
    "v_mfma_f32_16x16x32_bf16 v[8:11], v[48:51], v[192:195], v[8:11]\n"
    "v_mfma_f32_16x16x32_bf16 v[12:15], v[32:35], v[148:151], v[12:15]\n"
    "v_mfma_f32_16x16x32_bf16 v[12:15], v[52:55], v[192:195], v[12:15]\n"
    "v_mfma_f32_16x16x32_bf16 v[16:19], v[36:39], v[148:151], v[16:19]\n"
    "s_nop 3\n"
    "v_mfma_f32_16x16x32_bf16 v[16:19], v[56:59], v[192:195], v[16:19]\n"
    "s_waitcnt lgkmcnt(7)\n"
    "v_mfma_f32_16x16x32_bf16 a[0:3], v[60:63], v[196:199], a[0:3]\n"
    "s_waitcnt lgkmcnt(6)\n"
    "s_mov_b32 s85, 0x7400\n"
    "v_mfma_f32_16x16x32_bf16 a[0:3], v[116:119], v[212:215], a[0:3]\n"
    "v_mfma_f32_16x16x32_bf16 a[4:7], v[64:67], v[196:199], a[4:7]\n"
    "v_mfma_f32_16x16x32_bf16 a[4:7], v[120:123], v[212:215], a[4:7]\n"
    "s_waitcnt vmcnt(12)\n"
    "s_waitcnt lgkmcnt(0)\n"
    "s_barrier\n"
    "v_mfma_f32_16x16x32_bf16 a[8:11], v[68:71], v[196:199], a[8:11]\n"
    "ds_read_b128 v[20:23], v246 offset:8192\n"
    "v_mfma_f32_16x16x32_bf16 a[8:11], v[124:127], v[212:215], a[8:11]\n"
    "s_add_u32 m0, s68, 0x18000\n"
    "ds_read_b128 v[24:27], v246 offset:9216\n"
    "v_mfma_f32_16x16x32_bf16 a[12:15], v[72:75], v[196:199], a[12:15]\n"
    "buffer_load_dwordx4 v255, s[48:51], s88 offen sc1 lds\n"
    "ds_read_b128 v[28:31], v246 offset:10240\n"
    "v_mfma_f32_16x16x32_bf16 a[12:15], v[128:131], v[212:215], a[12:15]\n"
    "v_mfma_f32_16x16x32_bf16 a[16:19], v[76:79], v[196:199], a[16:19]\n"
    "ds_read_b128 v[32:35], v246 offset:11264\n"
    "v_mfma_f32_16x16x32_bf16 a[16:19], v[132:135], v[212:215], a[16:19]\n"
    "buffer_load_dwordx4 v255, s[48:51], s88 offen offset:1024 sc1 lds\n"
    "ds_read_b128 v[36:39], v246 offset:24576\n"
    "v_mfma_f32_16x16x32_bf16 a[20:23], v[60:63], v[200:203], a[20:23]\n"
    "buffer_load_dwordx4 v[216:219], v255, s[48:51], s85 offen sc1\n"
    "v_mfma_f32_16x16x32_bf16 a[20:23], v[116:119], a[60:63], a[20:23]\n"
    "ds_read_b128 v[40:43], v246 offset:25600\n"
    "v_mfma_f32_16x16x32_bf16 a[24:27], v[64:67], v[200:203], a[24:27]\n"
    "s_add_u32 s88, s88, 0x1000\n"
    "ds_read_b128 v[44:47], v246 offset:26624\n"
    "v_mfma_f32_16x16x32_bf16 a[24:27], v[120:123], a[60:63], a[24:27]\n"
    "s_add_u32 m0, s68, 0x1a000\n"
    "v_mfma_f32_16x16x32_bf16 a[28:31], v[68:71], v[200:203], a[28:31]\n"
    "buffer_load_dwordx4 v255, s[48:51], s88 offen sc1 lds\n"
    "ds_read_b128 v[48:51], v246 offset:27648\n"
    "v_mfma_f32_16x16x32_bf16 a[28:31], v[124:127], a[60:63], a[28:31]\n"
    "ds_read_b128 v[52:55], v244 offset:0\n"
    "v_mfma_f32_16x16x32_bf16 a[32:35], v[72:75], v[200:203], a[32:35]\n"
    "v_mfma_f32_16x16x32_bf16 a[32:35], v[128:131], a[60:63], a[32:35]\n"
    "ds_read_b128 v[56:59], v244 offset:1024\n"
    "buffer_load_dwordx4 v255, s[48:51], s88 offen offset:1024 sc1 lds\n"
    "v_mfma_f32_16x16x32_bf16 a[36:39], v[76:79], v[200:203], a[36:39]\n"
    "ds_read_b128 v[136:139], v254 offset:16384\n"
    "v_mfma_f32_16x16x32_bf16 a[36:39], v[132:135], a[60:63], a[36:39]\n"
    "ds_read_b128 v[152:155], v252 offset:24576\n"
    "v_mfma_f32_16x16x32_bf16 a[40:43], v[60:63], v[204:207], a[40:43]\n"
    "v_mfma_f32_16x16x32_bf16 a[40:43], v[116:119], a[64:67], a[40:43]\n"
    "s_add_u32 s88, s88, 0x1000\n"
    "ds_read_b128 v[140:143], v254 offset:17408\n"
    "v_mfma_f32_16x16x32_bf16 a[44:47], v[64:67], v[204:207], a[44:47]\n"
    "s_cmp_eq_u32 s67, 6\n"
    "s_cselect_b32 s66, 0x4000, 0\n"
    "s_add_u32 s88, s88, s66\n"
    "s_add_u32 m0, s68, 0x8000\n"
    "v_mfma_f32_16x16x32_bf16 a[44:47], v[120:123], a[64:67], a[44:47]\n"
    "buffer_load_dwordx4 v255, s[48:51], s88 offen sc1 lds\n"
    "ds_read_b128 v[156:159], v252 offset:25600\n"
    "v_mfma_f32_16x16x32_bf16 a[48:51], v[68:71], v[204:207], a[48:51]\n"
    "ds_read_b128 v[144:147], v254 offset:18432\n"
    "v_mfma_f32_16x16x32_bf16 a[48:51], v[124:127], a[64:67], a[48:51]\n"
    "ds_read_b128 v[160:163], v252 offset:26624\n"
    "v_mfma_f32_16x16x32_bf16 a[52:55], v[72:75], v[204:207], a[52:55]\n"
    "buffer_load_dwordx4 v255, s[48:51], s88 offen offset:1024 sc1 lds\n"
    "v_mfma_f32_16x16x32_bf16 a[52:55], v[128:131], a[64:67], a[52:55]\n"
    "ds_read_b128 v[148:151], v254 offset:19456\n"
    "v_mfma_f32_16x16x32_bf16 a[56:59], v[76:79], v[204:207], a[56:59]\n"
    "ds_read_b128 v[192:195], v252 offset:27648\n"
    "v_mfma_f32_16x16x32_bf16 a[56:59], v[132:135], a[64:67], a[56:59]\n"
    "s_add_u32 s88, s88, 0x1000\n"
    "v_mfma_f32_16x16x32_bf16 v[0:3], v[60:63], v[208:211], v[0:3]\n"
    "s_add_u32 s95, s94, 0x9000\n"
    "buffer_load_dwordx4 a[208:211], v255, s[48:51], s95 offen sc1\n"
    "v_mfma_f32_16x16x32_bf16 v[0:3], v[116:119], a[68:71], v[0:3]\n"
    "v_mfma_f32_16x16x32_bf16 v[4:7], v[64:67], v[208:211], v[4:7]\n"
    "v_mfma_f32_16x16x32_bf16 v[4:7], v[120:123], a[68:71], v[4:7]\n"
    "v_mfma_f32_16x16x32_bf16 v[8:11], v[68:71], v[208:211], v[8:11]\n"
    "buffer_load_dwordx4 a[212:215], v255, s[48:51], s95 offen offset:1024 sc1\n"
    "v_mfma_f32_16x16x32_bf16 v[8:11], v[124:127], a[68:71], v[8:11]\n"
    "v_mfma_f32_16x16x32_bf16 v[12:15], v[72:75], v[208:211], v[12:15]\n"
    "v_mfma_f32_16x16x32_bf16 v[12:15], v[128:131], a[68:71], v[12:15]\n"
    "v_mfma_f32_16x16x32_bf16 v[16:19], v[76:79], v[208:211], v[16:19]\n"
    "v_mfma_f32_16x16x32_bf16 v[16:19], v[132:135], a[68:71], v[16:19]\n"
    "s_waitcnt lgkmcnt(7)\n"
    "v_mfma_f32_16x16x32_bf16 a[0:3], v[20:23], v[136:139], a[0:3]\n"
    "s_waitcnt lgkmcnt(6)\n"
    "v_mfma_f32_16x16x32_bf16 a[0:3], v[40:43], v[152:155], a[0:3]\n"
    "v_mfma_f32_16x16x32_bf16 a[4:7], v[24:27], v[136:139], a[4:7]\n"
    "v_mfma_f32_16x16x32_bf16 a[4:7], v[44:47], v[152:155], a[4:7]\n"
    "s_waitcnt vmcnt(15)\n"
    "s_waitcnt lgkmcnt(0)\n"
    "s_barrier\n"
    "v_mfma_f32_16x16x32_bf16 a[8:11], v[28:31], v[136:139], a[8:11]\n"
    "ds_read_b128 v[60:63], v244 offset:2048\n"
    "v_mfma_f32_16x16x32_bf16 a[8:11], v[48:51], v[152:155], a[8:11]\n"
    "s_add_u32 m0, s68, 0xa000\n"
    "ds_read_b128 v[64:67], v244 offset:3072\n"
    "v_mfma_f32_16x16x32_bf16 a[12:15], v[32:35], v[136:139], a[12:15]\n"
    "buffer_load_dwordx4 v255, s[48:51], s88 offen sc1 lds\n"
    "ds_read_b128 v[68:71], v244 offset:49152\n"
    "v_mfma_f32_16x16x32_bf16 a[12:15], v[52:55], v[152:155], a[12:15]\n"
    "v_mfma_f32_16x16x32_bf16 a[16:19], v[36:39], v[136:139], a[16:19]\n"
    "ds_read_b128 v[72:75], v244 offset:50176\n"
    "v_mfma_f32_16x16x32_bf16 a[16:19], v[56:59], v[152:155], a[16:19]\n"
    "buffer_load_dwordx4 v255, s[48:51], s88 offen offset:1024 sc1 lds\n"
    "ds_read_b128 v[76:79], v244 offset:51200\n"
    "v_mfma_f32_16x16x32_bf16 a[20:23], v[20:23], v[140:143], a[20:23]\n"
    "v_mfma_f32_16x16x32_bf16 a[20:23], v[40:43], v[156:159], a[20:23]\n"
    "ds_read_b128 v[116:119], v244 offset:52224\n"
    "v_mfma_f32_16x16x32_bf16 a[24:27], v[24:27], v[140:143], a[24:27]\n"
    "s_add_u32 s88, s88, 0x1000\n"
    "ds_read_b128 v[120:123], v244 offset:57344\n"
    "v_mfma_f32_16x16x32_bf16 a[24:27], v[44:47], v[156:159], a[24:27]\n"
    "s_add_u32 m0, s68, 0x1c000\n"
    "v_mfma_f32_16x16x32_bf16 a[28:31], v[28:31], v[140:143], a[28:31]\n"
    "buffer_load_dwordx4 v255, s[48:51], s88 offen sc1 lds\n"
    "ds_read_b128 v[124:127], v244 offset:58368\n"
    "v_mfma_f32_16x16x32_bf16 a[28:31], v[48:51], v[156:159], a[28:31]\n"
    "ds_read_b128 v[128:131], v244 offset:59392\n"
    "v_mfma_f32_16x16x32_bf16 a[32:35], v[32:35], v[140:143], a[32:35]\n"
    "v_mfma_f32_16x16x32_bf16 a[32:35], v[52:55], v[156:159], a[32:35]\n"
    "ds_read_b128 v[132:135], v244 offset:60416\n"
    "buffer_load_dwordx4 v255, s[48:51], s88 offen offset:1024 sc1 lds\n"
    "v_mfma_f32_16x16x32_bf16 a[36:39], v[36:39], v[140:143], a[36:39]\n"
    "ds_read_b128 v[196:199], v253 offset:0\n"
    "v_mfma_f32_16x16x32_bf16 a[36:39], v[56:59], v[156:159], a[36:39]\n"
    "ds_read_b128 v[212:215], v253 offset:16384\n"
    "v_mfma_f32_16x16x32_bf16 a[40:43], v[20:23], v[144:147], a[40:43]\n"
    "v_mfma_f32_16x16x32_bf16 a[40:43], v[40:43], v[160:163], a[40:43]\n"
    "s_add_u32 s88, s88, 0x1000\n"
    "ds_read_b128 v[200:203], v253 offset:1024\n"
    "v_mfma_f32_16x16x32_bf16 a[44:47], v[24:27], v[144:147], a[44:47]\n"
    "s_add_u32 m0, s68, 0x1e000\n"
    "ds_read_b128 a[60:63], v253 offset:17408\n"
    "v_mfma_f32_16x16x32_bf16 a[44:47], v[44:47], v[160:163], a[44:47]\n"
    "buffer_load_dwordx4 v255, s[48:51], s88 offen sc1 lds\n"
    "v_mfma_f32_16x16x32_bf16 a[48:51], v[28:31], v[144:147], a[48:51]\n"
    "ds_read_b128 v[204:207], v253 offset:2048\n"
    "v_mfma_f32_16x16x32_bf16 a[48:51], v[48:51], v[160:163], a[48:51]\n"
    "ds_read_b128 a[64:67], v253 offset:18432\n"
    "v_mfma_f32_16x16x32_bf16 a[52:55], v[32:35], v[144:147], a[52:55]\n"
    "buffer_load_dwordx4 v255, s[48:51], s88 offen offset:1024 sc1 lds\n"
    "v_mfma_f32_16x16x32_bf16 a[52:55], v[52:55], v[160:163], a[52:55]\n"
    "ds_read_b128 v[208:211], v253 offset:3072\n"
    "v_mfma_f32_16x16x32_bf16 a[56:59], v[36:39], v[144:147], a[56:59]\n"
    "ds_read_b128 a[68:71], v253 offset:19456\n"
    "v_mfma_f32_16x16x32_bf16 a[56:59], v[56:59], v[160:163], a[56:59]\n"
    "s_add_u32 s88, s88, 0x1000\n"
    "v_mfma_f32_16x16x32_bf16 v[0:3], v[20:23], v[148:151], v[0:3]\n"
    "buffer_load_dwordx4 a[216:219], v255, s[48:51], s95 offen offset:2048 sc1\n"
    "v_mfma_f32_16x16x32_bf16 v[0:3], v[40:43], v[192:195], v[0:3]\n"
    "v_mfma_f32_16x16x32_bf16 v[4:7], v[24:27], v[148:151], v[4:7]\n"
    "v_mfma_f32_16x16x32_bf16 v[4:7], v[44:47], v[192:195], v[4:7]\n"
    "v_mfma_f32_16x16x32_bf16 v[8:11], v[28:31], v[148:151], v[8:11]\n"
    "buffer_load_dwordx4 a[220:223], v255, s[48:51], s95 offen offset:3072 sc1\n"
    "v_mfma_f32_16x16x32_bf16 v[8:11], v[48:51], v[192:195], v[8:11]\n"
    "v_mfma_f32_16x16x32_bf16 v[12:15], v[32:35], v[148:151], v[12:15]\n"
    "v_mfma_f32_16x16x32_bf16 v[12:15], v[52:55], v[192:195], v[12:15]\n"
    "v_mfma_f32_16x16x32_bf16 v[16:19], v[36:39], v[148:151], v[16:19]\n"
    "v_mfma_f32_16x16x32_bf16 v[16:19], v[56:59], v[192:195], v[16:19]\n"
    "s_waitcnt lgkmcnt(7)\n"
    "v_mfma_f32_16x16x32_bf16 a[0:3], v[60:63], v[196:199], a[0:3]\n"
    "s_waitcnt lgkmcnt(6)\n"
    "v_mfma_f32_16x16x32_bf16 a[0:3], v[116:119], v[212:215], a[0:3]\n"
    "v_mfma_f32_16x16x32_bf16 a[4:7], v[64:67], v[196:199], a[4:7]\n"
    "v_mfma_f32_16x16x32_bf16 a[4:7], v[120:123], v[212:215], a[4:7]\n"
    "s_waitcnt vmcnt(12)\n"
    "s_waitcnt lgkmcnt(0)\n"
    "s_barrier\n"
    "v_mfma_f32_16x16x32_bf16 a[8:11], v[68:71], v[196:199], a[8:11]\n"
    "ds_read_b128 v[20:23], v245 offset:8192\n"
    "v_mfma_f32_16x16x32_bf16 a[8:11], v[124:127], v[212:215], a[8:11]\n"
    "s_add_u32 s95, s94, 0xa000\n"
    "buffer_load_dwordx4 a[224:227], v255, s[48:51], s95 offen sc1\n"
    "v_mfma_f32_16x16x32_bf16 a[12:15], v[72:75], v[196:199], a[12:15]\n"
    "ds_read_b128 v[24:27], v245 offset:9216\n"
    "v_mfma_f32_16x16x32_bf16 a[12:15], v[128:131], v[212:215], a[12:15]\n"
    "v_mfma_f32_16x16x32_bf16 a[16:19], v[76:79], v[196:199], a[16:19]\n"
    "ds_read_b128 v[28:31], v245 offset:10240\n"
    "v_mfma_f32_16x16x32_bf16 a[16:19], v[132:135], v[212:215], a[16:19]\n"
    "v_mfma_f32_16x16x32_bf16 a[20:23], v[60:63], v[200:203], a[20:23]\n"
    "ds_read_b128 v[32:35], v245 offset:11264\n"
    "buffer_load_dwordx4 a[228:231], v255, s[48:51], s95 offen offset:1024 sc1\n"
    "v_mfma_f32_16x16x32_bf16 a[20:23], v[116:119], a[60:63], a[20:23]\n"
    "v_mfma_f32_16x16x32_bf16 a[24:27], v[64:67], v[200:203], a[24:27]\n"
    "ds_read_b128 v[36:39], v244 offset:16384\n"
    "v_mfma_f32_16x16x32_bf16 a[24:27], v[120:123], a[60:63], a[24:27]\n"
    "v_mfma_f32_16x16x32_bf16 a[28:31], v[68:71], v[200:203], a[28:31]\n"
    "ds_read_b128 v[40:43], v244 offset:17408\n"
    "v_mfma_f32_16x16x32_bf16 a[28:31], v[124:127], a[60:63], a[28:31]\n"
    "v_mfma_f32_16x16x32_bf16 a[32:35], v[72:75], v[200:203], a[32:35]\n"
    "buffer_load_dwordx4 a[232:235], v255, s[48:51], s95 offen offset:2048 sc1\n"
    "v_mfma_f32_16x16x32_bf16 a[32:35], v[128:131], a[60:63], a[32:35]\n"
    "ds_read_b128 v[44:47], v244 offset:18432\n"
    "v_mfma_f32_16x16x32_bf16 a[36:39], v[76:79], v[200:203], a[36:39]\n"
    "v_mfma_f32_16x16x32_bf16 a[36:39], v[132:135], a[60:63], a[36:39]\n"
    "ds_read_b128 v[48:51], v244 offset:19456\n"
    "v_mfma_f32_16x16x32_bf16 a[40:43], v[60:63], v[204:207], a[40:43]\n"
    "v_xor_b32_e64 v217, v216, v217\n"
    "v_mfma_f32_16x16x32_bf16 a[40:43], v[116:119], a[64:67], a[40:43]\n"
    "ds_read_b128 v[136:139], v253 offset:24576\n"
    "v_mfma_f32_16x16x32_bf16 a[44:47], v[64:67], v[204:207], a[44:47]\n"
    "buffer_load_dwordx4 a[236:239], v255, s[48:51], s95 offen offset:3072 sc1\n"
    "v_xor_b32_e64 v218, v216, v218\n"
    "v_mfma_f32_16x16x32_bf16 a[44:47], v[120:123], a[64:67], a[44:47]\n"
    "ds_read_b128 v[140:143], v252 offset:8192\n"
    "v_mfma_f32_16x16x32_bf16 a[48:51], v[68:71], v[204:207], a[48:51]\n"
    "v_xor_b32_e64 v219, v216, v219\n"
    "v_mfma_f32_16x16x32_bf16 a[48:51], v[124:127], a[64:67], a[48:51]\n"
    "ds_read_b128 v[144:147], v253 offset:32768\n"
    "v_mfma_f32_16x16x32_bf16 a[52:55], v[72:75], v[204:207], a[52:55]\n"
    "s_add_u32 s95, s94, 0xb000\n"
    "v_xor_b32_e64 v216, s78, v216\n"
    "v_mfma_f32_16x16x32_bf16 a[52:55], v[128:131], a[64:67], a[52:55]\n"
    "buffer_load_dwordx4 a[240:243], v255, s[48:51], s95 offen sc1\n"
    "ds_read_b128 v[148:151], v253 offset:40960\n"
    "v_mfma_f32_16x16x32_bf16 a[56:59], v[76:79], v[204:207], a[56:59]\n"
    "v_or3_b32 v216, v216, v217, v218\n"
    "v_mfma_f32_16x16x32_bf16 a[56:59], v[132:135], a[64:67], a[56:59]\n"
    "v_mfma_f32_16x16x32_bf16 v[0:3], v[60:63], v[208:211], v[0:3]\n"
    "v_or_b32_e64 v216, v216, v219\n"
    "v_mfma_f32_16x16x32_bf16 v[0:3], v[116:119], a[68:71], v[0:3]\n"
    "v_mfma_f32_16x16x32_bf16 v[4:7], v[64:67], v[208:211], v[4:7]\n"
    "v_cmp_eq_u32_e32 vcc, 0, v216\n"
    "v_mfma_f32_16x16x32_bf16 v[4:7], v[120:123], a[68:71], v[4:7]\n"
    "buffer_load_dwordx4 a[244:247], v255, s[48:51], s95 offen offset:1024 sc1\n"
    "v_mfma_f32_16x16x32_bf16 v[8:11], v[68:71], v[208:211], v[8:11]\n"
    "s_cmp_eq_u64 vcc, -1\n"
    "s_cbranch_scc1 POK5_%=\n"
    "s_mov_b32 s61, 20000\n"
    "PSP5_%=:\n"
    "s_sleep 4\n"
    "buffer_load_dwordx4 v[216:219], v255, s[48:51], s85 offen sc1\n"
    "s_waitcnt vmcnt(0)\n"
    "v_xor_b32_e64 v217, v216, v217\n"
    "v_xor_b32_e64 v218, v216, v218\n"
    "v_xor_b32_e64 v219, v216, v219\n"
    "v_xor_b32_e64 v216, s78, v216\n"
    "v_or3_b32 v216, v216, v217, v218\n"
    "v_or_b32_e64 v216, v216, v219\n"
    "v_cmp_eq_u32_e32 vcc, 0, v216\n"
    "s_nop 1\n"
    "s_cmp_eq_u64 vcc, -1\n"
    "s_cbranch_scc1 POK5_%=\n"
    "s_sub_u32 s61, s61, 1\n"
    "s_cmp_lg_u32 s61, 0\n"
    "s_cbranch_scc1 PSP5_%=\n"
    "POK5_%=:\n"
    "v_mfma_f32_16x16x32_bf16 v[8:11], v[124:127], a[68:71], v[8:11]\n"
    "PG9_%=:\n"
    "s_cbranch_execz PG13_%=\n"
    "v_mfma_f32_16x16x32_bf16 v[12:15], v[72:75], v[208:211], v[12:15]\n"
    "v_mfma_f32_16x16x32_bf16 v[12:15], v[128:131], a[68:71], v[12:15]\n"
    "v_mfma_f32_16x16x32_bf16 v[16:19], v[76:79], v[208:211], v[16:19]\n"
    "v_mfma_f32_16x16x32_bf16 v[16:19], v[132:135], a[68:71], v[16:19]\n"
    "s_waitcnt lgkmcnt(3)\n"
    "v_mfma_f32_16x16x32_bf16 a[0:3], v[20:23], v[136:139], a[0:3]\n"
    "s_waitcnt lgkmcnt(2)\n"
    "v_lshrrev_b32_e32 v226, 4, v255\n"
    "v_and_b32_e32 v227, 7, v226\n"
    "v_mfma_f32_16x16x32_bf16 a[0:3], v[40:43], v[140:143], a[0:3]\n"
    "s_waitcnt lgkmcnt(1)\n"
    "v_lshrrev_b32_e32 v228, 5, v226\n"
    "v_mfma_f32_16x16x32_bf16 a[0:3], v[172:175], v[144:147], a[0:3]\n"
    "s_waitcnt vmcnt(8) lgkmcnt(0)\n"
    "s_waitcnt lgkmcnt(0)\n"
    "s_barrier\n"
    "v_bfe_u32 v229, v226, 3, 2\n"
    "v_mfma_f32_16x16x32_bf16 a[0:3], a[128:131], v[148:151], a[0:3]\n"
    "ds_read_b128 v[152:155], v252 offset:32768\n"
    "v_mul_u32_u24_e32 v245, 0x500, v227\n"
    "v_mfma_f32_16x16x32_bf16 a[4:7], v[24:27], v[136:139], a[4:7]\n"
    "s_add_u32 m0, s68, 0x20000\n"
    "ds_read_b128 v[156:159], v252 offset:40960\n"
    "v_lshl_add_u32 v245, v229, 3, v245\n"
    "v_mfma_f32_16x16x32_bf16 a[4:7], v[44:47], v[140:143], a[4:7]\n"
    "s_add_u32 s92, s68, 0x13400\n"
    "buffer_load_dwordx4 v255, s[48:51], s92 offen sc1 lds\n"
    "v_mul_u32_u24_e32 v248, 0xa0, v228\n"
    "v_mfma_f32_16x16x32_bf16 a[4:7], v[176:179], v[144:147], a[4:7]\n"
    "ds_read_b128 v[160:163], v253 offset:49152\n"
    "v_add_u32_e32 v248, v248, v245\n"
    "v_mfma_f32_16x16x32_bf16 a[4:7], a[132:135], v[148:151], a[4:7]\n"
    "ds_read_b128 v[192:195], v253 offset:57344\n"
    "v_lshl_add_u32 v245, v228, 6, v245\n"
    "v_mfma_f32_16x16x32_bf16 a[8:11], v[28:31], v[136:139], a[8:11]\n"
    "buffer_load_dwordx4 v255, s[48:51], s92 offen offset:1024 sc1 lds\n"
    "v_lshlrev_b32_e64 v246, 4, v229\n"
    "v_mfma_f32_16x16x32_bf16 a[8:11], v[48:51], v[140:143], a[8:11]\n"
    "v_mul_u32_u24_e32 v249, 0x140, v228\n"
    "v_add_u32_e64 v249, v249, v246\n"
    "v_mfma_f32_16x16x32_bf16 a[8:11], v[180:183], v[144:147], a[8:11]\n"
    "s_add_u32 m0, s68, 0x24000\n"
    "ds_read_b128 v[196:199], v253 offset:25600\n"
    "v_lshl_add_u32 v246, v228, 7, v246\n"
    "v_mfma_f32_16x16x32_bf16 a[8:11], a[136:139], v[148:151], a[8:11]\n"
    "s_add_u32 s92, s62, 0\n"
    "v_lshlrev_b32_e32 v250, 4, v227\n"
    "v_lshl_add_u32 v250, v229, 8, v250\n"
    "v_mfma_f32_16x16x32_bf16 a[12:15], v[32:35], v[136:139], a[12:15]\n"
    "buffer_load_dwordx4 v255, s[48:51], s92 offen sc1 lds\n"
    "v_lshl_add_u32 v244, v228, 10, v250\n"
    "v_mfma_f32_16x16x32_bf16 a[12:15], v[164:167], v[140:143], a[12:15]\n"
    "ds_read_b128 v[200:203], v252 offset:9216\n"
    "v_lshl_add_u32 v251, v228, 30, v250\n"
    "v_mfma_f32_16x16x32_bf16 a[12:15], v[184:187], v[144:147], a[12:15]\n"
    "v_mul_u32_u24_e32 v247, 0x1400, v228\n"
    "v_add_u32_e64 v247, v247, v250\n"
    "v_mfma_f32_16x16x32_bf16 a[12:15], a[140:143], v[148:151], a[12:15]\n"
    "buffer_load_dwordx4 v255, s[48:51], s92 offen offset:1024 sc1 lds\n"
    "v_lshrrev_b32_e64 v247, 1, v247\n"
    "v_mfma_f32_16x16x32_bf16 a[16:19], v[36:39], v[136:139], a[16:19]\n"
    "ds_read_b128 v[204:207], v253 offset:33792\n"
    "v_lshl_add_u32 v250, v228, 11, v250\n"
    "v_mfma_f32_16x16x32_bf16 a[16:19], v[168:171], v[140:143], a[16:19]\n"
    "s_add_u32 m0, s68, 0x6000\n"
    "ds_read_b128 v[208:211], v253 offset:41984\n"
    "v_lshlrev_b32_e64 v227, 5, v227\n"
    "v_mfma_f32_16x16x32_bf16 a[16:19], v[188:191], v[144:147], a[16:19]\n"
    "s_waitcnt lgkmcnt(7)\n"
    "s_lshl_b32 s38, s89, 4\n"
    "v_mfma_f32_16x16x32_bf16 a[16:19], a[144:147], v[148:151], a[16:19]\n"
    "s_add_u32 s92, s62, 0x2000\n"
    "s_add_u32 s38, s38, s90\n"
    "buffer_load_dwordx4 v255, s[48:51], s92 offen sc1 lds\n"
    "v_mfma_f32_16x16x32_bf16 a[0:3], a[148:151], v[152:155], a[0:3]\n"
    "s_waitcnt lgkmcnt(6)\n"
    "s_mul_i32 s70, s38, 0x2800\n"
    "ds_read_b128 v[212:215], v252 offset:33792\n"
    "v_mfma_f32_16x16x32_bf16 a[0:3], a[168:171], v[156:159], a[0:3]\n"
    "s_waitcnt lgkmcnt(5)\n"
    "s_mul_i32 s71, s36, 0x140\n"
    "v_mfma_f32_16x16x32_bf16 a[0:3], a[188:191], v[160:163], a[0:3]\n"
    "s_add_u32 s70, s70, s71\n"
    "s_lshl_b32 s71, s71, 1\n"
    "buffer_load_dwordx4 v255, s[48:51], s92 offen offset:1024 sc1 lds\n"
    "v_mfma_f32_16x16x32_bf16 a[0:3], v[96:99], v[192:195], a[0:3]\n"
    "s_add_u32 s71, s71, 0x20000\n"
    "ds_read_b128 a[60:63], v252 offset:41984\n"
    "v_add_u32_e32 v246, s71, v246\n"
    "v_mfma_f32_16x16x32_bf16 a[4:7], a[152:155], v[152:155], a[4:7]\n"
    "s_add_u32 m0, s68, 0x22000\n"
    "v_add_u32_e64 v249, s71, v249\n"
    "v_mfma_f32_16x16x32_bf16 a[4:7], a[172:175], v[156:159], a[4:7]\n"
    "s_add_u32 s92, s62, 0x4000\n"
    "ds_read_b128 a[64:67], v253 offset:50176\n"
    "buffer_load_dwordx4 v255, s[48:51], s92 offen sc1 lds\n"
    "v_mfma_f32_16x16x32_bf16 a[4:7], v[80:83], v[160:163], a[4:7]\n"
    "v_mfma_f32_16x16x32_bf16 a[4:7], v[100:103], v[192:195], a[4:7]\n"
    "ds_read_b128 a[68:71], v253 offset:58368\n"
    "v_mfma_f32_16x16x32_bf16 a[8:11], a[156:159], v[152:155], a[8:11]\n"
    "v_mfma_f32_16x16x32_bf16 a[8:11], a[176:179], v[156:159], a[8:11]\n"
    "buffer_load_dwordx4 v255, s[48:51], s92 offen offset:1024 sc1 lds\n"
    "v_mfma_f32_16x16x32_bf16 a[8:11], v[84:87], v[160:163], a[8:11]\n"
    "v_mfma_f32_16x16x32_bf16 a[8:11], v[104:107], v[192:195], a[8:11]\n"
    "v_mfma_f32_16x16x32_bf16 a[12:15], a[160:163], v[152:155], a[12:15]\n"
    "buffer_load_dwordx4 a[248:251], v255, s[48:51], s95 offen offset:2048 sc1\n"
    "v_mfma_f32_16x16x32_bf16 a[12:15], a[180:183], v[156:159], a[12:15]\n"
    "v_mfma_f32_16x16x32_bf16 a[12:15], v[88:91], v[160:163], a[12:15]\n"
    "v_mfma_f32_16x16x32_bf16 a[12:15], v[108:111], v[192:195], a[12:15]\n"
    "v_mfma_f32_16x16x32_bf16 a[16:19], a[164:167], v[152:155], a[16:19]\n"
    "buffer_load_dwordx4 a[252:255], v255, s[48:51], s95 offen offset:3072 sc1\n"
    "v_mfma_f32_16x16x32_bf16 a[16:19], a[184:187], v[156:159], a[16:19]\n"
    "v_mfma_f32_16x16x32_bf16 a[16:19], v[92:95], v[160:163], a[16:19]\n"
    "v_mfma_f32_16x16x32_bf16 a[16:19], v[112:115], v[192:195], a[16:19]\n"
    "s_waitcnt lgkmcnt(7)\n"
    "v_mfma_f32_16x16x32_bf16 a[20:23], v[20:23], v[196:199], a[20:23]\n"
    "s_waitcnt lgkmcnt(6)\n"
    "v_mfma_f32_16x16x32_bf16 a[20:23], v[40:43], v[200:203], a[20:23]\n"
    "s_waitcnt lgkmcnt(5)\n"
    "v_accvgpr_read_b32 v232, a0\n"
    "v_accvgpr_read_b32 v233, a1\n"
    "v_mfma_f32_16x16x32_bf16 a[20:23], v[172:175], v[204:207], a[20:23]\n"
    "s_waitcnt lgkmcnt(4)\n"
    "v_cvt_pk_bf16_f32 v216, v232, v233\n"
    "v_accvgpr_read_b32 v234, a2\n"
    "v_mfma_f32_16x16x32_bf16 a[20:23], a[128:131], v[208:211], a[20:23]\n"
    "v_accvgpr_read_b32 v235, a3\n"
    "v_cvt_pk_bf16_f32 v217, v234, v235\n"
    "v_mfma_f32_16x16x32_bf16 a[24:27], v[24:27], v[196:199], a[24:27]\n"
    "v_accvgpr_read_b32 v236, a4\n"
    "v_accvgpr_read_b32 v237, a5\n"
    "v_mfma_f32_16x16x32_bf16 a[24:27], v[44:47], v[200:203], a[24:27]\n"
    "v_cvt_pk_bf16_f32 v218, v236, v237\n"
    "v_accvgpr_read_b32 v238, a6\n"
    "v_mfma_f32_16x16x32_bf16 a[24:27], v[176:179], v[204:207], a[24:27]\n"
    "v_accvgpr_read_b32 v239, a7\n"
    "v_cvt_pk_bf16_f32 v219, v238, v239\n"
    "v_mfma_f32_16x16x32_bf16 a[24:27], a[132:135], v[208:211], a[24:27]\n"
    "buffer_store_dwordx4 v[216:219], v255, s[48:51], s64 offen sc1\n"
    "v_accvgpr_read_b32 v232, a8\n"
    "v_mfma_f32_16x16x32_bf16 a[28:31], v[28:31], v[196:199], a[28:31]\n"
    "v_accvgpr_read_b32 v233, a9\n"
    "v_cvt_pk_bf16_f32 v220, v232, v233\n"
    "v_mfma_f32_16x16x32_bf16 a[28:31], v[48:51], v[200:203], a[28:31]\n"
    "v_accvgpr_read_b32 v234, a10\n"
    "v_accvgpr_read_b32 v235, a11\n"
    "v_mfma_f32_16x16x32_bf16 a[28:31], v[180:183], v[204:207], a[28:31]\n"
    "ds_read_b128 v[136:139], v253 offset:26624\n"
    "v_cvt_pk_bf16_f32 v221, v234, v235\n"
    "v_mfma_f32_16x16x32_bf16 a[28:31], a[136:139], v[208:211], a[28:31]\n"
    "v_accvgpr_read_b32 v236, a12\n"
    "v_accvgpr_read_b32 v237, a13\n"
    "v_mfma_f32_16x16x32_bf16 a[32:35], v[32:35], v[196:199], a[32:35]\n"
    "v_cvt_pk_bf16_f32 v222, v236, v237\n"
    "v_accvgpr_read_b32 v238, a14\n"
    "v_mfma_f32_16x16x32_bf16 a[32:35], v[164:167], v[200:203], a[32:35]\n"
    "ds_read_b128 v[140:143], v252 offset:10240\n"
    "v_accvgpr_read_b32 v239, a15\n"
    "v_mfma_f32_16x16x32_bf16 a[32:35], v[184:187], v[204:207], a[32:35]\n"
    "v_cvt_pk_bf16_f32 v223, v238, v239\n"
    "v_mfma_f32_16x16x32_bf16 a[32:35], a[140:143], v[208:211], a[32:35]\n"
    "buffer_store_dwordx4 v[220:223], v255, s[48:51], s64 offen offset:1024 sc1\n"
    "v_accvgpr_read_b32 v232, a16\n"
    "v_mfma_f32_16x16x32_bf16 a[36:39], v[36:39], v[196:199], a[36:39]\n"
    "ds_read_b128 v[144:147], v253 offset:34816\n"
    "v_accvgpr_read_b32 v233, a17\n"
    "v_mfma_f32_16x16x32_bf16 a[36:39], v[168:171], v[200:203], a[36:39]\n"
    "v_cvt_pk_bf16_f32 v224, v232, v233\n"
    "v_accvgpr_read_b32 v234, a18\n"
    "v_mfma_f32_16x16x32_bf16 a[36:39], v[188:191], v[204:207], a[36:39]\n"
    "v_accvgpr_read_b32 v235, a19\n"
    "v_cvt_pk_bf16_f32 v225, v234, v235\n"
    "v_mfma_f32_16x16x32_bf16 a[36:39], a[144:147], v[208:211], a[36:39]\n"
    "s_waitcnt lgkmcnt(6)\n"
    "buffer_store_dwordx2 v[224:225], v242, s[48:51], s64 offen offset:2048 sc1\n"
    "ds_read_b128 v[148:151], v253 offset:43008\n"
    "v_mfma_f32_16x16x32_bf16 a[20:23], a[148:151], v[212:215], a[20:23]\n"
    "s_waitcnt lgkmcnt(6)\n"
    "s_add_u32 s64, s64, 0x5000\n"
    "ds_read_b128 v[152:155], v252 offset:34816\n"
    "v_mfma_f32_16x16x32_bf16 a[20:23], a[168:171], a[60:63], a[20:23]\n"
    "s_waitcnt lgkmcnt(6)\n"
    "v_mfma_f32_16x16x32_bf16 a[20:23], a[188:191], a[64:67], a[20:23]\n"
    "s_waitcnt lgkmcnt(5)\n"
    "ds_read_b128 v[156:159], v252 offset:43008\n"
    "v_mfma_f32_16x16x32_bf16 a[20:23], v[96:99], a[68:71], a[20:23]\n"
    "v_mfma_f32_16x16x32_bf16 a[24:27], a[152:155], v[212:215], a[24:27]\n"
    "v_mfma_f32_16x16x32_bf16 a[24:27], a[172:175], a[60:63], a[24:27]\n"
    "ds_read_b128 v[160:163], v253 offset:51200\n"
    "v_mfma_f32_16x16x32_bf16 a[24:27], v[80:83], a[64:67], a[24:27]\n"
    "v_mfma_f32_16x16x32_bf16 a[24:27], v[100:103], a[68:71], a[24:27]\n"
    "ds_read_b128 v[192:195], v253 offset:59392\n"
    "v_mfma_f32_16x16x32_bf16 a[28:31], a[156:159], v[212:215], a[28:31]\n"
    "v_mfma_f32_16x16x32_bf16 a[28:31], a[176:179], a[60:63], a[28:31]\n"
    "v_mfma_f32_16x16x32_bf16 a[28:31], v[84:87], a[64:67], a[28:31]\n"
    "v_mfma_f32_16x16x32_bf16 a[28:31], v[104:107], a[68:71], a[28:31]\n"
    "v_mfma_f32_16x16x32_bf16 a[32:35], a[160:163], v[212:215], a[32:35]\n"
    "v_mfma_f32_16x16x32_bf16 a[32:35], a[180:183], a[60:63], a[32:35]\n"
    "v_mfma_f32_16x16x32_bf16 a[32:35], v[88:91], a[64:67], a[32:35]\n"
    "v_mfma_f32_16x16x32_bf16 a[32:35], v[108:111], a[68:71], a[32:35]\n"
    "v_mfma_f32_16x16x32_bf16 a[36:39], a[164:167], v[212:215], a[36:39]\n"
    "v_mfma_f32_16x16x32_bf16 a[36:39], a[184:187], a[60:63], a[36:39]\n"
    "v_mfma_f32_16x16x32_bf16 a[36:39], v[92:95], a[64:67], a[36:39]\n"
    "v_mfma_f32_16x16x32_bf16 a[36:39], v[112:115], a[68:71], a[36:39]\n"
    "s_waitcnt lgkmcnt(7)\n"
    "v_mfma_f32_16x16x32_bf16 a[40:43], v[20:23], v[136:139], a[40:43]\n"
    "s_waitcnt lgkmcnt(6)\n"
    "v_mfma_f32_16x16x32_bf16 a[40:43], v[40:43], v[140:143], a[40:43]\n"
    "s_waitcnt lgkmcnt(5)\n"
    "v_accvgpr_read_b32 v232, a20\n"
    "v_accvgpr_read_b32 v233, a21\n"
    "v_mfma_f32_16x16x32_bf16 a[40:43], v[172:175], v[144:147], a[40:43]\n"
    "s_waitcnt lgkmcnt(4)\n"
    "v_cvt_pk_bf16_f32 v216, v232, v233\n"
    "v_accvgpr_read_b32 v234, a22\n"
    "v_mfma_f32_16x16x32_bf16 a[40:43], a[128:131], v[148:151], a[40:43]\n"
    "v_accvgpr_read_b32 v235, a23\n"
    "v_cvt_pk_bf16_f32 v217, v234, v235\n"
    "v_mfma_f32_16x16x32_bf16 a[44:47], v[24:27], v[136:139], a[44:47]\n"
    "v_accvgpr_read_b32 v236, a24\n"
    "v_accvgpr_read_b32 v237, a25\n"
    "v_mfma_f32_16x16x32_bf16 a[44:47], v[44:47], v[140:143], a[44:47]\n"
    "v_cvt_pk_bf16_f32 v218, v236, v237\n"
    "v_accvgpr_read_b32 v238, a26\n"
    "v_mfma_f32_16x16x32_bf16 a[44:47], v[176:179], v[144:147], a[44:47]\n"
    "v_accvgpr_read_b32 v239, a27\n"
    "v_cvt_pk_bf16_f32 v219, v238, v239\n"
    "v_mfma_f32_16x16x32_bf16 a[44:47], a[132:135], v[148:151], a[44:47]\n"
    "buffer_store_dwordx4 v[216:219], v255, s[48:51], s64 offen sc1\n"
    "v_accvgpr_read_b32 v232, a28\n"
    "v_mfma_f32_16x16x32_bf16 a[48:51], v[28:31], v[136:139], a[48:51]\n"
    "v_accvgpr_read_b32 v233, a29\n"
    "v_cvt_pk_bf16_f32 v220, v232, v233\n"
    "v_mfma_f32_16x16x32_bf16 a[48:51], v[48:51], v[140:143], a[48:51]\n"
    "v_accvgpr_read_b32 v234, a30\n"
    "v_accvgpr_read_b32 v235, a31\n"
    "v_mfma_f32_16x16x32_bf16 a[48:51], v[180:183], v[144:147], a[48:51]\n"
    "ds_read_b128 v[196:199], v253 offset:27648\n"
    "v_cvt_pk_bf16_f32 v221, v234, v235\n"
    "v_mfma_f32_16x16x32_bf16 a[48:51], a[136:139], v[148:151], a[48:51]\n"
    "v_accvgpr_read_b32 v236, a32\n"
    "v_accvgpr_read_b32 v237, a33\n"
    "v_mfma_f32_16x16x32_bf16 a[52:55], v[32:35], v[136:139], a[52:55]\n"
    "v_cvt_pk_bf16_f32 v222, v236, v237\n"
    "v_accvgpr_read_b32 v238, a34\n"
    "v_mfma_f32_16x16x32_bf16 a[52:55], v[164:167], v[140:143], a[52:55]\n"
    "ds_read_b128 v[200:203], v252 offset:11264\n"
    "v_accvgpr_read_b32 v239, a35\n"
    "v_mfma_f32_16x16x32_bf16 a[52:55], v[184:187], v[144:147], a[52:55]\n"
    "v_cvt_pk_bf16_f32 v223, v238, v239\n"
    "v_mfma_f32_16x16x32_bf16 a[52:55], a[140:143], v[148:151], a[52:55]\n"
    "buffer_store_dwordx4 v[220:223], v255, s[48:51], s64 offen offset:1024 sc1\n"
    "v_accvgpr_read_b32 v232, a36\n"
    "v_mfma_f32_16x16x32_bf16 a[56:59], v[36:39], v[136:139], a[56:59]\n"
    "ds_read_b128 v[204:207], v253 offset:35840\n"
    "v_accvgpr_read_b32 v233, a37\n"
    "v_mfma_f32_16x16x32_bf16 a[56:59], v[168:171], v[140:143], a[56:59]\n"
    "v_cvt_pk_bf16_f32 v224, v232, v233\n"
    "v_accvgpr_read_b32 v234, a38\n"
    "v_mfma_f32_16x16x32_bf16 a[56:59], v[188:191], v[144:147], a[56:59]\n"
    "v_accvgpr_read_b32 v235, a39\n"
    "v_cvt_pk_bf16_f32 v225, v234, v235\n"
    "v_mfma_f32_16x16x32_bf16 a[56:59], a[144:147], v[148:151], a[56:59]\n"
    "s_waitcnt lgkmcnt(6)\n"
    "buffer_store_dwordx2 v[224:225], v242, s[48:51], s64 offen offset:2048 sc1\n"
    "ds_read_b128 v[208:211], v253 offset:44032\n"
    "v_mfma_f32_16x16x32_bf16 a[40:43], a[148:151], v[152:155], a[40:43]\n"
    "s_waitcnt lgkmcnt(6)\n"
    "s_add_u32 s64, s64, 0x5000\n"
    "ds_read_b128 v[212:215], v252 offset:35840\n"
    "v_mfma_f32_16x16x32_bf16 a[40:43], a[168:171], v[156:159], a[40:43]\n"
    "s_waitcnt lgkmcnt(6)\n"
    "v_mfma_f32_16x16x32_bf16 a[40:43], a[188:191], v[160:163], a[40:43]\n"
    "s_waitcnt lgkmcnt(5)\n"
    "ds_read_b128 a[60:63], v252 offset:44032\n"
    "v_mfma_f32_16x16x32_bf16 a[40:43], v[96:99], v[192:195], a[40:43]\n"
    "v_mfma_f32_16x16x32_bf16 a[44:47], a[152:155], v[152:155], a[44:47]\n"
    "v_mfma_f32_16x16x32_bf16 a[44:47], a[172:175], v[156:159], a[44:47]\n"
    "ds_read_b128 a[64:67], v253 offset:52224\n"
    "v_mfma_f32_16x16x32_bf16 a[44:47], v[80:83], v[160:163], a[44:47]\n"
    "v_mfma_f32_16x16x32_bf16 a[44:47], v[100:103], v[192:195], a[44:47]\n"
    "ds_read_b128 a[68:71], v253 offset:60416\n"
    "v_mfma_f32_16x16x32_bf16 a[48:51], a[156:159], v[152:155], a[48:51]\n"
    "v_mfma_f32_16x16x32_bf16 a[48:51], a[176:179], v[156:159], a[48:51]\n"
    "v_mfma_f32_16x16x32_bf16 a[48:51], v[84:87], v[160:163], a[48:51]\n"
    "v_mfma_f32_16x16x32_bf16 a[48:51], v[104:107], v[192:195], a[48:51]\n"
    "v_mfma_f32_16x16x32_bf16 a[52:55], a[160:163], v[152:155], a[52:55]\n"
    "v_mfma_f32_16x16x32_bf16 a[52:55], a[180:183], v[156:159], a[52:55]\n"
    "v_mfma_f32_16x16x32_bf16 a[52:55], v[88:91], v[160:163], a[52:55]\n"
    "v_mfma_f32_16x16x32_bf16 a[52:55], v[108:111], v[192:195], a[52:55]\n"
    "v_mfma_f32_16x16x32_bf16 a[56:59], a[164:167], v[152:155], a[56:59]\n"
    "v_mfma_f32_16x16x32_bf16 a[56:59], a[184:187], v[156:159], a[56:59]\n"
    "v_mfma_f32_16x16x32_bf16 a[56:59], v[92:95], v[160:163], a[56:59]\n"
    "v_mfma_f32_16x16x32_bf16 a[56:59], v[112:115], v[192:195], a[56:59]\n"
    "s_waitcnt lgkmcnt(7)\n"
    "v_mfma_f32_16x16x32_bf16 v[0:3], v[20:23], v[196:199], v[0:3]\n"
    "s_waitcnt lgkmcnt(6)\n"
    "v_mfma_f32_16x16x32_bf16 v[0:3], v[40:43], v[200:203], v[0:3]\n"
    "s_waitcnt lgkmcnt(5)\n"
    "v_accvgpr_read_b32 v232, a40\n"
    "v_accvgpr_read_b32 v233, a41\n"
    "v_mfma_f32_16x16x32_bf16 v[0:3], v[172:175], v[204:207], v[0:3]\n"
    "s_waitcnt lgkmcnt(4)\n"
    "v_cvt_pk_bf16_f32 v216, v232, v233\n"
    "v_accvgpr_read_b32 v234, a42\n"
    "v_mfma_f32_16x16x32_bf16 v[0:3], a[128:131], v[208:211], v[0:3]\n"
    "v_accvgpr_read_b32 v235, a43\n"
    "v_cvt_pk_bf16_f32 v217, v234, v235\n"
    "v_mfma_f32_16x16x32_bf16 v[4:7], v[24:27], v[196:199], v[4:7]\n"
    "v_accvgpr_read_b32 v236, a44\n"
    "v_accvgpr_read_b32 v237, a45\n"
    "v_mfma_f32_16x16x32_bf16 v[4:7], v[44:47], v[200:203], v[4:7]\n"
    "v_cvt_pk_bf16_f32 v218, v236, v237\n"
    "v_accvgpr_read_b32 v238, a46\n"
    "v_mfma_f32_16x16x32_bf16 v[4:7], v[176:179], v[204:207], v[4:7]\n"
    "v_accvgpr_read_b32 v239, a47\n"
    "v_cvt_pk_bf16_f32 v219, v238, v239\n"
    "v_mfma_f32_16x16x32_bf16 v[4:7], a[132:135], v[208:211], v[4:7]\n"
    "buffer_store_dwordx4 v[216:219], v255, s[48:51], s64 offen sc1\n"
    "v_accvgpr_read_b32 v232, a48\n"
    "v_mfma_f32_16x16x32_bf16 v[8:11], v[28:31], v[196:199], v[8:11]\n"
    "v_accvgpr_read_b32 v233, a49\n"
    "v_cvt_pk_bf16_f32 v220, v232, v233\n"
    "v_mfma_f32_16x16x32_bf16 v[8:11], v[48:51], v[200:203], v[8:11]\n"
    "v_accvgpr_read_b32 v234, a50\n"
    "v_accvgpr_read_b32 v235, a51\n"
    "v_mfma_f32_16x16x32_bf16 v[8:11], v[180:183], v[204:207], v[8:11]\n"
    "v_cvt_pk_bf16_f32 v221, v234, v235\n"
    "v_accvgpr_read_b32 v236, a52\n"
    "v_mfma_f32_16x16x32_bf16 v[8:11], a[136:139], v[208:211], v[8:11]\n"
    "v_accvgpr_read_b32 v237, a53\n"
    "v_cvt_pk_bf16_f32 v222, v236, v237\n"
    "v_mfma_f32_16x16x32_bf16 v[12:15], v[32:35], v[196:199], v[12:15]\n"
    "v_accvgpr_read_b32 v238, a54\n"
    "v_accvgpr_read_b32 v239, a55\n"
    "v_mfma_f32_16x16x32_bf16 v[12:15], v[164:167], v[200:203], v[12:15]\n"
    "v_cvt_pk_bf16_f32 v223, v238, v239\n"
    "v_mfma_f32_16x16x32_bf16 v[12:15], v[184:187], v[204:207], v[12:15]\n"
    "buffer_store_dwordx4 v[220:223], v255, s[48:51], s64 offen offset:1024 sc1\n"
    "v_accvgpr_read_b32 v232, a56\n"
    "v_mfma_f32_16x16x32_bf16 v[12:15], a[140:143], v[208:211], v[12:15]\n"
    "v_accvgpr_read_b32 v233, a57\n"
    "v_cvt_pk_bf16_f32 v224, v232, v233\n"
    "v_mfma_f32_16x16x32_bf16 v[16:19], v[36:39], v[196:199], v[16:19]\n"
    "v_accvgpr_read_b32 v234, a58\n"
    "v_accvgpr_read_b32 v235, a59\n"
    "v_mfma_f32_16x16x32_bf16 v[16:19], v[168:171], v[200:203], v[16:19]\n"
    "v_cvt_pk_bf16_f32 v225, v234, v235\n"
    "v_mfma_f32_16x16x32_bf16 v[16:19], v[188:191], v[204:207], v[16:19]\n"
    "buffer_store_dwordx2 v[224:225], v242, s[48:51], s64 offen offset:2048 sc1\n"
    "v_mfma_f32_16x16x32_bf16 v[16:19], a[144:147], v[208:211], v[16:19]\n"
    "s_waitcnt lgkmcnt(3)\n"
    "s_add_u32 s64, s64, 0x5000\n"
    "v_mfma_f32_16x16x32_bf16 v[0:3], a[148:151], v[212:215], v[0:3]\n"
    "s_waitcnt lgkmcnt(2)\n"
    "v_mfma_f32_16x16x32_bf16 v[0:3], a[168:171], a[60:63], v[0:3]\n"
    "s_waitcnt lgkmcnt(1)\n"
    "v_mfma_f32_16x16x32_bf16 v[0:3], a[188:191], a[64:67], v[0:3]\n"
    "s_waitcnt lgkmcnt(0)\n"
    "v_mfma_f32_16x16x32_bf16 v[0:3], v[96:99], a[68:71], v[0:3]\n"
    "v_mfma_f32_16x16x32_bf16 v[4:7], a[152:155], v[212:215], v[4:7]\n"
    "v_mfma_f32_16x16x32_bf16 v[4:7], a[172:175], a[60:63], v[4:7]\n"
    "v_mfma_f32_16x16x32_bf16 v[4:7], v[80:83], a[64:67], v[4:7]\n"
    "v_mfma_f32_16x16x32_bf16 v[4:7], v[100:103], a[68:71], v[4:7]\n"
    "v_mfma_f32_16x16x32_bf16 v[8:11], a[156:159], v[212:215], v[8:11]\n"
    "v_mfma_f32_16x16x32_bf16 v[8:11], a[176:179], a[60:63], v[8:11]\n"
    "v_mfma_f32_16x16x32_bf16 v[8:11], v[84:87], a[64:67], v[8:11]\n"
    "v_mfma_f32_16x16x32_bf16 v[8:11], v[104:107], a[68:71], v[8:11]\n"
    "v_mfma_f32_16x16x32_bf16 v[12:15], a[160:163], v[212:215], v[12:15]\n"
    "v_mfma_f32_16x16x32_bf16 v[12:15], a[180:183], a[60:63], v[12:15]\n"
    "v_mfma_f32_16x16x32_bf16 v[12:15], v[88:91], a[64:67], v[12:15]\n"
    "v_mfma_f32_16x16x32_bf16 v[12:15], v[108:111], a[68:71], v[12:15]\n"
    "v_mfma_f32_16x16x32_bf16 v[16:19], a[164:167], v[212:215], v[16:19]\n"
    "v_mfma_f32_16x16x32_bf16 v[16:19], a[184:187], a[60:63], v[16:19]\n"
    "v_mfma_f32_16x16x32_bf16 v[16:19], v[92:95], a[64:67], v[16:19]\n"
    "v_mfma_f32_16x16x32_bf16 v[16:19], v[112:115], a[68:71], v[16:19]\n"
    "v_cvt_pk_bf16_f32 v216, v0, v1\n"
    "v_cvt_pk_bf16_f32 v217, v2, v3\n"
    "v_cvt_pk_bf16_f32 v218, v4, v5\n"
    "v_cvt_pk_bf16_f32 v219, v6, v7\n"
    "buffer_store_dwordx4 v[216:219], v255, s[48:51], s64 offen sc1\n"
    "v_cvt_pk_bf16_f32 v220, v8, v9\n"
    "v_cvt_pk_bf16_f32 v221, v10, v11\n"
    "v_cvt_pk_bf16_f32 v222, v12, v13\n"
    "v_cvt_pk_bf16_f32 v223, v14, v15\n"
    "buffer_store_dwordx4 v[220:223], v255, s[48:51], s64 offen offset:1024 sc1\n"
    "v_cvt_pk_bf16_f32 v224, v16, v17\n"
    "v_cvt_pk_bf16_f32 v225, v18, v19\n"
    "buffer_store_dwordx2 v[224:225], v242, s[48:51], s64 offen offset:2048 sc1\n"
    "s_add_u32 s64, s64, 0x5000\n"
    "v_add_u32_e32 v1, 0x21f00, v227\n"
    "s_lshl_b32 s39, s36, 3\n"
    "v_add_u32_e32 v2, s39, v1\n"
    "s_lshr_b32 s39, s38, 1\n"
    "s_mul_i32 s39, s39, 0x5000\n"
    "s_and_b32 s60, s38, 1\n"
    "s_lshl_b32 s67, s60, 6\n"
    "s_mul_i32 s91, s36, 0x1400\n"
    "s_add_u32 s66, s39, s91\n"
    "s_add_u32 s66, s66, s67\n"
    "s_add_u32 s65, s66, s67\n"
    "s_add_u32 s66, s66, 0x800\n"
    "s_add_u32 s91, s65, 0x2f00000\n"
    "s_add_u32 s67, s66, 0x2f00000\n"
    "s_add_u32 s65, s65, 0x1900000\n"
    "s_add_u32 s66, s66, 0x1900000\n"
    "buffer_load_dwordx2 v[116:117], v245, s[52:55], s70 offen\n"
    "buffer_load_dwordx2 v[118:119], v245, s[52:55], s70 offen offset:32\n"
    "buffer_load_dwordx2 v[120:121], v245, s[52:55], s70 offen offset:160\n"
    "buffer_load_dwordx2 v[122:123], v245, s[52:55], s70 offen offset:192\n"
    "buffer_load_dwordx2 v[124:125], v248, s[52:55], s70 offen offset:128\n"
    "s_add_u32 s95, s94, 0xc000\n"
    "buffer_load_dwordx4 a[72:75], v255, s[48:51], s95 offen sc1\n"
    "buffer_load_dwordx4 a[76:79], v255, s[48:51], s95 offen offset:1024 sc1\n"
    "buffer_load_dwordx4 a[80:83], v255, s[48:51], s95 offen offset:2048 sc1\n"
    "v_add_u32_e32 v231, 1, v230\n"
    "s_waitcnt vmcnt(8) lgkmcnt(0)\n"
    "s_barrier\n"
    "buffer_store_dword v231, v241, s[48:51], s93 offen sc1\n"
    "buffer_load_dwordx4 a[84:87], v255, s[48:51], s95 offen offset:3072 sc1\n"
    "s_add_u32 s95, s94, 0xd000\n"
    "buffer_load_dwordx4 a[88:91], v255, s[48:51], s95 offen sc1\n"
    "buffer_load_dwordx4 a[92:95], v255, s[48:51], s95 offen offset:1024 sc1\n"
    "buffer_load_dwordx4 a[96:99], v255, s[48:51], s95 offen offset:2048 sc1\n"
    "buffer_load_dwordx4 a[100:103], v255, s[48:51], s95 offen offset:3072 sc1\n"
    "s_add_u32 s95, s94, 0xe000\n"
    "buffer_load_dwordx4 a[104:107], v255, s[48:51], s95 offen sc1\n"
    "buffer_load_dwordx4 a[108:111], v255, s[48:51], s95 offen offset:1024 sc1\n"
    "buffer_load_dwordx4 a[112:115], v255, s[48:51], s95 offen offset:2048 sc1\n"
    "buffer_load_dwordx4 a[116:119], v255, s[48:51], s95 offen offset:3072 sc1\n"
    "s_add_u32 s95, s94, 0xf000\n"
    "buffer_load_dwordx4 a[120:123], v255, s[48:51], s95 offen sc1\n"
    "buffer_load_dwordx4 a[124:127], v255, s[48:51], s95 offen offset:1024 sc1\n"
    "buffer_load_dwordx4 a[128:131], v255, s[48:51], s95 offen offset:2048 sc1\n"
    "v_bfe_u32 v240, v255, 4, 4\n"
    "v_lshlrev_b32_e32 v240, 2, v240\n"
    "buffer_load_dword v229, v240, s[48:51], s63 offen sc1\n"
    "buffer_load_dwordx4 a[132:135], v255, s[48:51], s95 offen offset:3072 sc1\n"
    "ds_read_b128 v[80:83], v246 offset:0\n"
    "ds_read_b128 v[84:87], v246 offset:64\n"
    "ds_read_b128 v[88:91], v246 offset:320\n"
    "ds_read_b128 v[92:95], v246 offset:384\n"
    "ds_read_b128 v[96:99], v249 offset:256\n"
    "ds_read_b128 v[24:27], v246 offset:5120\n"
    "ds_read_b128 v[28:31], v246 offset:5184\n"
    "ds_read_b128 v[40:43], v246 offset:5440\n"
    "ds_read_b128 v[44:47], v246 offset:5504\n"
    "ds_read_b128 v[56:59], v249 offset:5376\n"
    "ds_read_b128 v[32:35], v246 offset:2560\n"
    "ds_read_b128 v[36:39], v246 offset:2624\n"
    "ds_read_b128 v[48:51], v246 offset:2880\n"
    "ds_read_b128 v[52:55], v246 offset:2944\n"
    "ds_read_b128 v[60:63], v249 offset:2816\n"
    "s_lshr_b32 s39, s38, 2\n"
    "s_lshl_b32 s93, s39, 10\n"
    "s_and_b32 s92, s38, 3\n"
    "s_lshl_b32 s92, s92, 2\n"
    "s_add_u32 s93, s93, s92\n"
    "s_add_u32 s93, s93, 0x50000\n"
    "s_mul_i32 s39, s39, 0xa000\n"
    "s_and_b32 s38, s38, 2\n"
    "s_lshl_b32 s38, s38, 9\n"
    "s_add_u32 s39, s39, s38\n"
    "s_lshl_b32 s60, s60, 7\n"
    "s_add_u32 s39, s39, s60\n"
    "s_add_u32 s39, s39, 0x1c00000\n"
    "s_mul_i32 s60, s36, 0x2800\n"
    "s_add_u32 s38, s39, s60\n"
    "s_add_u32 s39, s38, 0x1000\n"
    "s_add_u32 s60, s38, 0x2000\n"
    "v_mov_b32_e32 v8, 0x3f80\n"
    "v_mov_b32_e32 v9, 0x3f800000\n"
    "s_waitcnt vmcnt(1)\n"
    "v_sub_u32_e32 v229, v229, v230\n"
    "v_subrev_u32_e32 v229, 1, v229\n"
    "v_cmp_gt_u32_e32 vcc, 3, v229\n"
    "s_nop 1\n"
    "s_cmp_eq_u64 vcc, -1\n"
    "s_cbranch_scc1 POK6_%=\n"
    "s_mov_b32 s61, 20000\n"
    "PSP6_%=:\n"
    "s_sleep 4\n"
    "buffer_load_dword v229, v240, s[48:51], s63 offen sc1\n"
    "s_waitcnt vmcnt(0)\n"
    "v_sub_u32_e32 v229, v229, v230\n"
    "v_subrev_u32_e32 v229, 1, v229\n"
    "v_cmp_gt_u32_e32 vcc, 3, v229\n"
    "s_nop 1\n"
    "s_cmp_eq_u64 vcc, -1\n"
    "s_cbranch_scc1 POK6_%=\n"
    "s_sub_u32 s61, s61, 1\n"
    "s_cmp_lg_u32 s61, 0\n"
    "s_cbranch_scc1 PSP6_%=\n"
    "POK6_%=:\n"
    "buffer_load_dwordx4 v[128:131], v244, s[48:51], s91 offen sc1\n"
    "s_add_u32 s92, s91, 0x280000\n"
    "buffer_load_dwordx4 v[132:135], v244, s[48:51], s92 offen sc1\n"
    "s_add_u32 s92, s91, 0x500000\n"
    "buffer_load_dwordx4 v[136:139], v244, s[48:51], s92 offen sc1\n"
    "s_add_u32 s92, s91, 0x780000\n"
    "buffer_load_dwordx4 v[140:143], v244, s[48:51], s92 offen sc1\n"
    "buffer_load_dwordx4 v[144:147], v244, s[48:51], s91 offen offset:2560 sc1\n"
    "s_add_u32 s92, s91, 0x280000\n"
    "buffer_load_dwordx4 v[148:151], v244, s[48:51], s92 offen offset:2560 sc1\n"
    "s_add_u32 s92, s91, 0x500000\n"
    "buffer_load_dwordx4 v[152:155], v244, s[48:51], s92 offen offset:2560 sc1\n"
    "s_add_u32 s92, s91, 0x780000\n"
    "buffer_load_dwordx4 v[156:159], v244, s[48:51], s92 offen offset:2560 sc1\n"
    "buffer_load_dwordx2 v[160:161], v247, s[48:51], s67 offen sc1\n"
    "s_add_u32 s92, s67, 0x280000\n"
    "buffer_load_dwordx2 v[162:163], v247, s[48:51], s92 offen sc1\n"
    "s_add_u32 s92, s67, 0x500000\n"
    "buffer_load_dwordx2 v[164:165], v247, s[48:51], s92 offen sc1\n"
    "s_add_u32 s92, s67, 0x780000\n"
    "buffer_load_dwordx2 v[166:167], v247, s[48:51], s92 offen sc1\n"
    "v_lshlrev_b32_e32 v10, 16, v116\n"
    "v_and_b32_e32 v11, 0xffff0000, v116\n"
    "v_lshlrev_b32_e64 v12, 16, v117\n"
    "v_and_b32_e32 v13, 0xffff0000, v117\n"
    "v_lshlrev_b32_e64 v14, 16, v118\n"
    "v_and_b32_e32 v15, 0xffff0000, v118\n"
    "v_lshlrev_b32_e64 v16, 16, v119\n"
    "v_and_b32_e32 v17, 0xffff0000, v119\n"
    "s_waitcnt vmcnt(11)\n"
    "v_lshlrev_b32_e32 v72, 16, v128\n"
    "v_and_b32_e32 v73, 0xffff0000, v128\n"
    "v_lshlrev_b32_e64 v74, 16, v129\n"
    "v_and_b32_e32 v75, 0xffff0000, v129\n"
    "v_lshlrev_b32_e64 v76, 16, v130\n"
    "v_and_b32_e32 v77, 0xffff0000, v130\n"
    "v_lshlrev_b32_e64 v78, 16, v131\n"
    "v_and_b32_e32 v79, 0xffff0000, v131\n"
    "s_waitcnt vmcnt(10)\n"
    "v_dot2_f32_bf16 v72, v132, v8, v72\n"
    "v_dot2_f32_bf16 v73, v132, v9, v73\n"
    "v_dot2_f32_bf16 v74, v133, v8, v74\n"
    "v_dot2_f32_bf16 v75, v133, v9, v75\n"
    "v_dot2_f32_bf16 v76, v134, v8, v76\n"
    "v_dot2_f32_bf16 v77, v134, v9, v77\n"
    "v_dot2_f32_bf16 v78, v135, v8, v78\n"
    "v_dot2_f32_bf16 v79, v135, v9, v79\n"
    "s_waitcnt vmcnt(9)\n"
    "v_dot2_f32_bf16 v72, v136, v8, v72\n"
    "v_dot2_f32_bf16 v73, v136, v9, v73\n"
    "v_dot2_f32_bf16 v74, v137, v8, v74\n"
    "v_dot2_f32_bf16 v75, v137, v9, v75\n"
    "v_dot2_f32_bf16 v76, v138, v8, v76\n"
    "v_dot2_f32_bf16 v77, v138, v9, v77\n"
    "v_dot2_f32_bf16 v78, v139, v8, v78\n"
    "v_dot2_f32_bf16 v79, v139, v9, v79\n"
    "s_waitcnt vmcnt(8)\n"
    "v_dot2_f32_bf16 v72, v140, v8, v72\n"
    "v_dot2_f32_bf16 v73, v140, v9, v73\n"
    "v_dot2_f32_bf16 v74, v141, v8, v74\n"
    "v_dot2_f32_bf16 v75, v141, v9, v75\n"
    "v_dot2_f32_bf16 v76, v142, v8, v76\n"
    "v_dot2_f32_bf16 v77, v142, v9, v77\n"
    "v_dot2_f32_bf16 v78, v143, v8, v78\n"
    "v_dot2_f32_bf16 v79, v143, v9, v79\n"
    "PG10_%=:\n"
    "s_cbranch_execz PG14_%=\n"
    "s_nop 2\n"
    "s_waitcnt lgkmcnt(14)\n"
    "v_pk_fma_f32 v[72:73], v[72:73], v[80:81], v[10:11]\n"
    "v_pk_fma_f32 v[74:75], v[74:75], v[82:83], v[12:13]\n"
    "s_waitcnt lgkmcnt(13)\n"
    "v_pk_fma_f32 v[76:77], v[76:77], v[84:85], v[14:15]\n"
    "v_pk_fma_f32 v[78:79], v[78:79], v[86:87], v[16:17]\n"
    "v_cvt_pk_bf16_f32 v116, v72, v73\n"
    "v_cvt_pk_bf16_f32 v117, v74, v75\n"
    "v_cvt_pk_bf16_f32 v118, v76, v77\n"
    "v_cvt_pk_bf16_f32 v119, v78, v79\n"
    "s_add_u32 m0, s68, 0x26000\n"
    "s_add_u32 s92, s62, 0x6000\n"
    "buffer_load_dwordx4 v255, s[48:51], s92 offen sc1 lds\n"
    "buffer_load_dwordx4 v255, s[48:51], s92 offen offset:1024 sc1 lds\n"
    "s_add_u32 m0, s68, 0x10000\n"
    "s_add_u32 s92, s62, 0x8000\n"
    "buffer_load_dwordx4 v255, s[48:51], s92 offen sc1 lds\n"
    "buffer_load_dwordx4 v255, s[48:51], s92 offen offset:1024 sc1 lds\n"
    "v_lshlrev_b32_e64 v10, 16, v120\n"
    "v_and_b32_e32 v11, 0xffff0000, v120\n"
    "v_lshlrev_b32_e64 v12, 16, v121\n"
    "v_and_b32_e32 v13, 0xffff0000, v121\n"
    "v_lshlrev_b32_e64 v14, 16, v122\n"
    "v_and_b32_e32 v15, 0xffff0000, v122\n"
    "v_lshlrev_b32_e64 v16, 16, v123\n"
    "v_and_b32_e32 v17, 0xffff0000, v123\n"
    "s_waitcnt vmcnt(11)\n"
    "v_lshlrev_b32_e32 v72, 16, v144\n"
    "v_and_b32_e32 v73, 0xffff0000, v144\n"
    "v_lshlrev_b32_e64 v74, 16, v145\n"
    "v_and_b32_e32 v75, 0xffff0000, v145\n"
    "v_lshlrev_b32_e64 v76, 16, v146\n"
    "v_and_b32_e32 v77, 0xffff0000, v146\n"
    "v_lshlrev_b32_e64 v78, 16, v147\n"
    "v_and_b32_e32 v79, 0xffff0000, v147\n"
    "s_waitcnt vmcnt(10)\n"
    "v_dot2_f32_bf16 v72, v148, v8, v72\n"
    "v_dot2_f32_bf16 v73, v148, v9, v73\n"
    "v_dot2_f32_bf16 v74, v149, v8, v74\n"
    "v_dot2_f32_bf16 v75, v149, v9, v75\n"
    "v_dot2_f32_bf16 v76, v150, v8, v76\n"
    "v_dot2_f32_bf16 v77, v150, v9, v77\n"
    "v_dot2_f32_bf16 v78, v151, v8, v78\n"
    "v_dot2_f32_bf16 v79, v151, v9, v79\n"
    "s_waitcnt vmcnt(9)\n"
    "v_dot2_f32_bf16 v72, v152, v8, v72\n"
    "v_dot2_f32_bf16 v73, v152, v9, v73\n"
    "v_dot2_f32_bf16 v74, v153, v8, v74\n"
    "v_dot2_f32_bf16 v75, v153, v9, v75\n"
    "v_dot2_f32_bf16 v76, v154, v8, v76\n"
    "v_dot2_f32_bf16 v77, v154, v9, v77\n"
    "v_dot2_f32_bf16 v78, v155, v8, v78\n"
    "v_dot2_f32_bf16 v79, v155, v9, v79\n"
    "s_waitcnt vmcnt(8)\n"
    "v_dot2_f32_bf16 v72, v156, v8, v72\n"
    "v_dot2_f32_bf16 v73, v156, v9, v73\n"
    "v_dot2_f32_bf16 v74, v157, v8, v74\n"
    "v_dot2_f32_bf16 v75, v157, v9, v75\n"
    "v_dot2_f32_bf16 v76, v158, v8, v76\n"
    "v_dot2_f32_bf16 v77, v158, v9, v77\n"
    "v_dot2_f32_bf16 v78, v159, v8, v78\n"
    "v_dot2_f32_bf16 v79, v159, v9, v79\n"
    "s_nop 2\n"
    "s_waitcnt lgkmcnt(12)\n"
    "v_pk_fma_f32 v[72:73], v[72:73], v[88:89], v[10:11]\n"
    "v_pk_fma_f32 v[74:75], v[74:75], v[90:91], v[12:13]\n"
    "s_waitcnt lgkmcnt(11)\n"
    "v_pk_fma_f32 v[76:77], v[76:77], v[92:93], v[14:15]\n"
    "v_pk_fma_f32 v[78:79], v[78:79], v[94:95], v[16:17]\n"
    "v_cvt_pk_bf16_f32 v120, v72, v73\n"
    "v_cvt_pk_bf16_f32 v121, v74, v75\n"
    "v_cvt_pk_bf16_f32 v122, v76, v77\n"
    "v_cvt_pk_bf16_f32 v123, v78, v79\n"
    "s_add_u32 m0, s68, 0x14000\n"
    "s_add_u32 s92, s62, 0xa000\n"
    "buffer_load_dwordx4 v255, s[48:51], s92 offen sc1 lds\n"
    "buffer_load_dwordx4 v255, s[48:51], s92 offen offset:1024 sc1 lds\n"
    "s_add_u32 m0, s68, 0\n"
    "s_add_u32 s92, s62, 0xc000\n"
    "buffer_load_dwordx4 v255, s[48:51], s92 offen sc1 lds\n"
    "buffer_load_dwordx4 v255, s[48:51], s92 offen offset:1024 sc1 lds\n"
    "v_lshlrev_b32_e32 v10, 16, v124\n"
    "v_and_b32_e32 v11, 0xffff0000, v124\n"
    "v_lshlrev_b32_e64 v12, 16, v125\n"
    "v_and_b32_e32 v13, 0xffff0000, v125\n"
    "s_waitcnt vmcnt(11)\n"
    "v_lshlrev_b32_e32 v72, 16, v160\n"
    "v_and_b32_e32 v73, 0xffff0000, v160\n"
    "v_lshlrev_b32_e64 v74, 16, v161\n"
    "v_and_b32_e32 v75, 0xffff0000, v161\n"
    "s_waitcnt vmcnt(10)\n"
    "v_dot2_f32_bf16 v72, v162, v8, v72\n"
    "v_dot2_f32_bf16 v73, v162, v9, v73\n"
    "v_dot2_f32_bf16 v74, v163, v8, v74\n"
    "v_dot2_f32_bf16 v75, v163, v9, v75\n"
    "s_waitcnt vmcnt(9)\n"
    "v_dot2_f32_bf16 v72, v164, v8, v72\n"
    "v_dot2_f32_bf16 v73, v164, v9, v73\n"
    "v_dot2_f32_bf16 v74, v165, v8, v74\n"
    "v_dot2_f32_bf16 v75, v165, v9, v75\n"
    "s_waitcnt vmcnt(8)\n"
    "v_dot2_f32_bf16 v72, v166, v8, v72\n"
    "v_dot2_f32_bf16 v73, v166, v9, v73\n"
    "v_dot2_f32_bf16 v74, v167, v8, v74\n"
    "v_dot2_f32_bf16 v75, v167, v9, v75\n"
    "s_nop 2\n"
    "s_waitcnt lgkmcnt(10)\n"
    "v_pk_fma_f32 v[72:73], v[72:73], v[96:97], v[10:11]\n"
    "v_pk_fma_f32 v[74:75], v[74:75], v[98:99], v[12:13]\n"
    "v_cvt_pk_bf16_f32 v124, v72, v73\n"
    "v_cvt_pk_bf16_f32 v125, v74, v75\n"
    "s_add_u32 m0, s68, 0xc000\n"
    "s_add_u32 s92, s62, 0xe000\n"
    "buffer_load_dwordx4 v255, s[48:51], s92 offen sc1 lds\n"
    "buffer_load_dwordx4 v255, s[48:51], s92 offen offset:1024 sc1 lds\n"
    "s_add_u32 m0, s68, 0xe000\n"
    "s_add_u32 s92, s62, 0x10000\n"
    "buffer_load_dwordx4 v255, s[48:51], s92 offen sc1 lds\n"
    "buffer_load_dwordx4 v255, s[48:51], s92 offen offset:1024 sc1 lds\n"
    "s_add_u32 m0, s68, 0x12000\n"
    "s_add_u32 s92, s62, 0x12000\n"
    "buffer_load_dwordx4 v255, s[48:51], s92 offen sc1 lds\n"
    "buffer_load_dwordx4 v255, s[48:51], s92 offen offset:1024 sc1 lds\n"
    "buffer_store_dwordx4 v[116:119], v244, s[48:51], s65 offen sc1\n"
    "buffer_store_dwordx4 v[120:123], v244, s[48:51], s65 offen offset:2560 sc1\n"
    "buffer_store_dwordx2 v[124:125], v247, s[48:51], s66 offen sc1\n"
    "v_mov_b32_e32 v0, 0x3f803f80\n"
    "v_mov_b32_e32 v4, 0\n"
    "v_mov_b32_e64 v5, 0\n"
    "v_dot2_f32_bf16 v4, v116, v0, v4\n"
    "v_dot2_f32_bf16 v5, v116, v116, v5\n"
    "v_dot2_f32_bf16 v4, v117, v0, v4\n"
    "v_dot2_f32_bf16 v5, v117, v117, v5\n"
    "v_dot2_f32_bf16 v4, v118, v0, v4\n"
    "v_dot2_f32_bf16 v5, v118, v118, v5\n"
    "v_dot2_f32_bf16 v4, v119, v0, v4\n"
    "v_dot2_f32_bf16 v5, v119, v119, v5\n"
    "v_dot2_f32_bf16 v4, v120, v0, v4\n"
    "v_dot2_f32_bf16 v5, v120, v120, v5\n"
    "v_dot2_f32_bf16 v4, v121, v0, v4\n"
    "v_dot2_f32_bf16 v5, v121, v121, v5\n"
    "v_dot2_f32_bf16 v4, v122, v0, v4\n"
    "v_dot2_f32_bf16 v5, v122, v122, v5\n"
    "v_dot2_f32_bf16 v4, v123, v0, v4\n"
    "v_dot2_f32_bf16 v5, v123, v123, v5\n"
    "v_dot2_f32_bf16 v4, v124, v0, v4\n"
    "v_dot2_f32_bf16 v5, v124, v124, v5\n"
    "v_dot2_f32_bf16 v4, v125, v0, v4\n"
    "v_dot2_f32_bf16 v5, v125, v125, v5\n"
    "s_nop 2\n"
    "v_add_f32_dpp v4, v4, v4 row_ror:8 row_mask:0xf bank_mask:0xf\n"
    "v_add_f32_dpp v5, v5, v5 row_ror:8 row_mask:0xf bank_mask:0xf\n"
    "v_mov_b32_e32 v6, v4\n"
    "v_mov_b32_e32 v7, v5\n"
    "s_nop 0\n"
    "v_permlane32_swap_b32 v4, v6\n"
    "v_permlane32_swap_b32 v5, v7\n"
    "v_add_f32_e32 v4, v4, v6\n"
    "v_add_f32_e32 v5, v5, v7\n"
    "v_mov_b32_e32 v6, v4\n"
    "v_mov_b32_e32 v7, v5\n"
    "s_nop 0\n"
    "v_permlane16_swap_b32 v4, v6\n"
    "v_permlane16_swap_b32 v5, v7\n"
    "v_add_f32_e32 v4, v4, v6\n"
    "v_add_f32_e64 v5, v5, v7\n"
    "ds_write_b64 v2, v[4:5] offset:0\n"
    "s_waitcnt lgkmcnt(0)\n"
    "s_barrier\n"
    "ds_read_b128 v[72:75], v1 offset:0\n"
    "ds_read_b128 v[76:79], v1 offset:16\n"
    "s_waitcnt lgkmcnt(1)\n"
    "v_pk_add_f32 v[72:73], v[72:73], v[74:75]\n"
    "s_waitcnt lgkmcnt(0)\n"
    "v_pk_add_f32 v[76:77], v[76:77], v[78:79]\n"
    "v_pk_add_f32 v[4:5], v[72:73], v[76:77]\n"
    "v_mul_f32_e32 v4, 0x3acccccd, v4\n"
    "v_mul_f32_e32 v5, 0x3acccccd, v5\n"
    "v_mul_f32_e32 v6, v4, v4\n"
    "v_sub_f32_e32 v5, v5, v6\n"
    "v_max_f32_e64 v5, 0, v5\n"
    "v_add_f32_e32 v5, 0x3727c5ac, v5\n"
    "v_rsq_f32_e32 v6, v5\n"
    "v_sub_f32_e32 v4, 0, v4\n"
    "v_mul_f32_e32 v4, v4, v6\n"
    "v_mov_b32_e32 v7, v6\n"
    "v_mov_b32_e32 v5, v4\n"
    "v_lshlrev_b32_e32 v72, 16, v116\n"
    "v_and_b32_e32 v73, 0xffff0000, v116\n"
    "v_lshlrev_b32_e64 v74, 16, v117\n"
    "v_and_b32_e32 v75, 0xffff0000, v117\n"
    "v_lshlrev_b32_e64 v76, 16, v118\n"
    "v_and_b32_e32 v77, 0xffff0000, v118\n"
    "v_lshlrev_b32_e64 v78, 16, v119\n"
    "v_and_b32_e32 v79, 0xffff0000, v119\n"
    "v_pk_fma_f32 v[72:73], v[72:73], v[6:7], v[4:5]\n"
    "v_pk_fma_f32 v[74:75], v[74:75], v[6:7], v[4:5]\n"
    "v_pk_fma_f32 v[76:77], v[76:77], v[6:7], v[4:5]\n"
    "v_pk_fma_f32 v[78:79], v[78:79], v[6:7], v[4:5]\n"
    "v_pk_fma_f32 v[72:73], v[72:73], v[24:25], v[32:33]\n"
    "v_pk_fma_f32 v[74:75], v[74:75], v[26:27], v[34:35]\n"
    "v_pk_fma_f32 v[76:77], v[76:77], v[28:29], v[36:37]\n"
    "v_pk_fma_f32 v[78:79], v[78:79], v[30:31], v[38:39]\n"
    "v_cvt_pk_bf16_f32 v128, v72, v73\n"
    "v_cvt_pk_bf16_f32 v129, v74, v75\n"
    "v_cvt_pk_bf16_f32 v130, v76, v77\n"
    "v_cvt_pk_bf16_f32 v131, v78, v79\n"
    "buffer_store_dwordx4 v[128:131], v250, s[48:51], s38 offen sc1\n"
    "v_lshlrev_b32_e64 v72, 16, v120\n"
    "v_and_b32_e32 v73, 0xffff0000, v120\n"
    "v_lshlrev_b32_e64 v74, 16, v121\n"
    "v_and_b32_e32 v75, 0xffff0000, v121\n"
    "v_lshlrev_b32_e64 v76, 16, v122\n"
    "v_and_b32_e32 v77, 0xffff0000, v122\n"
    "v_lshlrev_b32_e64 v78, 16, v123\n"
    "v_and_b32_e32 v79, 0xffff0000, v123\n"
    "v_pk_fma_f32 v[72:73], v[72:73], v[6:7], v[4:5]\n"
    "v_pk_fma_f32 v[74:75], v[74:75], v[6:7], v[4:5]\n"
    "v_pk_fma_f32 v[76:77], v[76:77], v[6:7], v[4:5]\n"
    "v_pk_fma_f32 v[78:79], v[78:79], v[6:7], v[4:5]\n"
    "v_pk_fma_f32 v[72:73], v[72:73], v[40:41], v[48:49]\n"
    "v_pk_fma_f32 v[74:75], v[74:75], v[42:43], v[50:51]\n"
    "v_pk_fma_f32 v[76:77], v[76:77], v[44:45], v[52:53]\n"
    "v_pk_fma_f32 v[78:79], v[78:79], v[46:47], v[54:55]\n"
    "v_cvt_pk_bf16_f32 v144, v72, v73\n"
    "v_cvt_pk_bf16_f32 v145, v74, v75\n"
    "v_cvt_pk_bf16_f32 v146, v76, v77\n"
    "v_cvt_pk_bf16_f32 v147, v78, v79\n"
    "buffer_store_dwordx4 v[144:147], v250, s[48:51], s39 offen sc1\n"
    "v_lshlrev_b32_e64 v72, 16, v124\n"
    "v_and_b32_e32 v73, 0xffff0000, v124\n"
    "v_lshlrev_b32_e64 v74, 16, v125\n"
    "v_and_b32_e32 v75, 0xffff0000, v125\n"
    "v_pk_fma_f32 v[72:73], v[72:73], v[6:7], v[4:5]\n"
    "v_pk_fma_f32 v[74:75], v[74:75], v[6:7], v[4:5]\n"
    "v_pk_fma_f32 v[72:73], v[72:73], v[56:57], v[60:61]\n"
    "v_pk_fma_f32 v[74:75], v[74:75], v[58:59], v[62:63]\n"
    "v_cvt_pk_bf16_f32 v160, v72, v73\n"
    "v_cvt_pk_bf16_f32 v161, v74, v75\n"
    "v_mov_b32_e32 v162, v160\n"
    "v_mov_b32_e32 v163, v161\n"
    "s_nop 0\n"
    "v_permlane32_swap_b32 v160, v162\n"
    "v_permlane32_swap_b32 v161, v163\n"
    "buffer_store_dwordx4 v[160:163], v251, s[48:51], s60 offen sc1\n"
    "s_add_u32 s38, s36, 1\n"
    "s_and_b32 s38, s38, 3\n"
    "s_lshl_b32 s38, s38, 10\n"
    "v_add_u32_e32 v244, s38, v255\n"
    "v_lshrrev_b32_e32 v249, 1, v244\n"
    "s_add_u32 s38, s36, 2\n"
    "s_and_b32 s38, s38, 3\n"
    "s_lshl_b32 s38, s38, 10\n"
    "v_add_u32_e32 v245, s38, v255\n"
    "v_lshrrev_b32_e32 v250, 1, v245\n"
    "s_add_u32 s38, s36, 3\n"
    "s_and_b32 s38, s38, 3\n"
    "s_lshl_b32 s38, s38, 10\n"
    "v_add_u32_e32 v246, s38, v255\n"
    "v_lshrrev_b32_e32 v251, 1, v246\n"
    "s_add_u32 s38, s36, 4\n"
    "s_and_b32 s38, s38, 3\n"
    "s_lshl_b32 s38, s38, 10\n"
    "v_add_u32_e32 v247, s38, v255\n"
    "v_lshrrev_b32_e32 v252, 1, v247\n"
    "v_lshrrev_b32_e32 v238, 8, v255\n"
    "v_lshlrev_b32_e64 v228, 4, v238\n"
    "v_bfe_u32 v239, v255, 4, 4\n"
    "v_mul_u32_u24_e32 v239, 0x500, v239\n"
    "v_lshl_add_u32 v227, v238, 3, v239\n"
    "v_lshrrev_b32_e32 v248, 1, v255\n"
    "s_lshl_b32 s65, s89, 2\n"
    "s_add_u32 s65, s65, s36\n"
    "s_lshl_b32 s63, s65, 10\n"
    "s_add_u32 s63, s63, 0x50000\n"
    "s_mul_i32 s88, s65, 0xa000\n"
    "s_add_u32 s88, s88, 0x1c00000\n"
    "s_add_u32 m0, s68, 0x4000\n"
    "s_add_u32 s92, s62, 0x14000\n"
    "buffer_load_dwordx4 v255, s[48:51], s92 offen sc1 lds\n"
    "buffer_load_dwordx4 v255, s[48:51], s92 offen offset:1024 sc1 lds\n"
    "s_add_u32 m0, s68, 0x16000\n"
    "s_add_u32 s92, s62, 0x16000\n"
    "buffer_load_dwordx4 v255, s[48:51], s92 offen sc1 lds\n"
    "buffer_load_dwordx4 v255, s[48:51], s92 offen offset:1024 sc1 lds\n"
    "s_add_u32 m0, s68, 0x2000\n"
    "s_add_u32 s92, s62, 0x18000\n"
    "buffer_load_dwordx4 v255, s[48:51], s92 offen sc1 lds\n"
    "buffer_load_dwordx4 v255, s[48:51], s92 offen offset:1024 sc1 lds\n"
    "s_add_u32 m0, s68, 0x18000\n"
    "s_add_u32 s92, s62, 0x1a000\n"
    "buffer_load_dwordx4 v255, s[48:51], s92 offen sc1 lds\n"
    "buffer_load_dwordx4 v255, s[48:51], s92 offen offset:1024 sc1 lds\n"
    "v_add_u32_e32 v231, 1, v230\n"
    "s_waitcnt vmcnt(8) lgkmcnt(0)\n"
    "s_barrier\n"
    "buffer_store_dword v231, v241, s[48:51], s93 offen sc1\n"
    "s_add_u32 m0, s68, 0x1a000\n"
    "s_add_u32 s92, s62, 0x1c000\n"
    "buffer_load_dwordx4 v255, s[48:51], s92 offen sc1 lds\n"
    "buffer_load_dwordx4 v255, s[48:51], s92 offen offset:1024 sc1 lds\n"
    "s_add_u32 m0, s68, 0x8000\n"
    "s_add_u32 s92, s62, 0x1e000\n"
    "buffer_load_dwordx4 v255, s[48:51], s92 offen sc1 lds\n"
    "buffer_load_dwordx4 v255, s[48:51], s92 offen offset:1024 sc1 lds\n"
    "s_add_u32 m0, s68, 0xa000\n"
    "s_add_u32 s92, s62, 0x20000\n"
    "buffer_load_dwordx4 v255, s[48:51], s92 offen sc1 lds\n"
    "buffer_load_dwordx4 v255, s[48:51], s92 offen offset:1024 sc1 lds\n"
    "s_add_u32 m0, s68, 0x1c000\n"
    "s_add_u32 s92, s62, 0x22000\n"
    "buffer_load_dwordx4 v255, s[48:51], s92 offen sc1 lds\n"
    "buffer_load_dwordx4 v255, s[48:51], s92 offen offset:1024 sc1 lds\n"
    "s_add_u32 m0, s68, 0x1e000\n"
    "s_add_u32 s92, s62, 0x24000\n"
    "buffer_load_dwordx4 v255, s[48:51], s92 offen sc1 lds\n"
    "buffer_load_dwordx4 v255, s[48:51], s92 offen offset:1024 sc1 lds\n"
    "s_add_u32 m0, s68, 0x20000\n"
    "s_add_u32 s92, s62, 0x26000\n"
    "buffer_load_dwordx4 v255, s[48:51], s92 offen sc1 lds\n"
    "buffer_load_dwordx4 v255, s[48:51], s92 offen offset:1024 sc1 lds\n"
    "s_add_u32 s95, s94, 0x10000\n"
    "buffer_load_dwordx4 a[136:139], v255, s[48:51], s95 offen sc1\n"
    "buffer_load_dwordx4 a[140:143], v255, s[48:51], s95 offen offset:1024 sc1\n"
    "buffer_load_dwordx4 a[144:147], v255, s[48:51], s95 offen offset:2048 sc1\n"
    "v_bfe_u32 v240, v255, 4, 2\n"
    "v_lshlrev_b32_e32 v240, 2, v240\n"
    "buffer_load_dword v229, v240, s[48:51], s63 offen sc1\n"
    "v_mov_b32_e64 v232, v255\n"
    "v_add_u32_e32 v233, 0x10000, v255\n"
    "v_add_u32_e32 v234, 0x20000, v255\n"
    "s_barrier\n"
    "ds_read_b128 v[136:139], v234 offset:16384\n"
    "ds_read_b128 v[140:143], v234 offset:17408\n"
    "ds_read_b128 v[144:147], v234 offset:18432\n"
    "ds_read_b128 v[148:151], v234 offset:19456\n"
    "ds_read_b128 v[152:155], v234 offset:20480\n"
    "ds_read_b128 v[156:159], v234 offset:21504\n"
    "ds_read_b128 v[160:163], v234 offset:22528\n"
    "ds_read_b128 v[164:167], v234 offset:23552\n"
    "s_add_u32 s66, s88, 0x1000\n"
    "s_waitcnt vmcnt(0)\n"
    "v_sub_u32_e32 v229, v229, v230\n"
    "v_subrev_u32_e32 v229, 1, v229\n"
    "v_cmp_gt_u32_e32 vcc, 3, v229\n"
    "s_nop 1\n"
    "s_cmp_eq_u64 vcc, -1\n"
    "s_cbranch_scc1 POK7_%=\n"
    "s_mov_b32 s61, 20000\n"
    "PSP7_%=:\n"
    "s_sleep 4\n"
    "buffer_load_dword v229, v240, s[48:51], s63 offen sc1\n"
    "s_waitcnt vmcnt(0)\n"
    "v_sub_u32_e32 v229, v229, v230\n"
    "v_subrev_u32_e32 v229, 1, v229\n"
    "v_cmp_gt_u32_e32 vcc, 3, v229\n"
    "s_nop 1\n"
    "s_cmp_eq_u64 vcc, -1\n"
    "s_cbranch_scc1 POK7_%=\n"
    "s_sub_u32 s61, s61, 1\n"
    "s_cmp_lg_u32 s61, 0\n"
    "s_cbranch_scc1 PSP7_%=\n"
    "POK7_%=:\n"
    "buffer_load_dwordx4 a[0:3], v255, s[48:51], s88 offen sc1\n"
    "buffer_load_dwordx4 a[8:11], v255, s[48:51], s88 offen offset:2048 sc1\n"
    "buffer_load_dwordx4 a[16:19], v255, s[48:51], s66 offen sc1\n"
    "buffer_load_dwordx4 a[24:27], v255, s[48:51], s66 offen offset:2048 sc1\n"
    "buffer_load_dwordx4 a[4:7], v255, s[48:51], s88 offen offset:1024 sc1\n"
    "buffer_load_dwordx4 a[12:15], v255, s[48:51], s88 offen offset:3072 sc1\n"
    "buffer_load_dwordx4 a[20:23], v255, s[48:51], s66 offen offset:1024 sc1\n"
    "buffer_load_dwordx4 a[28:31], v255, s[48:51], s66 offen offset:3072 sc1\n"
    "s_add_u32 s88, s88, 0x2000\n"
    "buffer_load_dwordx4 a[32:35], v255, s[48:51], s88 offen sc1\n"
    "buffer_load_dwordx4 a[36:39], v255, s[48:51], s88 offen offset:1024 sc1\n"
    "buffer_load_dwordx4 a[40:43], v255, s[48:51], s88 offen offset:2048 sc1\n"
    "buffer_load_dwordx4 a[44:47], v255, s[48:51], s88 offen offset:3072 sc1\n"
    "s_add_u32 s88, s88, 0x1000\n"
    "s_waitcnt vmcnt(11) lgkmcnt(7)\n"
    "v_mfma_f32_16x16x32_bf16 v[0:3], v[136:139], a[0:3], 0\n"
    "s_waitcnt vmcnt(10) lgkmcnt(6)\n"
    "ds_read_b128 v[168:171], v232 offset:24576\n"
    "ds_read_b128 v[172:175], v232 offset:25600\n"
    "v_mfma_f32_16x16x32_bf16 v[0:3], v[140:143], a[8:11], v[0:3]\n"
    "s_waitcnt vmcnt(9) lgkmcnt(7)\n"
    "ds_read_b128 v[176:179], v232 offset:26624\n"
    "ds_read_b128 v[180:183], v232 offset:27648\n"
    "v_mfma_f32_16x16x32_bf16 v[0:3], v[144:147], a[16:19], v[0:3]\n"
    "s_waitcnt vmcnt(8) lgkmcnt(8)\n"
    "buffer_load_dwordx4 a[48:51], v255, s[48:51], s88 offen sc1\n"
    "v_mfma_f32_16x16x32_bf16 v[0:3], v[148:151], a[24:27], v[0:3]\n"
    "s_waitcnt vmcnt(8)\n"
    "v_mfma_f32_16x16x32_bf16 v[4:7], v[136:139], a[4:7], 0\n"
    "s_waitcnt vmcnt(7)\n"
    "ds_read_b128 v[184:187], v232 offset:28672\n"
    "v_mfma_f32_16x16x32_bf16 v[4:7], v[140:143], a[12:15], v[4:7]\n"
    "s_waitcnt vmcnt(6)\n"
    "ds_read_b128 v[188:191], v232 offset:29696\n"
    "buffer_load_dwordx4 a[52:55], v255, s[48:51], s88 offen offset:1024 sc1\n"
    "v_mfma_f32_16x16x32_bf16 v[4:7], v[144:147], a[20:23], v[4:7]\n"
    "s_waitcnt vmcnt(6)\n"
    "ds_read_b128 v[192:195], v232 offset:30720\n"
    "v_mfma_f32_16x16x32_bf16 v[4:7], v[148:151], a[28:31], v[4:7]\n"
    "s_waitcnt lgkmcnt(10)\n"
    "ds_read_b128 v[196:199], v232 offset:31744\n"
    "v_mfma_f32_16x16x32_bf16 v[8:11], v[152:155], a[0:3], 0\n"
    "s_waitcnt lgkmcnt(10)\n"
    "v_mfma_f32_16x16x32_bf16 v[8:11], v[156:159], a[8:11], v[8:11]\n"
    "s_waitcnt lgkmcnt(9)\n"
    "v_mfma_f32_16x16x32_bf16 v[8:11], v[160:163], a[16:19], v[8:11]\n"
    "s_waitcnt lgkmcnt(8)\n"
    "buffer_load_dwordx4 a[56:59], v255, s[48:51], s88 offen offset:2048 sc1\n"
    "v_mfma_f32_16x16x32_bf16 v[8:11], v[164:167], a[24:27], v[8:11]\n"
    "v_mfma_f32_16x16x32_bf16 v[12:15], v[152:155], a[4:7], 0\n"
    "ds_read_b128 v[136:139], v234 offset:8192\n"
    "v_mfma_f32_16x16x32_bf16 v[12:15], v[156:159], a[12:15], v[12:15]\n"
    "ds_read_b128 v[140:143], v234 offset:9216\n"
    "v_mfma_f32_16x16x32_bf16 v[12:15], v[160:163], a[20:23], v[12:15]\n"
    "ds_read_b128 v[144:147], v234 offset:10240\n"
    "v_mfma_f32_16x16x32_bf16 v[12:15], v[164:167], a[28:31], v[12:15]\n"
    "s_waitcnt lgkmcnt(10)\n"
    "ds_read_b128 v[148:151], v234 offset:11264\n"
    "v_mfma_f32_16x16x32_bf16 v[16:19], v[168:171], a[0:3], 0\n"
    "s_waitcnt lgkmcnt(10)\n"
    "buffer_load_dwordx4 a[60:63], v255, s[48:51], s88 offen offset:3072 sc1\n"
    "v_mfma_f32_16x16x32_bf16 v[16:19], v[172:175], a[8:11], v[16:19]\n"
    "s_waitcnt lgkmcnt(9)\n"
    "v_mfma_f32_16x16x32_bf16 v[16:19], v[176:179], a[16:19], v[16:19]\n"
    "s_waitcnt lgkmcnt(8)\n"
    "v_mfma_f32_16x16x32_bf16 v[16:19], v[180:183], a[24:27], v[16:19]\n"
    "v_mfma_f32_16x16x32_bf16 v[20:23], v[168:171], a[4:7], 0\n"
    "s_lshr_b32 s38, s65, 1\n"
    "ds_read_b128 v[152:155], v234 offset:12288\n"
    "v_mfma_f32_16x16x32_bf16 v[20:23], v[172:175], a[12:15], v[20:23]\n"
    "s_add_u32 s88, s88, 0x1000\n"
    "ds_read_b128 v[156:159], v234 offset:13312\n"
    "buffer_load_dwordx4 a[64:67], v255, s[48:51], s88 offen sc1\n"
    "v_mfma_f32_16x16x32_bf16 v[20:23], v[176:179], a[20:23], v[20:23]\n"
    "ds_read_b128 v[160:163], v234 offset:14336\n"
    "v_mfma_f32_16x16x32_bf16 v[20:23], v[180:183], a[28:31], v[20:23]\n"
    "s_waitcnt lgkmcnt(10)\n"
    "s_and_b32 s60, s65, 1\n"
    "ds_read_b128 v[164:167], v234 offset:15360\n"
    "v_mfma_f32_16x16x32_bf16 v[24:27], v[184:187], a[0:3], 0\n"
    "s_waitcnt lgkmcnt(10)\n"
    "v_mfma_f32_16x16x32_bf16 v[24:27], v[188:191], a[8:11], v[24:27]\n"
    "s_waitcnt lgkmcnt(9)\n"
    "v_mfma_f32_16x16x32_bf16 v[24:27], v[192:195], a[16:19], v[24:27]\n"
    "s_waitcnt lgkmcnt(8)\n"
    "s_lshr_b32 s39, s90, 2\n"
    "buffer_load_dwordx4 a[68:71], v255, s[48:51], s88 offen offset:1024 sc1\n"
    "v_mfma_f32_16x16x32_bf16 v[24:27], v[196:199], a[24:27], v[24:27]\n"
    "v_mfma_f32_16x16x32_bf16 v[28:31], v[184:187], a[4:7], 0\n"
    "ds_read_b128 v[168:171], v234 offset:24576\n"
    "v_mfma_f32_16x16x32_bf16 v[28:31], v[188:191], a[12:15], v[28:31]\n"
    "s_lshl_b32 s93, s89, 2\n"
    "ds_read_b128 v[172:175], v234 offset:25600\n"
    "v_mfma_f32_16x16x32_bf16 v[28:31], v[192:195], a[20:23], v[28:31]\n"
    "ds_read_b128 v[176:179], v234 offset:26624\n"
    "v_mfma_f32_16x16x32_bf16 v[28:31], v[196:199], a[28:31], v[28:31]\n"
    "s_waitcnt lgkmcnt(10)\n"
    "ds_read_b128 v[180:183], v234 offset:27648\n"
    "v_mfma_f32_16x16x32_bf16 v[32:35], v[136:139], a[0:3], 0\n"
    "s_waitcnt lgkmcnt(10)\n"
    "s_add_u32 s93, s93, s39\n"
    "buffer_load_dwordx4 a[160:163], v255, s[48:51], s88 offen offset:2048 sc1\n"
    "v_mfma_f32_16x16x32_bf16 v[32:35], v[140:143], a[8:11], v[32:35]\n"
    "s_waitcnt lgkmcnt(9)\n"
    "v_mfma_f32_16x16x32_bf16 v[32:35], v[144:147], a[16:19], v[32:35]\n"
    "s_waitcnt lgkmcnt(8)\n"
    "v_mfma_f32_16x16x32_bf16 v[32:35], v[148:151], a[24:27], v[32:35]\n"
    "s_lshl_b32 s93, s93, 5\n"
    "v_mfma_f32_16x16x32_bf16 v[36:39], v[136:139], a[4:7], 0\n"
    "ds_read_b128 v[184:187], v234 offset:28672\n"
    "v_mfma_f32_16x16x32_bf16 v[36:39], v[140:143], a[12:15], v[36:39]\n"
    "ds_read_b128 v[188:191], v234 offset:29696\n"
    "buffer_load_dwordx4 a[164:167], v255, s[48:51], s88 offen offset:3072 sc1\n"
    "v_mfma_f32_16x16x32_bf16 v[36:39], v[144:147], a[20:23], v[36:39]\n"
    "s_and_b32 s66, s90, 3\n"
    "ds_read_b128 v[192:195], v234 offset:30720\n"
    "v_mfma_f32_16x16x32_bf16 v[36:39], v[148:151], a[28:31], v[36:39]\n"
    "s_waitcnt lgkmcnt(10)\n"
    "ds_read_b128 v[196:199], v234 offset:31744\n"
    "v_mfma_f32_16x16x32_bf16 v[40:43], v[152:155], a[0:3], 0\n"
    "s_waitcnt lgkmcnt(10)\n"
    "v_mfma_f32_16x16x32_bf16 v[40:43], v[156:159], a[8:11], v[40:43]\n"
    "s_waitcnt lgkmcnt(9)\n"
    "s_add_u32 s93, s93, s66\n"
    "v_mfma_f32_16x16x32_bf16 v[40:43], v[160:163], a[16:19], v[40:43]\n"
    "s_waitcnt lgkmcnt(8)\n"
    "s_add_u32 s88, s88, 0x1000\n"
    "buffer_load_dwordx4 a[168:171], v255, s[48:51], s88 offen sc1\n"
    "v_mfma_f32_16x16x32_bf16 v[40:43], v[164:167], a[24:27], v[40:43]\n"
    "v_mfma_f32_16x16x32_bf16 v[44:47], v[152:155], a[4:7], 0\n"
    "s_barrier\n"
    "ds_read_b128 v[136:139], v233 offset:0\n"
    "v_mfma_f32_16x16x32_bf16 v[44:47], v[156:159], a[12:15], v[44:47]\n"
    "s_lshl_b32 s93, s93, 2\n"
    "ds_read_b128 v[140:143], v233 offset:1024\n"
    "v_mfma_f32_16x16x32_bf16 v[44:47], v[160:163], a[20:23], v[44:47]\n"
    "ds_read_b128 v[144:147], v233 offset:2048\n"
    "v_mfma_f32_16x16x32_bf16 v[44:47], v[164:167], a[28:31], v[44:47]\n"
    "s_waitcnt vmcnt(12) lgkmcnt(10)\n"
    "s_add_u32 s93, s93, 0x4000\n"
    "ds_read_b128 v[148:151], v233 offset:3072\n"
    "v_mfma_f32_16x16x32_bf16 v[0:3], v[168:171], a[32:35], v[0:3]\n"
    "s_waitcnt vmcnt(10) lgkmcnt(10)\n"
    "buffer_load_dwordx4 a[172:175], v255, s[48:51], s88 offen offset:1024 sc1\n"
    "v_mfma_f32_16x16x32_bf16 v[0:3], v[172:175], a[40:43], v[0:3]\n"
    "s_waitcnt vmcnt(9) lgkmcnt(9)\n"
    "v_mfma_f32_16x16x32_bf16 v[0:3], v[176:179], a[48:51], v[0:3]\n"
    "s_waitcnt vmcnt(7) lgkmcnt(8)\n"
    "s_lshl_b32 s38, s38, 2\n"
    "v_mfma_f32_16x16x32_bf16 v[0:3], v[180:183], a[56:59], v[0:3]\n"
    "v_mfma_f32_16x16x32_bf16 v[4:7], v[168:171], a[36:39], v[4:7]\n"
    "ds_read_b128 v[152:155], v233 offset:4096\n"
    "v_mfma_f32_16x16x32_bf16 v[4:7], v[172:175], a[44:47], v[4:7]\n"
    "s_add_u32 s38, s38, s39\n"
    "ds_read_b128 v[156:159], v233 offset:5120\n"
    "buffer_load_dwordx4 a[176:179], v255, s[48:51], s88 offen offset:2048 sc1\n"
    "v_mfma_f32_16x16x32_bf16 v[4:7], v[176:179], a[52:55], v[4:7]\n"
    "s_waitcnt vmcnt(7)\n"
    "ds_read_b128 v[160:163], v233 offset:6144\n"
    "v_mfma_f32_16x16x32_bf16 v[4:7], v[180:183], a[60:63], v[4:7]\n"
    "s_waitcnt lgkmcnt(10)\n"
    "ds_read_b128 v[164:167], v233 offset:7168\n"
    "v_mfma_f32_16x16x32_bf16 v[8:11], v[184:187], a[32:35], v[8:11]\n"
    "s_waitcnt lgkmcnt(10)\n"
    "s_mul_i32 s38, s38, 0xe000\n"
    "v_mfma_f32_16x16x32_bf16 v[8:11], v[188:191], a[40:43], v[8:11]\n"
    "s_waitcnt lgkmcnt(9)\n"
    "v_mfma_f32_16x16x32_bf16 v[8:11], v[192:195], a[48:51], v[8:11]\n"
    "s_waitcnt lgkmcnt(8)\n"
    "buffer_load_dwordx4 a[180:183], v255, s[48:51], s88 offen offset:3072 sc1\n"
    "v_mfma_f32_16x16x32_bf16 v[8:11], v[196:199], a[56:59], v[8:11]\n"
    "s_add_u32 s38, s38, 0x2400000\n"
    "v_mfma_f32_16x16x32_bf16 v[12:15], v[184:187], a[36:39], v[12:15]\n"
    "ds_read_b128 v[168:171], v233 offset:16384\n"
    "v_mfma_f32_16x16x32_bf16 v[12:15], v[188:191], a[44:47], v[12:15]\n"
    "ds_read_b128 v[172:175], v233 offset:17408\n"
    "v_mfma_f32_16x16x32_bf16 v[12:15], v[192:195], a[52:55], v[12:15]\n"
    "s_and_b32 s39, s90, 3\n"
    "ds_read_b128 v[176:179], v233 offset:18432\n"
    "v_mfma_f32_16x16x32_bf16 v[12:15], v[196:199], a[60:63], v[12:15]\n"
    "s_waitcnt lgkmcnt(10)\n"
    "s_add_u32 s88, s88, 0x1000\n"
    "ds_read_b128 v[180:183], v233 offset:19456\n"
    "v_mfma_f32_16x16x32_bf16 v[16:19], v[136:139], a[32:35], v[16:19]\n"
    "s_waitcnt lgkmcnt(10)\n"
    "buffer_load_dwordx4 a[184:187], v255, s[48:51], s88 offen sc1\n"
    "v_mfma_f32_16x16x32_bf16 v[16:19], v[140:143], a[40:43], v[16:19]\n"
    "s_waitcnt lgkmcnt(9)\n"
    "s_lshl_b32 s39, s39, 1\n"
    "v_mfma_f32_16x16x32_bf16 v[16:19], v[144:147], a[48:51], v[16:19]\n"
    "s_waitcnt lgkmcnt(8)\n"
    "v_mfma_f32_16x16x32_bf16 v[16:19], v[148:151], a[56:59], v[16:19]\n"
    "v_mfma_f32_16x16x32_bf16 v[20:23], v[136:139], a[36:39], v[20:23]\n"
    "s_add_u32 s39, s39, s60\n"
    "ds_read_b128 v[184:187], v233 offset:20480\n"
    "v_mfma_f32_16x16x32_bf16 v[20:23], v[140:143], a[44:47], v[20:23]\n"
    "ds_read_b128 v[188:191], v233 offset:21504\n"
    "buffer_load_dwordx4 a[188:191], v255, s[48:51], s88 offen offset:1024 sc1\n"
    "v_mfma_f32_16x16x32_bf16 v[20:23], v[144:147], a[52:55], v[20:23]\n"
    "ds_read_b128 v[192:195], v233 offset:22528\n"
    "v_mfma_f32_16x16x32_bf16 v[20:23], v[148:151], a[60:63], v[20:23]\n"
    "s_waitcnt lgkmcnt(10)\n"
    "s_lshl_b32 s66, s39, 11\n"
    "ds_read_b128 v[196:199], v233 offset:23552\n"
    "v_mfma_f32_16x16x32_bf16 v[24:27], v[152:155], a[32:35], v[24:27]\n"
    "s_waitcnt lgkmcnt(10)\n"
    "v_mfma_f32_16x16x32_bf16 v[24:27], v[156:159], a[40:43], v[24:27]\n"
    "s_waitcnt lgkmcnt(9)\n"
    "v_mfma_f32_16x16x32_bf16 v[24:27], v[160:163], a[48:51], v[24:27]\n"
    "s_waitcnt lgkmcnt(8)\n"
    "s_add_u32 s66, s66, s38\n"
    "v_mfma_f32_16x16x32_bf16 v[24:27], v[164:167], a[56:59], v[24:27]\n"
    "buffer_load_dwordx4 v[80:83], v255, s[48:51], s88 offen offset:2048 sc1\n"
    "v_mfma_f32_16x16x32_bf16 v[28:31], v[152:155], a[36:39], v[28:31]\n"
    "ds_read_b128 v[136:139], v232 offset:0\n"
    "v_mfma_f32_16x16x32_bf16 v[28:31], v[156:159], a[44:47], v[28:31]\n"
    "s_lshl_b32 s91, s39, 10\n"
    "ds_read_b128 v[140:143], v232 offset:1024\n"
    "v_mfma_f32_16x16x32_bf16 v[28:31], v[160:163], a[52:55], v[28:31]\n"
    "ds_read_b128 v[144:147], v232 offset:2048\n"
    "v_mfma_f32_16x16x32_bf16 v[28:31], v[164:167], a[60:63], v[28:31]\n"
    "s_waitcnt lgkmcnt(10)\n"
    "ds_read_b128 v[148:151], v232 offset:3072\n"
    "v_mfma_f32_16x16x32_bf16 v[32:35], v[168:171], a[32:35], v[32:35]\n"
    "s_waitcnt lgkmcnt(10)\n"
    "s_add_u32 s91, s91, s38\n"
    "buffer_load_dwordx4 v[84:87], v255, s[48:51], s88 offen offset:3072 sc1\n"
    "v_mfma_f32_16x16x32_bf16 v[32:35], v[172:175], a[40:43], v[32:35]\n"
    "s_waitcnt lgkmcnt(9)\n"
    "v_mfma_f32_16x16x32_bf16 v[32:35], v[176:179], a[48:51], v[32:35]\n"
    "s_waitcnt lgkmcnt(8)\n"
    "v_mfma_f32_16x16x32_bf16 v[32:35], v[180:183], a[56:59], v[32:35]\n"
    "s_add_u32 s91, s91, 0x4000\n"
    "v_mfma_f32_16x16x32_bf16 v[36:39], v[168:171], a[36:39], v[36:39]\n"
    "s_add_u32 s88, s88, 0x1000\n"
    "ds_read_b128 v[152:155], v232 offset:4096\n"
    "v_mfma_f32_16x16x32_bf16 v[36:39], v[172:175], a[44:47], v[36:39]\n"
    "PG11_%=:\n"
    "s_cbranch_execz PG15_%=\n"
    "ds_read_b128 v[156:159], v232 offset:5120\n"
    "buffer_load_dwordx4 v[88:91], v255, s[48:51], s88 offen sc1\n"
    "v_mfma_f32_16x16x32_bf16 v[36:39], v[176:179], a[52:55], v[36:39]\n"
    "s_and_b32 s39, s90, 10\n"
    "ds_read_b128 v[160:163], v232 offset:6144\n"
    "v_mfma_f32_16x16x32_bf16 v[36:39], v[180:183], a[60:63], v[36:39]\n"
    "s_waitcnt lgkmcnt(10)\n"
    "ds_read_b128 v[164:167], v232 offset:7168\n"
    "v_mfma_f32_16x16x32_bf16 v[40:43], v[184:187], a[32:35], v[40:43]\n"
    "s_waitcnt lgkmcnt(10)\n"
    "s_nop 3\n"
    "v_mfma_f32_16x16x32_bf16 v[40:43], v[188:191], a[40:43], v[40:43]\n"
    "s_waitcnt lgkmcnt(9)\n"
    "s_cmp_eq_u32 s39, 10\n"
    "s_cselect_b32 s91, 0x40000000, s91\n"
    "v_mfma_f32_16x16x32_bf16 v[40:43], v[192:195], a[48:51], v[40:43]\n"
    "s_waitcnt lgkmcnt(8)\n"
    "v_mfma_f32_16x16x32_bf16 v[40:43], v[196:199], a[56:59], v[40:43]\n"
    "buffer_load_dwordx4 v[92:95], v255, s[48:51], s88 offen offset:1024 sc1\n"
    "s_nop 3\n"
    "v_mfma_f32_16x16x32_bf16 v[44:47], v[184:187], a[36:39], v[44:47]\n"
    "s_lshr_b32 s63, s75, 1\n"
    "ds_read_b128 v[168:171], v232 offset:49152\n"
    "v_mfma_f32_16x16x32_bf16 v[44:47], v[188:191], a[44:47], v[44:47]\n"
    "ds_read_b128 v[172:175], v232 offset:50176\n"
    "v_mfma_f32_16x16x32_bf16 v[44:47], v[192:195], a[52:55], v[44:47]\n"
    "ds_read_b128 v[176:179], v232 offset:51200\n"
    "s_nop 3\n"
    "v_mfma_f32_16x16x32_bf16 v[44:47], v[196:199], a[60:63], v[44:47]\n"
    "s_waitcnt vmcnt(13) lgkmcnt(10)\n"
    "s_lshl_b32 s63, s63, 2\n"
    "ds_read_b128 v[180:183], v232 offset:52224\n"
    "v_mfma_f32_16x16x32_bf16 v[0:3], v[136:139], a[64:67], v[0:3]\n"
    "s_waitcnt vmcnt(11) lgkmcnt(10)\n"
    "buffer_load_dwordx4 v[96:99], v255, s[48:51], s88 offen offset:2048 sc1\n"
    "v_mfma_f32_16x16x32_bf16 v[0:3], v[140:143], a[160:163], v[0:3]\n"
    "s_waitcnt vmcnt(10) lgkmcnt(9)\n"
    "s_nop 3\n"
    "v_mfma_f32_16x16x32_bf16 v[0:3], v[144:147], a[168:171], v[0:3]\n"
    "s_waitcnt vmcnt(8) lgkmcnt(8)\n"
    "s_add_u32 s63, s63, s36\n"
    "v_mfma_f32_16x16x32_bf16 v[0:3], v[148:151], a[176:179], v[0:3]\n"
    "v_mfma_f32_16x16x32_bf16 v[4:7], v[136:139], a[68:71], v[4:7]\n"
    "ds_read_b128 v[184:187], v232 offset:53248\n"
    "s_nop 3\n"
    "v_mfma_f32_16x16x32_bf16 v[4:7], v[140:143], a[164:167], v[4:7]\n"
    "s_lshl_b32 s63, s63, 7\n"
    "ds_read_b128 v[188:191], v232 offset:54272\n"
    "buffer_load_dwordx4 v[100:103], v255, s[48:51], s88 offen offset:3072 sc1\n"
    "v_mfma_f32_16x16x32_bf16 v[4:7], v[144:147], a[172:175], v[4:7]\n"
    "s_waitcnt vmcnt(8)\n"
    "ds_read_b128 v[192:195], v232 offset:55296\n"
    "v_mfma_f32_16x16x32_bf16 v[4:7], v[148:151], a[180:183], v[4:7]\n"
    "s_waitcnt lgkmcnt(10)\n"
    "ds_read_b128 v[196:199], v232 offset:56320\n"
    "s_nop 3\n"
    "v_mfma_f32_16x16x32_bf16 v[8:11], v[152:155], a[64:67], v[8:11]\n"
    "s_waitcnt lgkmcnt(10)\n"
    "s_add_u32 s63, s63, 0x4000\n"
    "v_mfma_f32_16x16x32_bf16 v[8:11], v[156:159], a[160:163], v[8:11]\n"
    "s_waitcnt lgkmcnt(9)\n"
    "v_mfma_f32_16x16x32_bf16 v[8:11], v[160:163], a[168:171], v[8:11]\n"
    "s_waitcnt lgkmcnt(8)\n"
    "s_add_u32 s88, s88, 0x1000\n"
    "s_nop 3\n"
    "v_mfma_f32_16x16x32_bf16 v[8:11], v[164:167], a[176:179], v[8:11]\n"
    "s_lshl_b32 s38, s75, 2\n"
    "buffer_load_dwordx4 v[104:107], v255, s[48:51], s88 offen sc1\n"
    "v_mfma_f32_16x16x32_bf16 v[12:15], v[152:155], a[68:71], v[12:15]\n"
    "s_barrier\n"
    "ds_read_b128 v[136:139], v232 offset:57344\n"
    "v_mfma_f32_16x16x32_bf16 v[12:15], v[156:159], a[164:167], v[12:15]\n"
    "ds_read_b128 v[140:143], v232 offset:58368\n"
    "s_nop 3\n"
    "v_mfma_f32_16x16x32_bf16 v[12:15], v[160:163], a[172:175], v[12:15]\n"
    "s_add_u32 s38, s38, s36\n"
    "ds_read_b128 v[144:147], v232 offset:59392\n"
    "v_mfma_f32_16x16x32_bf16 v[12:15], v[164:167], a[180:183], v[12:15]\n"
    "s_waitcnt lgkmcnt(10)\n"
    "ds_read_b128 v[148:151], v232 offset:60416\n"
    "v_mfma_f32_16x16x32_bf16 v[16:19], v[168:171], a[64:67], v[16:19]\n"
    "s_waitcnt lgkmcnt(10)\n"
    "buffer_load_dwordx4 v[108:111], v255, s[48:51], s88 offen offset:1024 sc1\n"
    "s_nop 3\n"
    "v_mfma_f32_16x16x32_bf16 v[16:19], v[172:175], a[160:163], v[16:19]\n"
    "s_waitcnt lgkmcnt(9)\n"
    "s_mul_i32 s64, s38, 0xe000\n"
    "v_mfma_f32_16x16x32_bf16 v[16:19], v[176:179], a[168:171], v[16:19]\n"
    "s_waitcnt lgkmcnt(8)\n"
    "v_mfma_f32_16x16x32_bf16 v[16:19], v[180:183], a[176:179], v[16:19]\n"
    "s_nop 3\n"
    "v_mfma_f32_16x16x32_bf16 v[20:23], v[168:171], a[68:71], v[20:23]\n"
    "s_add_u32 s64, s64, 0x2400000\n"
    "ds_read_b128 v[152:155], v232 offset:61440\n"
    "v_mfma_f32_16x16x32_bf16 v[20:23], v[172:175], a[164:167], v[20:23]\n"
    "ds_read_b128 v[156:159], v232 offset:62464\n"
    "buffer_load_dwordx4 v[112:115], v255, s[48:51], s88 offen offset:2048 sc1\n"
    "v_mfma_f32_16x16x32_bf16 v[20:23], v[176:179], a[172:175], v[20:23]\n"
    "ds_read_b128 v[160:163], v232 offset:63488\n"
    "s_nop 3\n"
    "v_mfma_f32_16x16x32_bf16 v[20:23], v[180:183], a[180:183], v[20:23]\n"
    "s_waitcnt lgkmcnt(10)\n"
    "ds_read_b128 v[164:167], v232 offset:64512\n"
    "v_mfma_f32_16x16x32_bf16 v[24:27], v[184:187], a[64:67], v[24:27]\n"
    "s_waitcnt lgkmcnt(10)\n"
    "v_mfma_f32_16x16x32_bf16 v[24:27], v[188:191], a[160:163], v[24:27]\n"
    "s_waitcnt lgkmcnt(9)\n"
    "s_nop 3\n"
    "v_mfma_f32_16x16x32_bf16 v[24:27], v[192:195], a[168:171], v[24:27]\n"
    "s_waitcnt lgkmcnt(8)\n"
    "v_mfma_f32_16x16x32_bf16 v[24:27], v[196:199], a[176:179], v[24:27]\n"
    "buffer_load_dwordx4 v[116:119], v255, s[48:51], s88 offen offset:3072 sc1\n"
    "v_mfma_f32_16x16x32_bf16 v[28:31], v[184:187], a[68:71], v[28:31]\n"
    "ds_read_b128 v[168:171], v233 offset:8192\n"
    "s_nop 3\n"
    "v_mfma_f32_16x16x32_bf16 v[28:31], v[188:191], a[164:167], v[28:31]\n"
    "ds_read_b128 v[172:175], v233 offset:9216\n"
    "v_mfma_f32_16x16x32_bf16 v[28:31], v[192:195], a[172:175], v[28:31]\n"
    "ds_read_b128 v[176:179], v233 offset:10240\n"
    "v_mfma_f32_16x16x32_bf16 v[28:31], v[196:199], a[180:183], v[28:31]\n"
    "s_waitcnt lgkmcnt(10)\n"
    "s_add_u32 s88, s88, 0x1000\n"
    "ds_read_b128 v[180:183], v233 offset:11264\n"
    "s_nop 3\n"
    "v_mfma_f32_16x16x32_bf16 v[32:35], v[136:139], a[64:67], v[32:35]\n"
    "s_waitcnt lgkmcnt(10)\n"
    "buffer_load_dwordx4 v[120:123], v255, s[48:51], s88 offen sc1\n"
    "v_mfma_f32_16x16x32_bf16 v[32:35], v[140:143], a[160:163], v[32:35]\n"
    "s_waitcnt lgkmcnt(9)\n"
    "v_mfma_f32_16x16x32_bf16 v[32:35], v[144:147], a[168:171], v[32:35]\n"
    "s_waitcnt lgkmcnt(8)\n"
    "s_nop 3\n"
    "v_mfma_f32_16x16x32_bf16 v[32:35], v[148:151], a[176:179], v[32:35]\n"
    "v_mfma_f32_16x16x32_bf16 v[36:39], v[136:139], a[68:71], v[36:39]\n"
    "ds_read_b128 v[184:187], v233 offset:12288\n"
    "v_mfma_f32_16x16x32_bf16 v[36:39], v[140:143], a[164:167], v[36:39]\n"
    "ds_read_b128 v[188:191], v233 offset:13312\n"
    "buffer_load_dwordx4 v[124:127], v255, s[48:51], s88 offen offset:1024 sc1\n"
    "s_nop 3\n"
    "v_mfma_f32_16x16x32_bf16 v[36:39], v[144:147], a[172:175], v[36:39]\n"
    "ds_read_b128 v[192:195], v233 offset:14336\n"
    "v_mfma_f32_16x16x32_bf16 v[36:39], v[148:151], a[180:183], v[36:39]\n"
    "s_waitcnt lgkmcnt(10)\n"
    "ds_read_b128 v[196:199], v233 offset:15360\n"
    "v_mfma_f32_16x16x32_bf16 v[40:43], v[152:155], a[64:67], v[40:43]\n"
    "s_waitcnt lgkmcnt(10)\n"
    "s_nop 3\n"
    "v_mfma_f32_16x16x32_bf16 v[40:43], v[156:159], a[160:163], v[40:43]\n"
    "s_waitcnt lgkmcnt(9)\n"
    "v_mfma_f32_16x16x32_bf16 v[40:43], v[160:163], a[168:171], v[40:43]\n"
    "s_waitcnt lgkmcnt(8)\n"
    "v_mfma_f32_16x16x32_bf16 v[40:43], v[164:167], a[176:179], v[40:43]\n"
    "buffer_load_dwordx4 v[128:131], v255, s[48:51], s88 offen offset:2048 sc1\n"
    "s_nop 3\n"
    "v_mfma_f32_16x16x32_bf16 v[44:47], v[152:155], a[68:71], v[44:47]\n"
    "ds_read_b128 v[136:139], v232 offset:16384\n"
    "v_mfma_f32_16x16x32_bf16 v[44:47], v[156:159], a[164:167], v[44:47]\n"
    "ds_read_b128 v[140:143], v232 offset:17408\n"
    "v_mfma_f32_16x16x32_bf16 v[44:47], v[160:163], a[172:175], v[44:47]\n"
    "ds_read_b128 v[144:147], v232 offset:18432\n"
    "s_nop 3\n"
    "v_mfma_f32_16x16x32_bf16 v[44:47], v[164:167], a[180:183], v[44:47]\n"
    "s_waitcnt vmcnt(14) lgkmcnt(10)\n"
    "ds_read_b128 v[148:151], v232 offset:19456\n"
    "v_mfma_f32_16x16x32_bf16 v[0:3], v[168:171], a[184:187], v[0:3]\n"
    "s_waitcnt vmcnt(12) lgkmcnt(10)\n"
    "buffer_load_dwordx4 v[132:135], v255, s[48:51], s88 offen offset:3072 sc1\n"
    "v_mfma_f32_16x16x32_bf16 v[0:3], v[172:175], v[80:83], v[0:3]\n"
    "s_waitcnt vmcnt(11) lgkmcnt(9)\n"
    "s_nop 3\n"
    "v_mfma_f32_16x16x32_bf16 v[0:3], v[176:179], v[88:91], v[0:3]\n"
    "s_waitcnt vmcnt(9) lgkmcnt(8)\n"
    "v_mfma_f32_16x16x32_bf16 v[0:3], v[180:183], v[96:99], v[0:3]\n"
    "v_mfma_f32_16x16x32_bf16 v[4:7], v[168:171], a[188:191], v[4:7]\n"
    "ds_read_b128 v[152:155], v232 offset:20480\n"
    "s_nop 3\n"
    "v_mfma_f32_16x16x32_bf16 v[4:7], v[172:175], v[84:87], v[4:7]\n"
    "s_add_u32 s88, s88, 0x1000\n"
    "ds_read_b128 v[156:159], v232 offset:21504\n"
    "v_mfma_f32_16x16x32_bf16 v[4:7], v[176:179], v[92:95], v[4:7]\n"
    "s_waitcnt vmcnt(8)\n"
    "ds_read_b128 v[160:163], v232 offset:22528\n"
    "v_mfma_f32_16x16x32_bf16 v[4:7], v[180:183], v[100:103], v[4:7]\n"
    "s_waitcnt lgkmcnt(10)\n"
    "ds_read_b128 v[164:167], v232 offset:23552\n"
    "s_nop 3\n"
    "v_mfma_f32_16x16x32_bf16 v[8:11], v[184:187], a[184:187], v[8:11]\n"
    "s_waitcnt lgkmcnt(10)\n"
    "v_mfma_f32_16x16x32_bf16 v[8:11], v[188:191], v[80:83], v[8:11]\n"
    "s_waitcnt lgkmcnt(9)\n"
    "v_mfma_f32_16x16x32_bf16 v[8:11], v[192:195], v[88:91], v[8:11]\n"
    "s_waitcnt lgkmcnt(8)\n"
    "s_nop 3\n"
    "v_mfma_f32_16x16x32_bf16 v[8:11], v[196:199], v[96:99], v[8:11]\n"
    "v_mfma_f32_16x16x32_bf16 v[12:15], v[184:187], a[188:191], v[12:15]\n"
    "s_add_u32 m0, s68, 0x24000\n"
    "ds_read_b128 v[168:171], v233 offset:24576\n"
    "v_mfma_f32_16x16x32_bf16 v[12:15], v[188:191], v[84:87], v[12:15]\n"
    "s_add_u32 s92, s62, 0x28000\n"
    "buffer_load_dwordx4 v255, s[48:51], s92 offen sc1 lds\n"
    "ds_read_b128 v[172:175], v233 offset:25600\n"
    "s_nop 3\n"
    "v_mfma_f32_16x16x32_bf16 v[12:15], v[192:195], v[92:95], v[12:15]\n"
    "ds_read_b128 v[176:179], v233 offset:26624\n"
    "v_mfma_f32_16x16x32_bf16 v[12:15], v[196:199], v[100:103], v[12:15]\n"
    "s_waitcnt lgkmcnt(10)\n"
    "ds_read_b128 v[180:183], v233 offset:27648\n"
    "buffer_load_dwordx4 v255, s[48:51], s92 offen offset:1024 sc1 lds\n"
    "v_mfma_f32_16x16x32_bf16 v[16:19], v[136:139], a[184:187], v[16:19]\n"
    "s_waitcnt lgkmcnt(10)\n"
    "s_nop 3\n"
    "v_mfma_f32_16x16x32_bf16 v[16:19], v[140:143], v[80:83], v[16:19]\n"
    "s_waitcnt lgkmcnt(9)\n"
    "v_mfma_f32_16x16x32_bf16 v[16:19], v[144:147], v[88:91], v[16:19]\n"
    "s_waitcnt lgkmcnt(8)\n"
    "v_mfma_f32_16x16x32_bf16 v[16:19], v[148:151], v[96:99], v[16:19]\n"
    "buffer_load_dwordx4 a[148:151], v255, s[48:51], s95 offen offset:3072 sc1\n"
    "s_nop 3\n"
    "v_mfma_f32_16x16x32_bf16 v[20:23], v[136:139], a[188:191], v[20:23]\n"
    "ds_read_b128 v[184:187], v233 offset:28672\n"
    "v_mfma_f32_16x16x32_bf16 v[20:23], v[140:143], v[84:87], v[20:23]\n"
    "ds_read_b128 v[188:191], v233 offset:29696\n"
    "v_mfma_f32_16x16x32_bf16 v[20:23], v[144:147], v[92:95], v[20:23]\n"
    "ds_read_b128 v[192:195], v233 offset:30720\n"
    "s_nop 3\n"
    "v_mfma_f32_16x16x32_bf16 v[20:23], v[148:151], v[100:103], v[20:23]\n"
    "s_waitcnt lgkmcnt(10)\n"
    "s_add_u32 m0, s68, 0x6000\n"
    "ds_read_b128 v[196:199], v233 offset:31744\n"
    "v_mfma_f32_16x16x32_bf16 v[24:27], v[152:155], a[184:187], v[24:27]\n"
    "s_waitcnt lgkmcnt(10)\n"
    "s_add_u32 s92, s62, 0x2a000\n"
    "buffer_load_dwordx4 v255, s[48:51], s92 offen sc1 lds\n"
    "v_mfma_f32_16x16x32_bf16 v[24:27], v[156:159], v[80:83], v[24:27]\n"
    "s_waitcnt lgkmcnt(9)\n"
    "s_nop 3\n"
    "v_mfma_f32_16x16x32_bf16 v[24:27], v[160:163], v[88:91], v[24:27]\n"
    "s_waitcnt lgkmcnt(8)\n"
    "buffer_load_dwordx4 v255, s[48:51], s92 offen offset:1024 sc1 lds\n"
    "v_mfma_f32_16x16x32_bf16 v[24:27], v[164:167], v[96:99], v[24:27]\n"
    "v_mfma_f32_16x16x32_bf16 v[28:31], v[152:155], a[188:191], v[28:31]\n"
    "s_barrier\n"
    "ds_read_b128 v[136:139], v232 offset:8192\n"
    "s_nop 3\n"
    "v_mfma_f32_16x16x32_bf16 v[28:31], v[156:159], v[84:87], v[28:31]\n"
    "s_add_u32 s95, s94, 0x11000\n"
    "ds_read_b128 v[140:143], v232 offset:9216\n"
    "v_mfma_f32_16x16x32_bf16 v[28:31], v[160:163], v[92:95], v[28:31]\n"
    "ds_read_b128 v[144:147], v232 offset:10240\n"
    "buffer_load_dwordx4 a[152:155], v255, s[48:51], s95 offen sc1\n"
    "v_mfma_f32_16x16x32_bf16 v[28:31], v[164:167], v[100:103], v[28:31]\n"
    "s_waitcnt lgkmcnt(10)\n"
    "ds_read_b128 v[148:151], v232 offset:11264\n"
    "s_nop 3\n"
    "v_mfma_f32_16x16x32_bf16 v[32:35], v[168:171], a[184:187], v[32:35]\n"
    "s_waitcnt lgkmcnt(10)\n"
    "v_mfma_f32_16x16x32_bf16 v[32:35], v[172:175], v[80:83], v[32:35]\n"
    "s_waitcnt lgkmcnt(9)\n"
    "v_mfma_f32_16x16x32_bf16 v[32:35], v[176:179], v[88:91], v[32:35]\n"
    "s_waitcnt lgkmcnt(8)\n"
    "s_add_u32 m0, s68, 0x22000\n"
    "v_mfma_f32_16x16x32_bf16 v[32:35], v[180:183], v[96:99], v[32:35]\n"
    "s_add_u32 s92, s62, 0x2c000\n"
    "buffer_load_dwordx4 v255, s[48:51], s92 offen sc1 lds\n"
    "v_mfma_f32_16x16x32_bf16 v[36:39], v[168:171], a[188:191], v[36:39]\n"
    "ds_read_b128 v[152:155], v232 offset:12288\n"
    "v_mfma_f32_16x16x32_bf16 v[36:39], v[172:175], v[84:87], v[36:39]\n"
    "ds_read_b128 v[156:159], v232 offset:13312\n"
    "buffer_load_dwordx4 v255, s[48:51], s92 offen offset:1024 sc1 lds\n"
    "v_mfma_f32_16x16x32_bf16 v[36:39], v[176:179], v[92:95], v[36:39]\n"
    "ds_read_b128 v[160:163], v232 offset:14336\n"
    "v_mfma_f32_16x16x32_bf16 v[36:39], v[180:183], v[100:103], v[36:39]\n"
    "s_waitcnt lgkmcnt(10)\n"
    "ds_read_b128 v[164:167], v232 offset:15360\n"
    "v_mfma_f32_16x16x32_bf16 v[40:43], v[184:187], a[184:187], v[40:43]\n"
    "s_waitcnt lgkmcnt(10)\n"
    "v_mfma_f32_16x16x32_bf16 v[40:43], v[188:191], v[80:83], v[40:43]\n"
    "s_waitcnt lgkmcnt(9)\n"
    "buffer_load_dwordx4 a[156:159], v255, s[48:51], s95 offen offset:1024 sc1\n"
    "v_mfma_f32_16x16x32_bf16 v[40:43], v[192:195], v[88:91], v[40:43]\n"
    "s_waitcnt lgkmcnt(8)\n"
    "v_mfma_f32_16x16x32_bf16 v[40:43], v[196:199], v[96:99], v[40:43]\n"
    "v_mfma_f32_16x16x32_bf16 v[44:47], v[184:187], a[188:191], v[44:47]\n"
    "ds_read_b128 v[168:171], v233 offset:32768\n"
    "v_mfma_f32_16x16x32_bf16 v[44:47], v[188:191], v[84:87], v[44:47]\n"
    "s_add_u32 m0, s68, 0x26000\n"
    "ds_read_b128 v[172:175], v233 offset:33792\n"
    "v_mfma_f32_16x16x32_bf16 v[44:47], v[192:195], v[92:95], v[44:47]\n"
    "s_add_u32 s92, s62, 0x2e000\n"
    "buffer_load_dwordx4 v255, s[48:51], s92 offen sc1 lds\n"
    "ds_read_b128 v[176:179], v233 offset:34816\n"
    "v_mfma_f32_16x16x32_bf16 v[44:47], v[196:199], v[100:103], v[44:47]\n"
    "s_waitcnt vmcnt(17) lgkmcnt(10)\n"
    "ds_read_b128 v[180:183], v233 offset:35840\n"
    "v_mfma_f32_16x16x32_bf16 v[0:3], v[136:139], v[104:107], v[0:3]\n"
    "s_waitcnt vmcnt(15) lgkmcnt(10)\n"
    "v_mfma_f32_16x16x32_bf16 v[0:3], v[140:143], v[112:115], v[0:3]\n"
    "s_waitcnt vmcnt(13) lgkmcnt(9)\n"
    "v_mfma_f32_16x16x32_bf16 v[0:3], v[144:147], v[120:123], v[0:3]\n"
    "s_waitcnt vmcnt(11) lgkmcnt(8)\n"
    "buffer_load_dwordx4 v255, s[48:51], s92 offen offset:1024 sc1 lds\n"
    "v_mfma_f32_16x16x32_bf16 v[0:3], v[148:151], v[128:131], v[0:3]\n"
    "v_mfma_f32_16x16x32_bf16 v[4:7], v[136:139], v[108:111], v[4:7]\n"
    "ds_read_b128 v[184:187], v233 offset:36864\n"
    "v_mfma_f32_16x16x32_bf16 v[4:7], v[140:143], v[116:119], v[4:7]\n"
    "ds_read_b128 v[188:191], v233 offset:37888\n"
    "v_mfma_f32_16x16x32_bf16 v[4:7], v[144:147], v[124:127], v[4:7]\n"
    "s_waitcnt vmcnt(11)\n"
    "ds_read_b128 v[192:195], v233 offset:38912\n"
    "v_mfma_f32_16x16x32_bf16 v[4:7], v[148:151], v[132:135], v[4:7]\n"
    "s_waitcnt lgkmcnt(10)\n"
    "ds_read_b128 v[196:199], v233 offset:39936\n"
    "v_mfma_f32_16x16x32_bf16 v[8:11], v[152:155], v[104:107], v[8:11]\n"
    "s_waitcnt lgkmcnt(10)\n"
    "s_add_u32 m0, s68, 0x10000\n"
    "v_mfma_f32_16x16x32_bf16 v[8:11], v[156:159], v[112:115], v[8:11]\n"
    "s_waitcnt lgkmcnt(9)\n"
    "s_add_u32 s92, s62, 0x30000\n"
    "buffer_load_dwordx4 v255, s[48:51], s92 offen sc1 lds\n"
    "v_mfma_f32_16x16x32_bf16 v[8:11], v[160:163], v[120:123], v[8:11]\n"
    "s_waitcnt lgkmcnt(8)\n"
    "v_mfma_f32_16x16x32_bf16 v[8:11], v[164:167], v[128:131], v[8:11]\n"
    "v_mfma_f32_16x16x32_bf16 v[12:15], v[152:155], v[108:111], v[12:15]\n"
    "ds_read_b128 v[136:139], v233 offset:40960\n"
    "v_mfma_f32_16x16x32_bf16 v[12:15], v[156:159], v[116:119], v[12:15]\n"
    "ds_read_b128 v[140:143], v233 offset:41984\n"
    "buffer_load_dwordx4 v255, s[48:51], s92 offen offset:1024 sc1 lds\n"
    "v_mfma_f32_16x16x32_bf16 v[12:15], v[160:163], v[124:127], v[12:15]\n"
    "ds_read_b128 v[144:147], v233 offset:43008\n"
    "v_mfma_f32_16x16x32_bf16 v[12:15], v[164:167], v[132:135], v[12:15]\n"
    "s_waitcnt lgkmcnt(10)\n"
    "ds_read_b128 v[148:151], v233 offset:44032\n"
    "v_mfma_f32_16x16x32_bf16 v[16:19], v[168:171], v[104:107], v[16:19]\n"
    "s_waitcnt lgkmcnt(10)\n"
    "v_mfma_f32_16x16x32_bf16 v[16:19], v[172:175], v[112:115], v[16:19]\n"
    "s_waitcnt lgkmcnt(9)\n"
    "v_mfma_f32_16x16x32_bf16 v[16:19], v[176:179], v[120:123], v[16:19]\n"
    "s_waitcnt lgkmcnt(8)\n"
    "v_mfma_f32_16x16x32_bf16 v[16:19], v[180:183], v[128:131], v[16:19]\n"
    "s_add_u32 m0, s68, 0x14000\n"
    "v_mfma_f32_16x16x32_bf16 v[20:23], v[168:171], v[108:111], v[20:23]\n"
    "s_add_u32 s92, s62, 0x32000\n"
    "buffer_load_dwordx4 v255, s[48:51], s92 offen sc1 lds\n"
    "ds_read_b128 v[152:155], v233 offset:45056\n"
    "v_mfma_f32_16x16x32_bf16 v[20:23], v[172:175], v[116:119], v[20:23]\n"
    "ds_read_b128 v[156:159], v233 offset:46080\n"
    "v_mfma_f32_16x16x32_bf16 v[20:23], v[176:179], v[124:127], v[20:23]\n"
    "ds_read_b128 v[160:163], v233 offset:47104\n"
    "v_mfma_f32_16x16x32_bf16 v[20:23], v[180:183], v[132:135], v[20:23]\n"
    "s_waitcnt lgkmcnt(10)\n"
    "ds_read_b128 v[164:167], v233 offset:48128\n"
    "v_mfma_f32_16x16x32_bf16 v[24:27], v[184:187], v[104:107], v[24:27]\n"
    "s_waitcnt lgkmcnt(10)\n"
    "buffer_load_dwordx4 v255, s[48:51], s92 offen offset:1024 sc1 lds\n"
    "v_mfma_f32_16x16x32_bf16 v[24:27], v[188:191], v[112:115], v[24:27]\n"
    "s_waitcnt lgkmcnt(9)\n"
    "v_mfma_f32_16x16x32_bf16 v[24:27], v[192:195], v[120:123], v[24:27]\n"
    "s_waitcnt lgkmcnt(8)\n"
    "v_mfma_f32_16x16x32_bf16 v[24:27], v[196:199], v[128:131], v[24:27]\n"
    "v_mfma_f32_16x16x32_bf16 v[28:31], v[184:187], v[108:111], v[28:31]\n"
    "ds_read_b128 v[168:171], v232 offset:32768\n"
    "v_mfma_f32_16x16x32_bf16 v[28:31], v[188:191], v[116:119], v[28:31]\n"
    "ds_read_b128 v[172:175], v232 offset:33792\n"
    "v_mfma_f32_16x16x32_bf16 v[28:31], v[192:195], v[124:127], v[28:31]\n"
    "s_waitcnt lgkmcnt(9)\n"
    "s_add_u32 m0, s68, 0\n"
    "ds_read_b128 v[176:179], v232 offset:34816\n"
    "v_mfma_f32_16x16x32_bf16 v[28:31], v[196:199], v[132:135], v[28:31]\n"
    "s_add_u32 s92, s62, 0x34000\n"
    "buffer_load_dwordx4 v255, s[48:51], s92 offen sc1 lds\n"
    "ds_read_b128 v[180:183], v232 offset:35840\n"
    "v_mfma_f32_16x16x32_bf16 v[32:35], v[136:139], v[104:107], v[32:35]\n"
    "s_waitcnt lgkmcnt(10)\n"
    "v_mfma_f32_16x16x32_bf16 v[32:35], v[140:143], v[112:115], v[32:35]\n"
    "s_waitcnt lgkmcnt(9)\n"
    "v_mfma_f32_16x16x32_bf16 v[32:35], v[144:147], v[120:123], v[32:35]\n"
    "s_waitcnt lgkmcnt(8)\n"
    "v_mfma_f32_16x16x32_bf16 v[32:35], v[148:151], v[128:131], v[32:35]\n"
    "buffer_load_dwordx4 v255, s[48:51], s92 offen offset:1024 sc1 lds\n"
    "v_mfma_f32_16x16x32_bf16 v[36:39], v[136:139], v[108:111], v[36:39]\n"
    "ds_read_b128 v[184:187], v232 offset:36864\n"
    "v_mfma_f32_16x16x32_bf16 v[36:39], v[140:143], v[116:119], v[36:39]\n"
    "ds_read_b128 v[188:191], v232 offset:37888\n"
    "v_mfma_f32_16x16x32_bf16 v[36:39], v[144:147], v[124:127], v[36:39]\n"
    "ds_read_b128 v[192:195], v232 offset:38912\n"
    "v_mfma_f32_16x16x32_bf16 v[36:39], v[148:151], v[132:135], v[36:39]\n"
    "s_waitcnt lgkmcnt(10)\n"
    "ds_read_b128 v[196:199], v232 offset:39936\n"
    "v_mfma_f32_16x16x32_bf16 v[40:43], v[152:155], v[104:107], v[40:43]\n"
    "s_waitcnt lgkmcnt(10)\n"
    "v_mfma_f32_16x16x32_bf16 v[40:43], v[156:159], v[112:115], v[40:43]\n"
    "s_waitcnt lgkmcnt(9)\n"
    "s_add_u32 m0, s68, 0xc000\n"
    "v_mfma_f32_16x16x32_bf16 v[40:43], v[160:163], v[120:123], v[40:43]\n"
    "s_waitcnt lgkmcnt(8)\n"
    "s_add_u32 s92, s62, 0x36000\n"
    "buffer_load_dwordx4 v255, s[48:51], s92 offen sc1 lds\n"
    "v_mfma_f32_16x16x32_bf16 v[40:43], v[164:167], v[128:131], v[40:43]\n"
    "v_mfma_f32_16x16x32_bf16 v[44:47], v[152:155], v[108:111], v[44:47]\n"
    "s_barrier\n"
    "ds_read_b128 v[136:139], v232 offset:40960\n"
    "v_mfma_f32_16x16x32_bf16 v[44:47], v[156:159], v[116:119], v[44:47]\n"
    "ds_read_b128 v[140:143], v232 offset:41984\n"
    "v_mfma_f32_16x16x32_bf16 v[44:47], v[160:163], v[124:127], v[44:47]\n"
    "ds_read_b128 v[144:147], v232 offset:43008\n"
    "buffer_load_dwordx4 v255, s[48:51], s92 offen offset:1024 sc1 lds\n"
    "v_mfma_f32_16x16x32_bf16 v[44:47], v[164:167], v[132:135], v[44:47]\n"
    "s_waitcnt lgkmcnt(10)\n"
    "ds_read_b128 v[148:151], v232 offset:44032\n"
    "v_mfma_f32_16x16x32_bf16 v[48:51], v[168:171], a[0:3], 0\n"
    "s_waitcnt lgkmcnt(10)\n"
    "v_mfma_f32_16x16x32_bf16 v[48:51], v[172:175], a[8:11], v[48:51]\n"
    "s_waitcnt lgkmcnt(9)\n"
    "v_mfma_f32_16x16x32_bf16 v[48:51], v[176:179], a[16:19], v[48:51]\n"
    "s_waitcnt lgkmcnt(8)\n"
    "v_mfma_f32_16x16x32_bf16 v[48:51], v[180:183], a[24:27], v[48:51]\n"
    "v_max_f32_e32 v0, 0, v0\n"
    "v_max_f32_e32 v1, 0, v1\n"
    "v_mfma_f32_16x16x32_bf16 v[52:55], v[168:171], a[4:7], 0\n"
    "s_add_u32 m0, s68, 0xe000\n"
    "ds_read_b128 v[152:155], v232 offset:45056\n"
    "v_max_f32_e64 v2, 0, v2\n"
    "v_mfma_f32_16x16x32_bf16 v[52:55], v[172:175], a[12:15], v[52:55]\n"
    "s_add_u32 s92, s62, 0x38000\n"
    "buffer_load_dwordx4 v255, s[48:51], s92 offen sc1 lds\n"
    "v_max_f32_e64 v3, 0, v3\n"
    "v_mfma_f32_16x16x32_bf16 v[52:55], v[176:179], a[20:23], v[52:55]\n"
    "ds_read_b128 v[156:159], v232 offset:46080\n"
    "v_mul_f32_e64 v0, v0, v8\n"
    "v_mfma_f32_16x16x32_bf16 v[52:55], v[180:183], a[28:31], v[52:55]\n"
    "s_waitcnt lgkmcnt(9)\n"
    "ds_read_b128 v[160:163], v232 offset:47104\n"
    "v_mul_f32_e32 v1, v1, v9\n"
    "v_mfma_f32_16x16x32_bf16 v[56:59], v[184:187], a[0:3], 0\n"
    "s_waitcnt lgkmcnt(9)\n"
    "ds_read_b128 v[164:167], v232 offset:48128\n"
    "v_mul_f32_e32 v2, v2, v10\n"
    "v_mfma_f32_16x16x32_bf16 v[56:59], v[188:191], a[8:11], v[56:59]\n"
    "s_waitcnt lgkmcnt(9)\n"
    "v_mul_f32_e32 v3, v3, v11\n"
    "v_cvt_pk_bf16_f32 v200, v0, v1\n"
    "v_mfma_f32_16x16x32_bf16 v[56:59], v[192:195], a[16:19], v[56:59]\n"
    "s_waitcnt lgkmcnt(8)\n"
    "v_cvt_pk_bf16_f32 v201, v2, v3\n"
    "v_max_f32_e32 v16, 0, v16\n"
    "v_mfma_f32_16x16x32_bf16 v[56:59], v[196:199], a[24:27], v[56:59]\n"
    "buffer_load_dwordx4 v255, s[48:51], s92 offen offset:1024 sc1 lds\n"
    "v_max_f32_e64 v17, 0, v17\n"
    "v_mfma_f32_16x16x32_bf16 v[60:63], v[184:187], a[4:7], 0\n"
    "v_max_f32_e32 v18, 0, v18\n"
    "v_max_f32_e32 v19, 0, v19\n"
    "v_mfma_f32_16x16x32_bf16 v[60:63], v[188:191], a[12:15], v[60:63]\n"
    "ds_read_b128 v[168:171], v233 offset:49152\n"
    "v_mul_f32_e64 v16, v16, v24\n"
    "v_mfma_f32_16x16x32_bf16 v[60:63], v[192:195], a[20:23], v[60:63]\n"
    "s_waitcnt lgkmcnt(7)\n"
    "ds_read_b128 v[172:175], v233 offset:50176\n"
    "v_mul_f32_e32 v17, v17, v25\n"
    "v_mfma_f32_16x16x32_bf16 v[60:63], v[196:199], a[28:31], v[60:63]\n"
    "s_add_u32 m0, s68, 0x12000\n"
    "v_mul_f32_e32 v18, v18, v26\n"
    "v_mul_f32_e32 v19, v19, v27\n"
    "v_mfma_f32_16x16x32_bf16 v[64:67], v[136:139], a[0:3], 0\n"
    "s_add_u32 s92, s62, 0x3a000\n"
    "ds_read_b128 v[176:179], v233 offset:51200\n"
    "v_cvt_pk_bf16_f32 v202, v16, v17\n"
    "v_mfma_f32_16x16x32_bf16 v[64:67], v[140:143], a[8:11], v[64:67]\n"
    "s_waitcnt lgkmcnt(8)\n"
    "ds_read_b128 v[180:183], v233 offset:52224\n"
    "v_cvt_pk_bf16_f32 v203, v18, v19\n"
    "v_mfma_f32_16x16x32_bf16 v[64:67], v[144:147], a[16:19], v[64:67]\n"
    "s_waitcnt lgkmcnt(8)\n"
    "buffer_load_dwordx4 v255, s[48:51], s92 offen sc1 lds\n"
    "buffer_store_dwordx4 v[200:203], v255, s[48:51], s66 offen sc1\n"
    "v_mfma_f32_16x16x32_bf16 v[64:67], v[148:151], a[24:27], v[64:67]\n"
    "v_max_f32_e32 v32, 0, v32\n"
    "v_max_f32_e32 v33, 0, v33\n"
    "v_mfma_f32_16x16x32_bf16 v[68:71], v[136:139], a[4:7], 0\n"
    "v_max_f32_e32 v34, 0, v34\n"
    "v_max_f32_e32 v35, 0, v35\n"
    "v_mfma_f32_16x16x32_bf16 v[68:71], v[140:143], a[12:15], v[68:71]\n"
    "v_mul_f32_e32 v32, v32, v40\n"
    "v_mul_f32_e32 v33, v33, v41\n"
    "v_mfma_f32_16x16x32_bf16 v[68:71], v[144:147], a[20:23], v[68:71]\n"
    "ds_read_b128 v[184:187], v233 offset:53248\n"
    "v_mul_f32_e64 v34, v34, v42\n"
    "v_mfma_f32_16x16x32_bf16 v[68:71], v[148:151], a[28:31], v[68:71]\n"
    "s_waitcnt lgkmcnt(8)\n"
    "buffer_load_dwordx4 v255, s[48:51], s92 offen offset:1024 sc1 lds\n"
    "v_mul_f32_e32 v35, v35, v43\n"
    "v_mfma_f32_16x16x32_bf16 v[72:75], v[152:155], a[0:3], 0\n"
    "s_waitcnt lgkmcnt(7)\n"
    "ds_read_b128 v[188:191], v233 offset:54272\n"
    "v_cvt_pk_bf16_f32 v204, v32, v33\n"
    "v_mfma_f32_16x16x32_bf16 v[72:75], v[156:159], a[8:11], v[72:75]\n"
    "s_waitcnt lgkmcnt(6)\n"
    "ds_read_b128 v[192:195], v233 offset:55296\n"
    "v_cvt_pk_bf16_f32 v205, v34, v35\n"
    "v_mfma_f32_16x16x32_bf16 v[72:75], v[160:163], a[16:19], v[72:75]\n"
    "s_add_u32 m0, s68, 0x4000\n"
    "buffer_store_dwordx2 v[204:205], v248, s[48:51], s91 offen sc1\n"
    "ds_read_b128 v[196:199], v233 offset:56320\n"
    "v_mfma_f32_16x16x32_bf16 v[72:75], v[164:167], a[24:27], v[72:75]\n"
    "s_add_u32 s92, s62, 0x3c000\n"
    "v_max_f32_e32 v4, 0, v4\n"
    "v_max_f32_e32 v5, 0, v5\n"
    "v_mfma_f32_16x16x32_bf16 v[76:79], v[152:155], a[4:7], 0\n"
    "v_max_f32_e32 v6, 0, v6\n"
    "v_max_f32_e32 v7, 0, v7\n"
    "v_mfma_f32_16x16x32_bf16 v[76:79], v[156:159], a[12:15], v[76:79]\n"
    "buffer_load_dwordx4 v255, s[48:51], s92 offen sc1 lds\n"
    "v_mul_f32_e64 v4, v4, v12\n"
    "v_mfma_f32_16x16x32_bf16 v[76:79], v[160:163], a[20:23], v[76:79]\n"
    "v_mul_f32_e32 v5, v5, v13\n"
    "v_mul_f32_e32 v6, v6, v14\n"
    "v_mfma_f32_16x16x32_bf16 v[76:79], v[164:167], a[28:31], v[76:79]\n"
    "s_waitcnt lgkmcnt(7)\n"
    "ds_read_b128 v[136:139], v233 offset:57344\n"
    "v_mul_f32_e32 v7, v7, v15\n"
    "v_mfma_f32_16x16x32_bf16 v[48:51], v[168:171], a[32:35], v[48:51]\n"
    "s_waitcnt lgkmcnt(7)\n"
    "v_cvt_pk_bf16_f32 v206, v4, v5\n"
    "v_cvt_pk_bf16_f32 v207, v6, v7\n"
    "v_mfma_f32_16x16x32_bf16 v[48:51], v[172:175], a[40:43], v[48:51]\n"
    "s_waitcnt lgkmcnt(6)\n"
    "ds_read_b128 v[140:143], v233 offset:58368\n"
    "v_max_f32_e64 v20, 0, v20\n"
    "v_mfma_f32_16x16x32_bf16 v[48:51], v[176:179], a[48:51], v[48:51]\n"
    "s_waitcnt lgkmcnt(6)\n"
    "ds_read_b128 v[144:147], v233 offset:59392\n"
    "v_max_f32_e32 v21, 0, v21\n"
    "v_mfma_f32_16x16x32_bf16 v[48:51], v[180:183], a[56:59], v[48:51]\n"
    "ds_read_b128 v[148:151], v233 offset:60416\n"
    "v_max_f32_e64 v22, 0, v22\n"
    "v_mfma_f32_16x16x32_bf16 v[52:55], v[168:171], a[36:39], v[52:55]\n"
    "buffer_load_dwordx4 v255, s[48:51], s92 offen offset:1024 sc1 lds\n"
    "v_max_f32_e64 v23, 0, v23\n"
    "v_mfma_f32_16x16x32_bf16 v[52:55], v[172:175], a[44:47], v[52:55]\n"
    "s_add_u32 m0, s68, 0x16000\n"
    "v_mul_f32_e32 v20, v20, v28\n"
    "v_mul_f32_e32 v21, v21, v29\n"
    "v_mfma_f32_16x16x32_bf16 v[52:55], v[176:179], a[52:55], v[52:55]\n"
    "s_add_u32 s92, s62, 0x3e000\n"
    "v_mul_f32_e32 v22, v22, v30\n"
    "v_mul_f32_e32 v23, v23, v31\n"
    "v_mfma_f32_16x16x32_bf16 v[52:55], v[180:183], a[60:63], v[52:55]\n"
    "s_waitcnt lgkmcnt(7)\n"
    "v_cvt_pk_bf16_f32 v208, v20, v21\n"
    "v_cvt_pk_bf16_f32 v209, v22, v23\n"
    "v_mfma_f32_16x16x32_bf16 v[56:59], v[184:187], a[32:35], v[56:59]\n"
    "s_waitcnt lgkmcnt(6)\n"
    "buffer_store_dwordx4 v[206:209], v255, s[48:51], s66 offen offset:1024 sc1\n"
    "ds_read_b128 v[152:155], v233 offset:61440\n"
    "v_mfma_f32_16x16x32_bf16 v[56:59], v[188:191], a[40:43], v[56:59]\n"
    "s_waitcnt lgkmcnt(6)\n"
    "ds_read_b128 v[156:159], v233 offset:62464\n"
    "v_max_f32_e32 v36, 0, v36\n"
    "v_mfma_f32_16x16x32_bf16 v[56:59], v[192:195], a[48:51], v[56:59]\n"
    "s_waitcnt lgkmcnt(6)\n"
    "buffer_load_dwordx4 v255, s[48:51], s92 offen sc1 lds\n"
    "v_max_f32_e32 v37, 0, v37\n"
    "v_mfma_f32_16x16x32_bf16 v[56:59], v[196:199], a[56:59], v[56:59]\n"
    "ds_read_b128 v[160:163], v233 offset:63488\n"
    "v_max_f32_e64 v38, 0, v38\n"
    "v_mfma_f32_16x16x32_bf16 v[60:63], v[184:187], a[36:39], v[60:63]\n"
    "v_max_f32_e32 v39, 0, v39\n"
    "v_mul_f32_e32 v36, v36, v44\n"
    "v_mfma_f32_16x16x32_bf16 v[60:63], v[188:191], a[44:47], v[60:63]\n"
    "ds_read_b128 v[164:167], v233 offset:64512\n"
    "v_mul_f32_e64 v37, v37, v45\n"
    "v_mfma_f32_16x16x32_bf16 v[60:63], v[192:195], a[52:55], v[60:63]\n"
    "v_mul_f32_e32 v38, v38, v46\n"
    "v_mul_f32_e32 v39, v39, v47\n"
    "v_mfma_f32_16x16x32_bf16 v[60:63], v[196:199], a[60:63], v[60:63]\n"
    "s_waitcnt lgkmcnt(7)\n"
    "v_cvt_pk_bf16_f32 v210, v36, v37\n"
    "v_cvt_pk_bf16_f32 v211, v38, v39\n"
    "v_mfma_f32_16x16x32_bf16 v[64:67], v[136:139], a[32:35], v[64:67]\n"
    "s_waitcnt lgkmcnt(4)\n"
    "buffer_load_dwordx4 v255, s[48:51], s92 offen offset:1024 sc1 lds\n"
    "buffer_store_dwordx2 v[210:211], v248, s[48:51], s91 offen offset:512 sc1\n"
    "v_mfma_f32_16x16x32_bf16 v[64:67], v[140:143], a[40:43], v[64:67]\n"
    "s_add_u32 s66, s66, 0x6000\n"
    "ds_read_b128 v[168:171], v234 offset:0\n"
    "ds_read_b128 v[172:175], v234 offset:1024\n"
    "v_mfma_f32_16x16x32_bf16 v[64:67], v[144:147], a[48:51], v[64:67]\n"
    "s_add_u32 m0, s68, 0x2000\n"
    "ds_read_b128 v[176:179], v234 offset:2048\n"
    "ds_read_b128 v[180:183], v234 offset:3072\n"
    "v_mfma_f32_16x16x32_bf16 v[64:67], v[148:151], a[56:59], v[64:67]\n"
    "s_add_u32 s92, s62, 0x40000\n"
    "buffer_load_dwordx4 v255, s[48:51], s92 offen sc1 lds\n"
    "v_mfma_f32_16x16x32_bf16 v[68:71], v[136:139], a[36:39], v[68:71]\n"
    "ds_read_b128 v[184:187], v234 offset:4096\n"
    "v_mfma_f32_16x16x32_bf16 v[68:71], v[140:143], a[44:47], v[68:71]\n"
    "ds_read_b128 v[188:191], v234 offset:5120\n"
    "buffer_load_dwordx4 v255, s[48:51], s92 offen offset:1024 sc1 lds\n"
    "v_mfma_f32_16x16x32_bf16 v[68:71], v[144:147], a[52:55], v[68:71]\n"
    "ds_read_b128 v[192:195], v234 offset:6144\n"
    "v_mfma_f32_16x16x32_bf16 v[68:71], v[148:151], a[60:63], v[68:71]\n"
    "s_waitcnt lgkmcnt(10)\n"
    "ds_read_b128 v[196:199], v234 offset:7168\n"
    "v_mfma_f32_16x16x32_bf16 v[72:75], v[152:155], a[32:35], v[72:75]\n"
    "s_waitcnt lgkmcnt(10)\n"
    "v_mfma_f32_16x16x32_bf16 v[72:75], v[156:159], a[40:43], v[72:75]\n"
    "PG12_%=:\n"
    "s_cbranch_execz PG16_%=\n"
    "s_waitcnt lgkmcnt(9)\n"
    "v_mfma_f32_16x16x32_bf16 v[72:75], v[160:163], a[48:51], v[72:75]\n"
    "s_waitcnt lgkmcnt(8)\n"
    "v_mfma_f32_16x16x32_bf16 v[72:75], v[164:167], a[56:59], v[72:75]\n"
    "s_add_u32 m0, s68, 0x18000\n"
    "v_mfma_f32_16x16x32_bf16 v[76:79], v[152:155], a[36:39], v[76:79]\n"
    "s_waitcnt vmcnt(22)\n"
    "s_add_u32 s92, s62, 0x42000\n"
    "buffer_load_dwordx4 v255, s[48:51], s92 offen sc1 lds\n"
    "v_mfma_f32_16x16x32_bf16 v[76:79], v[156:159], a[44:47], v[76:79]\n"
    "s_barrier\n"
    "s_lshl_b32 s39, s38, 3\n"
    "ds_read_b128 v[136:139], v234 offset:16384\n"
    "v_mfma_f32_16x16x32_bf16 v[76:79], v[160:163], a[52:55], v[76:79]\n"
    "s_add_u32 s39, s39, s86\n"
    "s_mul_i32 s39, s39, 0xa00\n"
    "s_add_u32 s39, s39, 0x1900000\n"
    "v_mfma_f32_16x16x32_bf16 v[76:79], v[164:167], a[60:63], v[76:79]\n"
    "s_waitcnt lgkmcnt(8)\n"
    "buffer_load_dwordx4 v[216:219], v255, s[48:51], s39 offen sc1\n"
    "ds_read_b128 v[140:143], v234 offset:17408\n"
    "v_mfma_f32_16x16x32_bf16 v[48:51], v[168:171], a[64:67], v[48:51]\n"
    "s_waitcnt lgkmcnt(8)\n"
    "ds_read_b128 v[144:147], v234 offset:18432\n"
    "ds_read_b128 v[148:151], v234 offset:19456\n"
    "v_mfma_f32_16x16x32_bf16 v[48:51], v[172:175], a[160:163], v[48:51]\n"
    "s_waitcnt lgkmcnt(9)\n"
    "buffer_load_dwordx4 v255, s[48:51], s92 offen offset:1024 sc1 lds\n"
    "v_mfma_f32_16x16x32_bf16 v[48:51], v[176:179], a[168:171], v[48:51]\n"
    "s_waitcnt lgkmcnt(8)\n"
    "v_mfma_f32_16x16x32_bf16 v[48:51], v[180:183], a[176:179], v[48:51]\n"
    "v_mfma_f32_16x16x32_bf16 v[52:55], v[168:171], a[68:71], v[52:55]\n"
    "ds_read_b128 v[152:155], v234 offset:20480\n"
    "v_mfma_f32_16x16x32_bf16 v[52:55], v[172:175], a[164:167], v[52:55]\n"
    "s_waitcnt lgkmcnt(8)\n"
    "ds_read_b128 v[156:159], v234 offset:21504\n"
    "v_mfma_f32_16x16x32_bf16 v[52:55], v[176:179], a[172:175], v[52:55]\n"
    "s_add_u32 m0, s68, 0x1a000\n"
    "ds_read_b128 v[160:163], v234 offset:22528\n"
    "buffer_load_dwordx4 v[220:223], v255, s[48:51], s39 offen offset:1024 sc1\n"
    "v_mfma_f32_16x16x32_bf16 v[52:55], v[180:183], a[180:183], v[52:55]\n"
    "s_add_u32 s92, s62, 0x44000\n"
    "buffer_load_dwordx4 v255, s[48:51], s92 offen sc1 lds\n"
    "ds_read_b128 v[164:167], v234 offset:23552\n"
    "v_mfma_f32_16x16x32_bf16 v[56:59], v[184:187], a[64:67], v[56:59]\n"
    "s_waitcnt lgkmcnt(10)\n"
    "v_mfma_f32_16x16x32_bf16 v[56:59], v[188:191], a[160:163], v[56:59]\n"
    "s_waitcnt lgkmcnt(9)\n"
    "v_mfma_f32_16x16x32_bf16 v[56:59], v[192:195], a[168:171], v[56:59]\n"
    "s_waitcnt lgkmcnt(8)\n"
    "v_mfma_f32_16x16x32_bf16 v[56:59], v[196:199], a[176:179], v[56:59]\n"
    "buffer_load_dwordx4 v255, s[48:51], s92 offen offset:1024 sc1 lds\n"
    "v_mfma_f32_16x16x32_bf16 v[60:63], v[184:187], a[68:71], v[60:63]\n"
    "ds_read_b128 v[168:171], v232 offset:24576\n"
    "v_mfma_f32_16x16x32_bf16 v[60:63], v[188:191], a[164:167], v[60:63]\n"
    "ds_read_b128 v[172:175], v232 offset:25600\n"
    "v_mfma_f32_16x16x32_bf16 v[60:63], v[192:195], a[172:175], v[60:63]\n"
    "ds_read_b128 v[176:179], v232 offset:26624\n"
    "v_mfma_f32_16x16x32_bf16 v[60:63], v[196:199], a[180:183], v[60:63]\n"
    "s_waitcnt lgkmcnt(10)\n"
    "ds_read_b128 v[180:183], v232 offset:27648\n"
    "v_mfma_f32_16x16x32_bf16 v[64:67], v[136:139], a[64:67], v[64:67]\n"
    "s_waitcnt lgkmcnt(10)\n"
    "buffer_load_dwordx2 v[224:225], v248, s[48:51], s39 offen offset:2048 sc1\n"
    "v_mfma_f32_16x16x32_bf16 v[64:67], v[140:143], a[160:163], v[64:67]\n"
    "s_waitcnt lgkmcnt(9)\n"
    "s_add_u32 m0, s68, 0x8000\n"
    "v_mfma_f32_16x16x32_bf16 v[64:67], v[144:147], a[168:171], v[64:67]\n"
    "s_waitcnt lgkmcnt(8)\n"
    "s_add_u32 s92, s94, 0\n"
    "buffer_load_dwordx4 v255, s[48:51], s92 offen sc1 lds\n"
    "v_mfma_f32_16x16x32_bf16 v[64:67], v[148:151], a[176:179], v[64:67]\n"
    "v_mfma_f32_16x16x32_bf16 v[68:71], v[136:139], a[68:71], v[68:71]\n"
    "ds_read_b128 v[184:187], v232 offset:28672\n"
    "v_mfma_f32_16x16x32_bf16 v[68:71], v[140:143], a[164:167], v[68:71]\n"
    "ds_read_b128 v[188:191], v232 offset:29696\n"
    "v_mfma_f32_16x16x32_bf16 v[68:71], v[144:147], a[172:175], v[68:71]\n"
    "ds_read_b128 v[192:195], v232 offset:30720\n"
    "buffer_load_dwordx4 v255, s[48:51], s92 offen offset:1024 sc1 lds\n"
    "v_mfma_f32_16x16x32_bf16 v[68:71], v[148:151], a[180:183], v[68:71]\n"
    "s_waitcnt lgkmcnt(10)\n"
    "ds_read_b128 v[196:199], v232 offset:31744\n"
    "v_mfma_f32_16x16x32_bf16 v[72:75], v[152:155], a[64:67], v[72:75]\n"
    "s_waitcnt lgkmcnt(10)\n"
    "v_mfma_f32_16x16x32_bf16 v[72:75], v[156:159], a[160:163], v[72:75]\n"
    "s_waitcnt lgkmcnt(9)\n"
    "s_mul_i32 s39, s86, 0x140\n"
    "v_mfma_f32_16x16x32_bf16 v[72:75], v[160:163], a[168:171], v[72:75]\n"
    "s_waitcnt lgkmcnt(8)\n"
    "s_add_u32 s39, s39, 0x15200\n"
    "buffer_load_dwordx4 v[212:215], v255, s[48:51], s39 offen sc1\n"
    "v_mfma_f32_16x16x32_bf16 v[72:75], v[164:167], a[176:179], v[72:75]\n"
    "v_mfma_f32_16x16x32_bf16 v[76:79], v[152:155], a[68:71], v[76:79]\n"
    "s_add_u32 m0, s68, 0xa000\n"
    "ds_read_b128 v[136:139], v234 offset:8192\n"
    "v_mfma_f32_16x16x32_bf16 v[76:79], v[156:159], a[164:167], v[76:79]\n"
    "s_add_u32 s92, s94, 0x800\n"
    "buffer_load_dwordx4 v255, s[48:51], s92 offen sc1 lds\n"
    "ds_read_b128 v[140:143], v234 offset:9216\n"
    "v_mfma_f32_16x16x32_bf16 v[76:79], v[160:163], a[172:175], v[76:79]\n"
    "ds_read_b128 v[144:147], v234 offset:10240\n"
    "v_mfma_f32_16x16x32_bf16 v[76:79], v[164:167], a[180:183], v[76:79]\n"
    "s_waitcnt lgkmcnt(10)\n"
    "ds_read_b128 v[148:151], v234 offset:11264\n"
    "v_mfma_f32_16x16x32_bf16 v[48:51], v[168:171], a[184:187], v[48:51]\n"
    "s_waitcnt lgkmcnt(10)\n"
    "v_mfma_f32_16x16x32_bf16 v[48:51], v[172:175], v[80:83], v[48:51]\n"
    "s_waitcnt lgkmcnt(9)\n"
    "buffer_load_dwordx4 v255, s[48:51], s92 offen offset:1024 sc1 lds\n"
    "v_mfma_f32_16x16x32_bf16 v[48:51], v[176:179], v[88:91], v[48:51]\n"
    "s_waitcnt lgkmcnt(8)\n"
    "v_mfma_f32_16x16x32_bf16 v[48:51], v[180:183], v[96:99], v[48:51]\n"
    "v_mfma_f32_16x16x32_bf16 v[52:55], v[168:171], a[188:191], v[52:55]\n"
    "ds_read_b128 v[152:155], v234 offset:12288\n"
    "v_mfma_f32_16x16x32_bf16 v[52:55], v[172:175], v[84:87], v[52:55]\n"
    "ds_read_b128 v[156:159], v234 offset:13312\n"
    "v_mfma_f32_16x16x32_bf16 v[52:55], v[176:179], v[92:95], v[52:55]\n"
    "ds_read_b128 v[160:163], v234 offset:14336\n"
    "v_mfma_f32_16x16x32_bf16 v[52:55], v[180:183], v[100:103], v[52:55]\n"
    "s_waitcnt lgkmcnt(10)\n"
    "s_add_u32 m0, s68, 0x1c000\n"
    "ds_read_b128 v[164:167], v234 offset:15360\n"
    "v_mfma_f32_16x16x32_bf16 v[56:59], v[184:187], a[184:187], v[56:59]\n"
    "s_waitcnt lgkmcnt(10)\n"
    "s_add_u32 s92, s94, 0x1000\n"
    "buffer_load_dwordx4 v255, s[48:51], s92 offen sc1 lds\n"
    "v_mfma_f32_16x16x32_bf16 v[56:59], v[188:191], v[80:83], v[56:59]\n"
    "s_waitcnt lgkmcnt(9)\n"
    "v_mfma_f32_16x16x32_bf16 v[56:59], v[192:195], v[88:91], v[56:59]\n"
    "s_waitcnt lgkmcnt(8)\n"
    "v_mfma_f32_16x16x32_bf16 v[56:59], v[196:199], v[96:99], v[56:59]\n"
    "v_mfma_f32_16x16x32_bf16 v[60:63], v[184:187], a[188:191], v[60:63]\n"
    "ds_read_b128 v[168:171], v234 offset:24576\n"
    "buffer_load_dwordx4 v255, s[48:51], s92 offen offset:1024 sc1 lds\n"
    "v_mfma_f32_16x16x32_bf16 v[60:63], v[188:191], v[84:87], v[60:63]\n"
    "ds_read_b128 v[172:175], v234 offset:25600\n"
    "v_add_u32_e32 v231, 1, v230\n"
    "v_mfma_f32_16x16x32_bf16 v[60:63], v[192:195], v[92:95], v[60:63]\n"
    "s_waitcnt vmcnt(16)\n"
    "s_barrier\n"
    "ds_read_b128 v[176:179], v234 offset:26624\n"
    "v_mfma_f32_16x16x32_bf16 v[60:63], v[196:199], v[100:103], v[60:63]\n"
    "s_waitcnt lgkmcnt(10)\n"
    "ds_read_b128 v[180:183], v234 offset:27648\n"
    "buffer_store_dword v231, v241, s[48:51], s93 offen sc1\n"
    "v_mfma_f32_16x16x32_bf16 v[64:67], v[136:139], a[184:187], v[64:67]\n"
    "s_waitcnt lgkmcnt(10)\n"
    "v_mfma_f32_16x16x32_bf16 v[64:67], v[140:143], v[80:83], v[64:67]\n"
    "s_waitcnt lgkmcnt(9)\n"
    "v_mfma_f32_16x16x32_bf16 v[64:67], v[144:147], v[88:91], v[64:67]\n"
    "s_waitcnt lgkmcnt(8)\n"
    "s_add_u32 m0, s68, 0x1e000\n"
    "v_mfma_f32_16x16x32_bf16 v[64:67], v[148:151], v[96:99], v[64:67]\n"
    "s_add_u32 s92, s94, 0x1800\n"
    "buffer_load_dwordx4 v255, s[48:51], s92 offen sc1 lds\n"
    "v_mfma_f32_16x16x32_bf16 v[68:71], v[136:139], a[188:191], v[68:71]\n"
    "ds_read_b128 v[184:187], v234 offset:28672\n"
    "v_mfma_f32_16x16x32_bf16 v[68:71], v[140:143], v[84:87], v[68:71]\n"
    "ds_read_b128 v[188:191], v234 offset:29696\n"
    "v_mfma_f32_16x16x32_bf16 v[68:71], v[144:147], v[92:95], v[68:71]\n"
    "ds_read_b128 v[192:195], v234 offset:30720\n"
    "v_mfma_f32_16x16x32_bf16 v[68:71], v[148:151], v[100:103], v[68:71]\n"
    "s_waitcnt lgkmcnt(10)\n"
    "ds_read_b128 v[196:199], v234 offset:31744\n"
    "buffer_load_dwordx4 v255, s[48:51], s92 offen offset:1024 sc1 lds\n"
    "v_mfma_f32_16x16x32_bf16 v[72:75], v[152:155], a[184:187], v[72:75]\n"
    "s_waitcnt lgkmcnt(10)\n"
    "v_mfma_f32_16x16x32_bf16 v[72:75], v[156:159], v[80:83], v[72:75]\n"
    "s_waitcnt lgkmcnt(9)\n"
    "v_mfma_f32_16x16x32_bf16 v[72:75], v[160:163], v[88:91], v[72:75]\n"
    "s_waitcnt lgkmcnt(8)\n"
    "v_mfma_f32_16x16x32_bf16 v[72:75], v[164:167], v[96:99], v[72:75]\n"
    "v_mfma_f32_16x16x32_bf16 v[76:79], v[152:155], a[188:191], v[76:79]\n"
    "s_barrier\n"
    "ds_read_b128 v[136:139], v233 offset:0\n"
    "v_mfma_f32_16x16x32_bf16 v[76:79], v[156:159], v[84:87], v[76:79]\n"
    "ds_read_b128 v[140:143], v233 offset:1024\n"
    "v_mfma_f32_16x16x32_bf16 v[76:79], v[160:163], v[92:95], v[76:79]\n"
    "s_waitcnt lgkmcnt(9)\n"
    "s_add_u32 m0, s68, 0x20000\n"
    "ds_read_b128 v[144:147], v233 offset:2048\n"
    "v_mfma_f32_16x16x32_bf16 v[76:79], v[164:167], v[100:103], v[76:79]\n"
    "s_add_u32 s92, s94, 0x2000\n"
    "buffer_load_dwordx4 v255, s[48:51], s92 offen sc1 lds\n"
    "ds_read_b128 v[148:151], v233 offset:3072\n"
    "v_mfma_f32_16x16x32_bf16 v[48:51], v[168:171], v[104:107], v[48:51]\n"
    "s_waitcnt lgkmcnt(10)\n"
    "v_mfma_f32_16x16x32_bf16 v[48:51], v[172:175], v[112:115], v[48:51]\n"
    "s_waitcnt lgkmcnt(9)\n"
    "v_mfma_f32_16x16x32_bf16 v[48:51], v[176:179], v[120:123], v[48:51]\n"
    "s_waitcnt lgkmcnt(8)\n"
    "v_mfma_f32_16x16x32_bf16 v[48:51], v[180:183], v[128:131], v[48:51]\n"
    "buffer_load_dwordx4 v255, s[48:51], s92 offen offset:1024 sc1 lds\n"
    "v_mfma_f32_16x16x32_bf16 v[52:55], v[168:171], v[108:111], v[52:55]\n"
    "ds_read_b128 v[152:155], v233 offset:4096\n"
    "v_mfma_f32_16x16x32_bf16 v[52:55], v[172:175], v[116:119], v[52:55]\n"
    "ds_read_b128 v[156:159], v233 offset:5120\n"
    "v_mfma_f32_16x16x32_bf16 v[52:55], v[176:179], v[124:127], v[52:55]\n"
    "ds_read_b128 v[160:163], v233 offset:6144\n"
    "v_mfma_f32_16x16x32_bf16 v[52:55], v[180:183], v[132:135], v[52:55]\n"
    "s_waitcnt lgkmcnt(10)\n"
    "ds_read_b128 v[164:167], v233 offset:7168\n"
    "v_mfma_f32_16x16x32_bf16 v[56:59], v[184:187], v[104:107], v[56:59]\n"
    "s_waitcnt lgkmcnt(10)\n"
    "v_mfma_f32_16x16x32_bf16 v[56:59], v[188:191], v[112:115], v[56:59]\n"
    "s_waitcnt lgkmcnt(9)\n"
    "s_add_u32 m0, s68, 0x24000\n"
    "v_mfma_f32_16x16x32_bf16 v[56:59], v[192:195], v[120:123], v[56:59]\n"
    "s_waitcnt lgkmcnt(8)\n"
    "s_add_u32 s92, s94, 0x2800\n"
    "buffer_load_dwordx4 v255, s[48:51], s92 offen sc1 lds\n"
    "v_mfma_f32_16x16x32_bf16 v[56:59], v[196:199], v[128:131], v[56:59]\n"
    "v_mfma_f32_16x16x32_bf16 v[60:63], v[184:187], v[108:111], v[60:63]\n"
    "ds_read_b128 v[168:171], v233 offset:16384\n"
    "v_mfma_f32_16x16x32_bf16 v[60:63], v[188:191], v[116:119], v[60:63]\n"
    "ds_read_b128 v[172:175], v233 offset:17408\n"
    "v_mfma_f32_16x16x32_bf16 v[60:63], v[192:195], v[124:127], v[60:63]\n"
    "ds_read_b128 v[176:179], v233 offset:18432\n"
    "buffer_load_dwordx4 v255, s[48:51], s92 offen offset:1024 sc1 lds\n"
    "v_mfma_f32_16x16x32_bf16 v[60:63], v[196:199], v[132:135], v[60:63]\n"
    "s_waitcnt lgkmcnt(10)\n"
    "ds_read_b128 v[180:183], v233 offset:19456\n"
    "v_mfma_f32_16x16x32_bf16 v[64:67], v[136:139], v[104:107], v[64:67]\n"
    "s_waitcnt lgkmcnt(10)\n"
    "v_mfma_f32_16x16x32_bf16 v[64:67], v[140:143], v[112:115], v[64:67]\n"
    "s_waitcnt lgkmcnt(9)\n"
    "v_mfma_f32_16x16x32_bf16 v[64:67], v[144:147], v[120:123], v[64:67]\n"
    "s_waitcnt lgkmcnt(8)\n"
    "v_mfma_f32_16x16x32_bf16 v[64:67], v[148:151], v[128:131], v[64:67]\n"
    "v_mfma_f32_16x16x32_bf16 v[68:71], v[136:139], v[108:111], v[68:71]\n"
    "s_add_u32 m0, s68, 0x6000\n"
    "ds_read_b128 v[184:187], v233 offset:20480\n"
    "v_mfma_f32_16x16x32_bf16 v[68:71], v[140:143], v[116:119], v[68:71]\n"
    "s_add_u32 s92, s94, 0x3000\n"
    "buffer_load_dwordx4 v255, s[48:51], s92 offen sc1 lds\n"
    "ds_read_b128 v[188:191], v233 offset:21504\n"
    "v_mfma_f32_16x16x32_bf16 v[68:71], v[144:147], v[124:127], v[68:71]\n"
    "ds_read_b128 v[192:195], v233 offset:22528\n"
    "v_mfma_f32_16x16x32_bf16 v[68:71], v[148:151], v[132:135], v[68:71]\n"
    "s_waitcnt lgkmcnt(10)\n"
    "ds_read_b128 v[196:199], v233 offset:23552\n"
    "v_mfma_f32_16x16x32_bf16 v[72:75], v[152:155], v[104:107], v[72:75]\n"
    "s_waitcnt lgkmcnt(10)\n"
    "v_mfma_f32_16x16x32_bf16 v[72:75], v[156:159], v[112:115], v[72:75]\n"
    "s_waitcnt lgkmcnt(9)\n"
    "buffer_load_dwordx4 v255, s[48:51], s92 offen offset:1024 sc1 lds\n"
    "v_mfma_f32_16x16x32_bf16 v[72:75], v[160:163], v[120:123], v[72:75]\n"
    "s_waitcnt lgkmcnt(8)\n"
    "v_mfma_f32_16x16x32_bf16 v[72:75], v[164:167], v[128:131], v[72:75]\n"
    "v_mfma_f32_16x16x32_bf16 v[76:79], v[152:155], v[108:111], v[76:79]\n"
    "ds_read_b128 v[136:139], v232 offset:0\n"
    "v_mfma_f32_16x16x32_bf16 v[76:79], v[156:159], v[116:119], v[76:79]\n"
    "ds_read_b128 v[140:143], v232 offset:1024\n"
    "v_mfma_f32_16x16x32_bf16 v[76:79], v[160:163], v[124:127], v[76:79]\n"
    "ds_read_b128 v[144:147], v232 offset:2048\n"
    "v_mfma_f32_16x16x32_bf16 v[76:79], v[164:167], v[132:135], v[76:79]\n"
    "s_waitcnt lgkmcnt(10)\n"
    "s_add_u32 m0, s68, 0x22000\n"
    "ds_read_b128 v[148:151], v232 offset:3072\n"
    "v_mfma_f32_16x16x32_bf16 v[0:3], v[168:171], a[0:3], 0\n"
    "s_waitcnt lgkmcnt(10)\n"
    "s_add_u32 s92, s94, 0x3800\n"
    "buffer_load_dwordx4 v255, s[48:51], s92 offen sc1 lds\n"
    "v_mfma_f32_16x16x32_bf16 v[0:3], v[172:175], a[8:11], v[0:3]\n"
    "s_waitcnt lgkmcnt(9)\n"
    "v_mfma_f32_16x16x32_bf16 v[0:3], v[176:179], a[16:19], v[0:3]\n"
    "s_waitcnt lgkmcnt(8)\n"
    "v_mfma_f32_16x16x32_bf16 v[0:3], v[180:183], a[24:27], v[0:3]\n"
    "v_max_f32_e32 v48, 0, v48\n"
    "v_max_f32_e64 v49, 0, v49\n"
    "v_mfma_f32_16x16x32_bf16 v[4:7], v[168:171], a[4:7], 0\n"
    "ds_read_b128 v[152:155], v232 offset:4096\n"
    "v_max_f32_e64 v50, 0, v50\n"
    "v_mfma_f32_16x16x32_bf16 v[4:7], v[172:175], a[12:15], v[4:7]\n"
    "buffer_load_dwordx4 v255, s[48:51], s92 offen offset:1024 sc1 lds\n"
    "v_max_f32_e64 v51, 0, v51\n"
    "v_mfma_f32_16x16x32_bf16 v[4:7], v[176:179], a[20:23], v[4:7]\n"
    "ds_read_b128 v[156:159], v232 offset:5120\n"
    "v_mul_f32_e64 v48, v48, v56\n"
    "v_mfma_f32_16x16x32_bf16 v[4:7], v[180:183], a[28:31], v[4:7]\n"
    "s_waitcnt lgkmcnt(9)\n"
    "ds_read_b128 v[160:163], v232 offset:6144\n"
    "v_mul_f32_e32 v49, v49, v57\n"
    "v_mfma_f32_16x16x32_bf16 v[8:11], v[184:187], a[0:3], 0\n"
    "s_waitcnt lgkmcnt(9)\n"
    "ds_read_b128 v[164:167], v232 offset:7168\n"
    "v_mul_f32_e32 v50, v50, v58\n"
    "v_mfma_f32_16x16x32_bf16 v[8:11], v[188:191], a[8:11], v[8:11]\n"
    "s_waitcnt lgkmcnt(9)\n"
    "v_mul_f32_e32 v51, v51, v59\n"
    "v_cvt_pk_bf16_f32 v200, v48, v49\n"
    "v_mfma_f32_16x16x32_bf16 v[8:11], v[192:195], a[16:19], v[8:11]\n"
    "s_waitcnt lgkmcnt(8)\n"
    "v_cvt_pk_bf16_f32 v201, v50, v51\n"
    "v_max_f32_e32 v64, 0, v64\n"
    "v_mfma_f32_16x16x32_bf16 v[8:11], v[196:199], a[24:27], v[8:11]\n"
    "v_max_f32_e32 v65, 0, v65\n"
    "v_max_f32_e32 v66, 0, v66\n"
    "v_mfma_f32_16x16x32_bf16 v[12:15], v[184:187], a[4:7], 0\n"
    "v_max_f32_e32 v67, 0, v67\n"
    "v_mul_f32_e32 v64, v64, v72\n"
    "v_mfma_f32_16x16x32_bf16 v[12:15], v[188:191], a[12:15], v[12:15]\n"
    "ds_read_b128 v[168:171], v232 offset:49152\n"
    "v_mul_f32_e64 v65, v65, v73\n"
    "v_mfma_f32_16x16x32_bf16 v[12:15], v[192:195], a[20:23], v[12:15]\n"
    "ds_read_b128 v[172:175], v232 offset:50176\n"
    "v_mul_f32_e64 v66, v66, v74\n"
    "v_mfma_f32_16x16x32_bf16 v[12:15], v[196:199], a[28:31], v[12:15]\n"
    "s_waitcnt lgkmcnt(9)\n"
    "ds_read_b128 v[176:179], v232 offset:51200\n"
    "v_mul_f32_e32 v67, v67, v75\n"
    "v_mfma_f32_16x16x32_bf16 v[16:19], v[136:139], a[0:3], 0\n"
    "s_waitcnt lgkmcnt(9)\n"
    "v_cvt_pk_bf16_f32 v202, v64, v65\n"
    "v_cvt_pk_bf16_f32 v203, v66, v67\n"
    "v_mfma_f32_16x16x32_bf16 v[16:19], v[140:143], a[8:11], v[16:19]\n"
    "s_waitcnt lgkmcnt(8)\n"
    "ds_read_b128 v[180:183], v232 offset:52224\n"
    "buffer_store_dwordx4 v[200:203], v255, s[48:51], s66 offen sc1\n"
    "v_mfma_f32_16x16x32_bf16 v[16:19], v[144:147], a[16:19], v[16:19]\n"
    "s_waitcnt lgkmcnt(8)\n"
    "v_max_f32_e32 v52, 0, v52\n"
    "v_max_f32_e64 v53, 0, v53\n"
    "v_mfma_f32_16x16x32_bf16 v[16:19], v[148:151], a[24:27], v[16:19]\n"
    "v_max_f32_e32 v54, 0, v54\n"
    "v_max_f32_e32 v55, 0, v55\n"
    "v_mfma_f32_16x16x32_bf16 v[20:23], v[136:139], a[4:7], 0\n"
    "v_mul_f32_e32 v52, v52, v60\n"
    "v_mul_f32_e32 v53, v53, v61\n"
    "v_mfma_f32_16x16x32_bf16 v[20:23], v[140:143], a[12:15], v[20:23]\n"
    "ds_read_b128 v[184:187], v232 offset:53248\n"
    "v_mul_f32_e64 v54, v54, v62\n"
    "v_mfma_f32_16x16x32_bf16 v[20:23], v[144:147], a[20:23], v[20:23]\n"
    "ds_read_b128 v[188:191], v232 offset:54272\n"
    "v_mul_f32_e64 v55, v55, v63\n"
    "v_mfma_f32_16x16x32_bf16 v[20:23], v[148:151], a[28:31], v[20:23]\n"
    "s_waitcnt lgkmcnt(9)\n"
    "v_cvt_pk_bf16_f32 v206, v52, v53\n"
    "v_cvt_pk_bf16_f32 v207, v54, v55\n"
    "v_mfma_f32_16x16x32_bf16 v[24:27], v[152:155], a[0:3], 0\n"
    "s_waitcnt lgkmcnt(8)\n"
    "ds_read_b128 v[192:195], v232 offset:55296\n"
    "v_max_f32_e64 v68, 0, v68\n"
    "v_mfma_f32_16x16x32_bf16 v[24:27], v[156:159], a[8:11], v[24:27]\n"
    "s_waitcnt lgkmcnt(8)\n"
    "ds_read_b128 v[196:199], v232 offset:56320\n"
    "v_max_f32_e32 v69, 0, v69\n"
    "v_mfma_f32_16x16x32_bf16 v[24:27], v[160:163], a[16:19], v[24:27]\n"
    "s_waitcnt lgkmcnt(8)\n"
    "v_max_f32_e32 v70, 0, v70\n"
    "v_max_f32_e64 v71, 0, v71\n"
    "v_mfma_f32_16x16x32_bf16 v[24:27], v[164:167], a[24:27], v[24:27]\n"
    "v_mul_f32_e32 v68, v68, v76\n"
    "v_mul_f32_e32 v69, v69, v77\n"
    "v_mfma_f32_16x16x32_bf16 v[28:31], v[152:155], a[4:7], 0\n"
    "s_barrier\n"
    "v_mul_f32_e32 v70, v70, v78\n"
    "v_mul_f32_e64 v71, v71, v79\n"
    "v_mfma_f32_16x16x32_bf16 v[28:31], v[156:159], a[12:15], v[28:31]\n"
    "s_waitcnt lgkmcnt(7)\n"
    "ds_read_b128 v[136:139], v232 offset:57344\n"
    "v_cvt_pk_bf16_f32 v208, v68, v69\n"
    "v_mfma_f32_16x16x32_bf16 v[28:31], v[160:163], a[20:23], v[28:31]\n"
    "s_add_u32 m0, s68, 0x26000\n"
    "ds_read_b128 v[140:143], v232 offset:58368\n"
    "v_cvt_pk_bf16_f32 v209, v70, v71\n"
    "v_mfma_f32_16x16x32_bf16 v[28:31], v[164:167], a[28:31], v[28:31]\n"
    "s_add_u32 s92, s94, 0x4000\n"
    "buffer_store_dwordx4 v[206:209], v255, s[48:51], s66 offen offset:1024 sc1\n"
    "ds_read_b128 v[144:147], v232 offset:59392\n"
    "v_mfma_f32_16x16x32_bf16 v[0:3], v[168:171], a[32:35], v[0:3]\n"
    "s_waitcnt lgkmcnt(9)\n"
    "s_add_u32 s66, s66, 0x4000\n"
    "buffer_load_dwordx4 v255, s[48:51], s92 offen sc1 lds\n"
    "ds_read_b128 v[148:151], v232 offset:60416\n"
    "v_mfma_f32_16x16x32_bf16 v[0:3], v[172:175], a[40:43], v[0:3]\n"
    "s_waitcnt lgkmcnt(9)\n"
    "v_mfma_f32_16x16x32_bf16 v[0:3], v[176:179], a[48:51], v[0:3]\n"
    "s_waitcnt lgkmcnt(8)\n"
    "v_mfma_f32_16x16x32_bf16 v[0:3], v[180:183], a[56:59], v[0:3]\n"
    "buffer_load_dwordx4 v255, s[48:51], s92 offen offset:1024 sc1 lds\n"
    "v_mfma_f32_16x16x32_bf16 v[4:7], v[168:171], a[36:39], v[4:7]\n"
    "ds_read_b128 v[152:155], v232 offset:61440\n"
    "v_mfma_f32_16x16x32_bf16 v[4:7], v[172:175], a[44:47], v[4:7]\n"
    "ds_read_b128 v[156:159], v232 offset:62464\n"
    "v_mfma_f32_16x16x32_bf16 v[4:7], v[176:179], a[52:55], v[4:7]\n"
    "ds_read_b128 v[160:163], v232 offset:63488\n"
    "v_mfma_f32_16x16x32_bf16 v[4:7], v[180:183], a[60:63], v[4:7]\n"
    "s_waitcnt lgkmcnt(10)\n"
    "ds_read_b128 v[164:167], v232 offset:64512\n"
    "v_mfma_f32_16x16x32_bf16 v[8:11], v[184:187], a[32:35], v[8:11]\n"
    "s_waitcnt lgkmcnt(10)\n"
    "v_mfma_f32_16x16x32_bf16 v[8:11], v[188:191], a[40:43], v[8:11]\n"
    "s_waitcnt lgkmcnt(9)\n"
    "s_add_u32 m0, s68, 0x10000\n"
    "v_mfma_f32_16x16x32_bf16 v[8:11], v[192:195], a[48:51], v[8:11]\n"
    "s_waitcnt lgkmcnt(8)\n"
    "s_add_u32 s92, s94, 0x4800\n"
    "buffer_load_dwordx4 v255, s[48:51], s92 offen sc1 lds\n"
    "v_mfma_f32_16x16x32_bf16 v[8:11], v[196:199], a[56:59], v[8:11]\n"
    "v_mfma_f32_16x16x32_bf16 v[12:15], v[184:187], a[36:39], v[12:15]\n"
    "ds_read_b128 v[168:171], v233 offset:8192\n"
    "v_bfe_u32 v240, v255, 4, 2\n"
    "v_mfma_f32_16x16x32_bf16 v[12:15], v[188:191], a[44:47], v[12:15]\n"
    "ds_read_b128 v[172:175], v233 offset:9216\n"
    "v_lshlrev_b32_e64 v240, 2, v240\n"
    "v_mfma_f32_16x16x32_bf16 v[12:15], v[192:195], a[52:55], v[12:15]\n"
    "ds_read_b128 v[176:179], v233 offset:10240\n"
    "buffer_load_dwordx4 v255, s[48:51], s92 offen offset:1024 sc1 lds\n"
    "v_mfma_f32_16x16x32_bf16 v[12:15], v[196:199], a[60:63], v[12:15]\n"
    "s_waitcnt lgkmcnt(10)\n"
    "buffer_load_dword v229, v240, s[48:51], s63 offen sc1\n"
    "ds_read_b128 v[180:183], v233 offset:11264\n"
    "v_mfma_f32_16x16x32_bf16 v[16:19], v[136:139], a[32:35], v[16:19]\n"
    "s_waitcnt lgkmcnt(10)\n"
    "v_mfma_f32_16x16x32_bf16 v[16:19], v[140:143], a[40:43], v[16:19]\n"
    "s_waitcnt lgkmcnt(9)\n"
    "v_mfma_f32_16x16x32_bf16 v[16:19], v[144:147], a[48:51], v[16:19]\n"
    "s_waitcnt lgkmcnt(8)\n"
    "v_mfma_f32_16x16x32_bf16 v[16:19], v[148:151], a[56:59], v[16:19]\n"
    "v_mfma_f32_16x16x32_bf16 v[20:23], v[136:139], a[36:39], v[20:23]\n"
    "s_add_u32 m0, s68, 0x14000\n"
    "ds_read_b128 v[184:187], v233 offset:12288\n"
    "v_mfma_f32_16x16x32_bf16 v[20:23], v[140:143], a[44:47], v[20:23]\n"
    "s_add_u32 s92, s94, 0x5000\n"
    "buffer_load_dwordx4 v255, s[48:51], s92 offen sc1 lds\n"
    "ds_read_b128 v[188:191], v233 offset:13312\n"
    "v_mfma_f32_16x16x32_bf16 v[20:23], v[144:147], a[52:55], v[20:23]\n"
    "ds_read_b128 v[192:195], v233 offset:14336\n"
    "v_mfma_f32_16x16x32_bf16 v[20:23], v[148:151], a[60:63], v[20:23]\n"
    "s_waitcnt lgkmcnt(10)\n"
    "ds_read_b128 v[196:199], v233 offset:15360\n"
    "v_mfma_f32_16x16x32_bf16 v[24:27], v[152:155], a[32:35], v[24:27]\n"
    "s_waitcnt lgkmcnt(10)\n"
    "v_mfma_f32_16x16x32_bf16 v[24:27], v[156:159], a[40:43], v[24:27]\n"
    "s_waitcnt lgkmcnt(9)\n"
    "buffer_load_dwordx4 v255, s[48:51], s92 offen offset:1024 sc1 lds\n"
    "v_mfma_f32_16x16x32_bf16 v[24:27], v[160:163], a[48:51], v[24:27]\n"
    "s_waitcnt lgkmcnt(8)\n"
    "v_mfma_f32_16x16x32_bf16 v[24:27], v[164:167], a[56:59], v[24:27]\n"
    "v_mfma_f32_16x16x32_bf16 v[28:31], v[152:155], a[36:39], v[28:31]\n"
    "ds_read_b128 v[136:139], v232 offset:16384\n"
    "v_mfma_f32_16x16x32_bf16 v[28:31], v[156:159], a[44:47], v[28:31]\n"
    "ds_read_b128 v[140:143], v232 offset:17408\n"
    "v_mfma_f32_16x16x32_bf16 v[28:31], v[160:163], a[52:55], v[28:31]\n"
    "ds_read_b128 v[144:147], v232 offset:18432\n"
    "v_mfma_f32_16x16x32_bf16 v[28:31], v[164:167], a[60:63], v[28:31]\n"
    "s_waitcnt lgkmcnt(10)\n"
    "s_add_u32 m0, s68, 0\n"
    "ds_read_b128 v[148:151], v232 offset:19456\n"
    "v_mfma_f32_16x16x32_bf16 v[0:3], v[168:171], a[64:67], v[0:3]\n"
    "s_waitcnt lgkmcnt(10)\n"
    "s_add_u32 s92, s94, 0x5800\n"
    "buffer_load_dwordx4 v255, s[48:51], s92 offen sc1 lds\n"
    "v_mfma_f32_16x16x32_bf16 v[0:3], v[172:175], a[160:163], v[0:3]\n"
    "s_waitcnt lgkmcnt(9)\n"
    "v_mfma_f32_16x16x32_bf16 v[0:3], v[176:179], a[168:171], v[0:3]\n"
    "s_waitcnt lgkmcnt(8)\n"
    "v_mfma_f32_16x16x32_bf16 v[0:3], v[180:183], a[176:179], v[0:3]\n"
    "v_mfma_f32_16x16x32_bf16 v[4:7], v[168:171], a[68:71], v[4:7]\n"
    "ds_read_b128 v[152:155], v232 offset:20480\n"
    "buffer_load_dwordx4 v255, s[48:51], s92 offen offset:1024 sc1 lds\n"
    "v_mfma_f32_16x16x32_bf16 v[4:7], v[172:175], a[164:167], v[4:7]\n"
    "ds_read_b128 v[156:159], v232 offset:21504\n"
    "v_mfma_f32_16x16x32_bf16 v[4:7], v[176:179], a[172:175], v[4:7]\n"
    "ds_read_b128 v[160:163], v232 offset:22528\n"
    "v_mfma_f32_16x16x32_bf16 v[4:7], v[180:183], a[180:183], v[4:7]\n"
    "s_waitcnt lgkmcnt(10)\n"
    "ds_read_b128 v[164:167], v232 offset:23552\n"
    "v_mfma_f32_16x16x32_bf16 v[8:11], v[184:187], a[64:67], v[8:11]\n"
    "s_waitcnt lgkmcnt(10)\n"
    "v_mfma_f32_16x16x32_bf16 v[8:11], v[188:191], a[160:163], v[8:11]\n"
    "s_waitcnt vmcnt(4) lgkmcnt(9)\n"
    "v_sub_u32_e32 v229, v229, v230\n"
    "v_subrev_u32_e32 v229, 1, v229\n"
    "v_mfma_f32_16x16x32_bf16 v[8:11], v[192:195], a[168:171], v[8:11]\n"
    "s_waitcnt lgkmcnt(8)\n"
    "v_cmp_gt_u32_e32 vcc, 3, v229\n"
    "v_mfma_f32_16x16x32_bf16 v[8:11], v[196:199], a[176:179], v[8:11]\n"
    "v_mfma_f32_16x16x32_bf16 v[12:15], v[184:187], a[68:71], v[12:15]\n"
    "s_cmp_eq_u64 vcc, -1\n"
    "s_cbranch_scc1 POK8_%=\n"
    "s_mov_b32 s61, 20000\n"
    "PSP8_%=:\n"
    "s_sleep 4\n"
    "buffer_load_dword v229, v240, s[48:51], s63 offen sc1\n"
    "s_waitcnt vmcnt(0)\n"
    "v_sub_u32_e32 v229, v229, v230\n"
    "v_subrev_u32_e32 v229, 1, v229\n"
    "v_cmp_gt_u32_e32 vcc, 3, v229\n"
    "s_nop 1\n"
    "s_cmp_eq_u64 vcc, -1\n"
    "s_cbranch_scc1 POK8_%=\n"
    "s_sub_u32 s61, s61, 1\n"
    "s_cmp_lg_u32 s61, 0\n"
    "s_cbranch_scc1 PSP8_%=\n"
    "POK8_%=:\n"
    "ds_read_b128 v[168:171], v233 offset:24576\n"
    "buffer_load_dwordx4 a[0:3], v244, s[48:51], s64 offen sc1\n"
    "v_mfma_f32_16x16x32_bf16 v[12:15], v[188:191], a[164:167], v[12:15]\n"
    "ds_read_b128 v[172:175], v233 offset:25600\n"
    "v_mfma_f32_16x16x32_bf16 v[12:15], v[192:195], a[172:175], v[12:15]\n"
    "ds_read_b128 v[176:179], v233 offset:26624\n"
    "v_mfma_f32_16x16x32_bf16 v[12:15], v[196:199], a[180:183], v[12:15]\n"
    "s_waitcnt lgkmcnt(10)\n"
    "ds_read_b128 v[180:183], v233 offset:27648\n"
    "v_mfma_f32_16x16x32_bf16 v[16:19], v[136:139], a[64:67], v[16:19]\n"
    "s_waitcnt lgkmcnt(10)\n"
    "v_mfma_f32_16x16x32_bf16 v[16:19], v[140:143], a[160:163], v[16:19]\n"
    "s_waitcnt lgkmcnt(9)\n"
    "buffer_load_dwordx4 a[4:7], v245, s[48:51], s64 offen sc1\n"
    "v_mfma_f32_16x16x32_bf16 v[16:19], v[144:147], a[168:171], v[16:19]\n"
    "s_waitcnt lgkmcnt(8)\n"
    "v_mfma_f32_16x16x32_bf16 v[16:19], v[148:151], a[176:179], v[16:19]\n"
    "v_mfma_f32_16x16x32_bf16 v[20:23], v[136:139], a[68:71], v[20:23]\n"
    "ds_read_b128 v[184:187], v233 offset:28672\n"
    "v_mfma_f32_16x16x32_bf16 v[20:23], v[140:143], a[164:167], v[20:23]\n"
    "ds_read_b128 v[188:191], v233 offset:29696\n"
    "v_mfma_f32_16x16x32_bf16 v[20:23], v[144:147], a[172:175], v[20:23]\n"
    "ds_read_b128 v[192:195], v233 offset:30720\n"
    "buffer_load_dwordx4 a[8:11], v246, s[48:51], s64 offen sc1\n"
    "v_mfma_f32_16x16x32_bf16 v[20:23], v[148:151], a[180:183], v[20:23]\n"
    "s_waitcnt lgkmcnt(10)\n"
    "ds_read_b128 v[196:199], v233 offset:31744\n"
    "v_mfma_f32_16x16x32_bf16 v[24:27], v[152:155], a[64:67], v[24:27]\n"
    "s_waitcnt lgkmcnt(10)\n"
    "v_mfma_f32_16x16x32_bf16 v[24:27], v[156:159], a[160:163], v[24:27]\n"
    "s_waitcnt lgkmcnt(9)\n"
    "v_mfma_f32_16x16x32_bf16 v[24:27], v[160:163], a[168:171], v[24:27]\n"
    "s_waitcnt lgkmcnt(8)\n"
    "v_mfma_f32_16x16x32_bf16 v[24:27], v[164:167], a[176:179], v[24:27]\n"
    "buffer_load_dwordx4 a[12:15], v247, s[48:51], s64 offen sc1\n"
    "v_mfma_f32_16x16x32_bf16 v[28:31], v[152:155], a[68:71], v[28:31]\n"
    "s_barrier\n"
    "ds_read_b128 v[136:139], v232 offset:8192\n"
    "v_mfma_f32_16x16x32_bf16 v[28:31], v[156:159], a[164:167], v[28:31]\n"
    "ds_read_b128 v[140:143], v232 offset:9216\n"
    "v_mfma_f32_16x16x32_bf16 v[28:31], v[160:163], a[172:175], v[28:31]\n"
    "s_waitcnt lgkmcnt(9)\n"
    "s_add_u32 m0, s68, 0xc000\n"
    "ds_read_b128 v[144:147], v232 offset:10240\n"
    "v_mfma_f32_16x16x32_bf16 v[28:31], v[164:167], a[180:183], v[28:31]\n"
    "s_add_u32 s92, s94, 0x6000\n"
    "buffer_load_dwordx4 v255, s[48:51], s92 offen sc1 lds\n"
    "ds_read_b128 v[148:151], v232 offset:11264\n"
    "v_mfma_f32_16x16x32_bf16 v[0:3], v[168:171], a[184:187], v[0:3]\n"
    "s_waitcnt lgkmcnt(10)\n"
    "s_add_u32 s64, s64, 0x1000\n"
    "buffer_load_dwordx4 a[16:19], v244, s[48:51], s64 offen sc1\n"
    "v_mfma_f32_16x16x32_bf16 v[0:3], v[172:175], v[80:83], v[0:3]\n"
    "s_waitcnt lgkmcnt(9)\n"
    "v_mfma_f32_16x16x32_bf16 v[0:3], v[176:179], v[88:91], v[0:3]\n"
    "s_waitcnt lgkmcnt(8)\n"
    "v_mfma_f32_16x16x32_bf16 v[0:3], v[180:183], v[96:99], v[0:3]\n"
    "buffer_load_dwordx4 v255, s[48:51], s92 offen offset:1024 sc1 lds\n"
    "v_mfma_f32_16x16x32_bf16 v[4:7], v[168:171], a[188:191], v[4:7]\n"
    "ds_read_b128 v[152:155], v232 offset:12288\n"
    "v_mfma_f32_16x16x32_bf16 v[4:7], v[172:175], v[84:87], v[4:7]\n"
    "ds_read_b128 v[156:159], v232 offset:13312\n"
    "buffer_load_dwordx4 a[20:23], v245, s[48:51], s64 offen sc1\n"
    "v_mfma_f32_16x16x32_bf16 v[4:7], v[176:179], v[92:95], v[4:7]\n"
    "ds_read_b128 v[160:163], v232 offset:14336\n"
    "v_mfma_f32_16x16x32_bf16 v[4:7], v[180:183], v[100:103], v[4:7]\n"
    "s_waitcnt lgkmcnt(10)\n"
    "ds_read_b128 v[164:167], v232 offset:15360\n"
    "v_mfma_f32_16x16x32_bf16 v[8:11], v[184:187], a[184:187], v[8:11]\n"
    "s_waitcnt lgkmcnt(10)\n"
    "v_mfma_f32_16x16x32_bf16 v[8:11], v[188:191], v[80:83], v[8:11]\n"
    "s_waitcnt lgkmcnt(8)\n"
    "s_add_u32 m0, s68, 0xe000\n"
    "v_mfma_f32_16x16x32_bf16 v[8:11], v[192:195], v[88:91], v[8:11]\n"
    "s_add_u32 s92, s94, 0x6800\n"
    "buffer_load_dwordx4 v255, s[48:51], s92 offen sc1 lds\n"
    "buffer_load_dwordx4 a[24:27], v246, s[48:51], s64 offen sc1\n"
    "v_mfma_f32_16x16x32_bf16 v[8:11], v[196:199], v[96:99], v[8:11]\n"
    "v_mfma_f32_16x16x32_bf16 v[12:15], v[184:187], a[188:191], v[12:15]\n"
    "ds_read_b128 v[168:171], v233 offset:32768\n"
    "v_mfma_f32_16x16x32_bf16 v[12:15], v[188:191], v[84:87], v[12:15]\n"
    "ds_read_b128 v[172:175], v233 offset:33792\n"
    "v_add_u32_e32 v231, 2, v230\n"
    "v_mfma_f32_16x16x32_bf16 v[12:15], v[192:195], v[92:95], v[12:15]\n"
    "s_barrier\n"
    "ds_read_b128 v[176:179], v233 offset:34816\n"
    "buffer_load_dwordx4 v255, s[48:51], s92 offen offset:1024 sc1 lds\n"
    "v_mfma_f32_16x16x32_bf16 v[12:15], v[196:199], v[100:103], v[12:15]\n"
    "s_waitcnt lgkmcnt(10)\n"
    "ds_read_b128 v[180:183], v233 offset:35840\n"
    "buffer_store_dword v231, v241, s[48:51], s93 offen sc1\n"
    "v_mfma_f32_16x16x32_bf16 v[16:19], v[136:139], a[184:187], v[16:19]\n"
    "s_waitcnt lgkmcnt(10)\n"
    "buffer_load_dwordx4 a[28:31], v247, s[48:51], s64 offen sc1\n"
    "v_mfma_f32_16x16x32_bf16 v[16:19], v[140:143], v[80:83], v[16:19]\n"
    "PG13_%=:\n"
    "s_cbranch_execz PG17_%=\n"
    "s_waitcnt lgkmcnt(9)\n"
    "v_mfma_f32_16x16x32_bf16 v[16:19], v[144:147], v[88:91], v[16:19]\n"
    "s_waitcnt lgkmcnt(8)\n"
    "v_mfma_f32_16x16x32_bf16 v[16:19], v[148:151], v[96:99], v[16:19]\n"
    "s_add_u32 s64, s64, 0x1000\n"
    "v_mfma_f32_16x16x32_bf16 v[20:23], v[136:139], a[188:191], v[20:23]\n"
    "s_add_u32 m0, s68, 0x12000\n"
    "ds_read_b128 v[184:187], v233 offset:36864\n"
    "buffer_load_dwordx4 a[32:35], v244, s[48:51], s64 offen sc1\n"
    "v_mfma_f32_16x16x32_bf16 v[20:23], v[140:143], v[84:87], v[20:23]\n"
    "s_add_u32 s92, s94, 0x7000\n"
    "buffer_load_dwordx4 v255, s[48:51], s92 offen sc1 lds\n"
    "ds_read_b128 v[188:191], v233 offset:37888\n"
    "v_mfma_f32_16x16x32_bf16 v[20:23], v[144:147], v[92:95], v[20:23]\n"
    "ds_read_b128 v[192:195], v233 offset:38912\n"
    "v_mfma_f32_16x16x32_bf16 v[20:23], v[148:151], v[100:103], v[20:23]\n"
    "s_waitcnt lgkmcnt(10)\n"
    "ds_read_b128 v[196:199], v233 offset:39936\n"
    "v_mfma_f32_16x16x32_bf16 v[24:27], v[152:155], a[184:187], v[24:27]\n"
    "s_waitcnt lgkmcnt(10)\n"
    "v_mfma_f32_16x16x32_bf16 v[24:27], v[156:159], v[80:83], v[24:27]\n"
    "s_waitcnt lgkmcnt(9)\n"
    "buffer_load_dwordx4 a[36:39], v245, s[48:51], s64 offen sc1\n"
    "buffer_load_dwordx4 v255, s[48:51], s92 offen offset:1024 sc1 lds\n"
    "v_mfma_f32_16x16x32_bf16 v[24:27], v[160:163], v[88:91], v[24:27]\n"
    "s_waitcnt lgkmcnt(8)\n"
    "v_mfma_f32_16x16x32_bf16 v[24:27], v[164:167], v[96:99], v[24:27]\n"
    "v_mfma_f32_16x16x32_bf16 v[28:31], v[152:155], a[188:191], v[28:31]\n"
    "ds_read_b128 v[136:139], v233 offset:40960\n"
    "v_mfma_f32_16x16x32_bf16 v[28:31], v[156:159], v[84:87], v[28:31]\n"
    "ds_read_b128 v[140:143], v233 offset:41984\n"
    "v_mfma_f32_16x16x32_bf16 v[28:31], v[160:163], v[92:95], v[28:31]\n"
    "ds_read_b128 v[144:147], v233 offset:43008\n"
    "buffer_load_dwordx4 a[40:43], v246, s[48:51], s64 offen sc1\n"
    "v_mfma_f32_16x16x32_bf16 v[28:31], v[164:167], v[100:103], v[28:31]\n"
    "s_waitcnt lgkmcnt(10)\n"
    "s_add_u32 m0, s68, 0x4000\n"
    "ds_read_b128 v[148:151], v233 offset:44032\n"
    "v_mfma_f32_16x16x32_bf16 v[0:3], v[168:171], v[104:107], v[0:3]\n"
    "s_waitcnt lgkmcnt(10)\n"
    "s_add_u32 s92, s94, 0x7800\n"
    "buffer_load_dwordx4 v255, s[48:51], s92 offen sc1 lds\n"
    "v_mfma_f32_16x16x32_bf16 v[0:3], v[172:175], v[112:115], v[0:3]\n"
    "s_waitcnt lgkmcnt(9)\n"
    "v_mfma_f32_16x16x32_bf16 v[0:3], v[176:179], v[120:123], v[0:3]\n"
    "s_waitcnt lgkmcnt(8)\n"
    "v_mfma_f32_16x16x32_bf16 v[0:3], v[180:183], v[128:131], v[0:3]\n"
    "buffer_load_dwordx4 a[44:47], v247, s[48:51], s64 offen sc1\n"
    "v_mfma_f32_16x16x32_bf16 v[4:7], v[168:171], v[108:111], v[4:7]\n"
    "ds_read_b128 v[152:155], v233 offset:45056\n"
    "buffer_load_dwordx4 v255, s[48:51], s92 offen offset:1024 sc1 lds\n"
    "v_mfma_f32_16x16x32_bf16 v[4:7], v[172:175], v[116:119], v[4:7]\n"
    "ds_read_b128 v[156:159], v233 offset:46080\n"
    "v_mfma_f32_16x16x32_bf16 v[4:7], v[176:179], v[124:127], v[4:7]\n"
    "ds_read_b128 v[160:163], v233 offset:47104\n"
    "v_mfma_f32_16x16x32_bf16 v[4:7], v[180:183], v[132:135], v[4:7]\n"
    "s_waitcnt lgkmcnt(10)\n"
    "s_add_u32 s64, s64, 0x1000\n"
    "ds_read_b128 v[164:167], v233 offset:48128\n"
    "v_mfma_f32_16x16x32_bf16 v[8:11], v[184:187], v[104:107], v[8:11]\n"
    "s_waitcnt lgkmcnt(10)\n"
    "buffer_load_dwordx4 a[48:51], v244, s[48:51], s64 offen sc1\n"
    "v_mfma_f32_16x16x32_bf16 v[8:11], v[188:191], v[112:115], v[8:11]\n"
    "s_waitcnt lgkmcnt(9)\n"
    "v_mfma_f32_16x16x32_bf16 v[8:11], v[192:195], v[120:123], v[8:11]\n"
    "s_waitcnt lgkmcnt(8)\n"
    "v_mfma_f32_16x16x32_bf16 v[8:11], v[196:199], v[128:131], v[8:11]\n"
    "v_mfma_f32_16x16x32_bf16 v[12:15], v[184:187], v[108:111], v[12:15]\n"
    "v_mfma_f32_16x16x32_bf16 v[12:15], v[188:191], v[116:119], v[12:15]\n"
    "buffer_load_dwordx4 a[52:55], v245, s[48:51], s64 offen sc1\n"
    "v_mfma_f32_16x16x32_bf16 v[12:15], v[192:195], v[124:127], v[12:15]\n"
    "v_mfma_f32_16x16x32_bf16 v[12:15], v[196:199], v[132:135], v[12:15]\n"
    "s_waitcnt lgkmcnt(7)\n"
    "v_mfma_f32_16x16x32_bf16 v[16:19], v[136:139], v[104:107], v[16:19]\n"
    "s_waitcnt lgkmcnt(6)\n"
    "v_mfma_f32_16x16x32_bf16 v[16:19], v[140:143], v[112:115], v[16:19]\n"
    "s_waitcnt lgkmcnt(5)\n"
    "v_mfma_f32_16x16x32_bf16 v[16:19], v[144:147], v[120:123], v[16:19]\n"
    "s_waitcnt lgkmcnt(4)\n"
    "buffer_load_dwordx4 a[56:59], v246, s[48:51], s64 offen sc1\n"
    "v_mfma_f32_16x16x32_bf16 v[16:19], v[148:151], v[128:131], v[16:19]\n"
    "v_mfma_f32_16x16x32_bf16 v[20:23], v[136:139], v[108:111], v[20:23]\n"
    "v_mfma_f32_16x16x32_bf16 v[20:23], v[140:143], v[116:119], v[20:23]\n"
    "v_mfma_f32_16x16x32_bf16 v[20:23], v[144:147], v[124:127], v[20:23]\n"
    "v_mfma_f32_16x16x32_bf16 v[20:23], v[148:151], v[132:135], v[20:23]\n"
    "s_waitcnt lgkmcnt(3)\n"
    "buffer_load_dwordx4 a[60:63], v247, s[48:51], s64 offen sc1\n"
    "v_mfma_f32_16x16x32_bf16 v[24:27], v[152:155], v[104:107], v[24:27]\n"
    "s_waitcnt lgkmcnt(2)\n"
    "v_mfma_f32_16x16x32_bf16 v[24:27], v[156:159], v[112:115], v[24:27]\n"
    "s_waitcnt lgkmcnt(1)\n"
    "v_mfma_f32_16x16x32_bf16 v[24:27], v[160:163], v[120:123], v[24:27]\n"
    "s_waitcnt lgkmcnt(0)\n"
    "v_mfma_f32_16x16x32_bf16 v[24:27], v[164:167], v[128:131], v[24:27]\n"
    "s_add_u32 s64, s64, 0x1000\n"
    "v_mfma_f32_16x16x32_bf16 v[28:31], v[152:155], v[108:111], v[28:31]\n"
    "v_mfma_f32_16x16x32_bf16 v[28:31], v[156:159], v[116:119], v[28:31]\n"
    "v_mfma_f32_16x16x32_bf16 v[28:31], v[160:163], v[124:127], v[28:31]\n"
    "v_mfma_f32_16x16x32_bf16 v[28:31], v[164:167], v[132:135], v[28:31]\n"
    "v_max_f32_e32 v0, 0, v0\n"
    "v_max_f32_e32 v1, 0, v1\n"
    "v_max_f32_e32 v2, 0, v2\n"
    "v_max_f32_e32 v3, 0, v3\n"
    "v_mul_f32_e32 v0, v0, v8\n"
    "v_mul_f32_e32 v1, v1, v9\n"
    "v_mul_f32_e32 v2, v2, v10\n"
    "v_mul_f32_e64 v3, v3, v11\n"
    "v_cvt_pk_bf16_f32 v200, v0, v1\n"
    "v_cvt_pk_bf16_f32 v201, v2, v3\n"
    "v_max_f32_e32 v16, 0, v16\n"
    "v_max_f32_e32 v17, 0, v17\n"
    "v_max_f32_e32 v18, 0, v18\n"
    "v_max_f32_e32 v19, 0, v19\n"
    "v_mul_f32_e32 v16, v16, v24\n"
    "v_mul_f32_e32 v17, v17, v25\n"
    "v_mul_f32_e32 v18, v18, v26\n"
    "v_mul_f32_e32 v19, v19, v27\n"
    "v_cvt_pk_bf16_f32 v202, v16, v17\n"
    "v_cvt_pk_bf16_f32 v203, v18, v19\n"
    "buffer_store_dwordx4 v[200:203], v255, s[48:51], s66 offen sc1\n"
    "v_max_f32_e32 v4, 0, v4\n"
    "v_max_f32_e32 v5, 0, v5\n"
    "v_max_f32_e32 v6, 0, v6\n"
    "v_max_f32_e32 v7, 0, v7\n"
    "v_mul_f32_e32 v4, v4, v12\n"
    "v_mul_f32_e32 v5, v5, v13\n"
    "v_mul_f32_e32 v6, v6, v14\n"
    "v_mul_f32_e32 v7, v7, v15\n"
    "v_cvt_pk_bf16_f32 v206, v4, v5\n"
    "v_cvt_pk_bf16_f32 v207, v6, v7\n"
    "v_max_f32_e32 v20, 0, v20\n"
    "v_max_f32_e32 v21, 0, v21\n"
    "v_max_f32_e32 v22, 0, v22\n"
    "v_max_f32_e32 v23, 0, v23\n"
    "v_mul_f32_e32 v20, v20, v28\n"
    "v_mul_f32_e32 v21, v21, v29\n"
    "v_mul_f32_e32 v22, v22, v30\n"
    "v_mul_f32_e32 v23, v23, v31\n"
    "v_cvt_pk_bf16_f32 v208, v20, v21\n"
    "v_cvt_pk_bf16_f32 v209, v22, v23\n"
    "buffer_store_dwordx4 v[206:209], v255, s[48:51], s66 offen offset:1024 sc1\n"
    "v_add_u32_e32 v232, s68, v232\n"
    "v_add_u32_e32 v233, s68, v233\n"
    "v_add_u32_e64 v234, s68, v234\n"
    "ds_read_b128 v[32:35], v232 offset:32768\n"
    "ds_read_b128 v[36:39], v232 offset:33792\n"
    "ds_read_b128 v[40:43], v232 offset:40960\n"
    "ds_read_b128 v[44:47], v232 offset:41984\n"
    "ds_read_b128 v[48:51], v233 offset:49152\n"
    "ds_read_b128 v[52:55], v233 offset:50176\n"
    "ds_read_b128 v[56:59], v233 offset:57344\n"
    "ds_read_b128 v[60:63], v233 offset:58368\n"
    "ds_read_b128 v[64:67], v234 offset:0\n"
    "ds_read_b128 v[68:71], v234 offset:1024\n"
    "s_waitcnt vmcnt(26) lgkmcnt(9)\n"
    "v_mfma_f32_16x16x32_bf16 v[136:139], v[32:35], a[0:3], 0\n"
    "s_waitcnt vmcnt(21) lgkmcnt(4)\n"
    "ds_read_b128 v[72:75], v234 offset:16384\n"
    "v_mfma_f32_16x16x32_bf16 v[136:139], v[52:55], a[16:19], v[136:139]\n"
    "v_mfma_f32_16x16x32_bf16 v[140:143], v[36:39], a[0:3], 0\n"
    "s_waitcnt lgkmcnt(4)\n"
    "buffer_load_dwordx2 a[184:185], v249, s[48:51], s64 offen sc1\n"
    "ds_read_b128 v[76:79], v234 offset:17408\n"
    "v_mfma_f32_16x16x32_bf16 v[140:143], v[56:59], a[16:19], v[140:143]\n"
    "v_mfma_f32_16x16x32_bf16 v[144:147], v[40:43], a[0:3], 0\n"
    "s_waitcnt lgkmcnt(4)\n"
    "buffer_load_dwordx2 a[186:187], v249, s[48:51], s64 offen offset:2048 sc1\n"
    "ds_read_b128 a[64:67], v232 offset:24576\n"
    "v_mfma_f32_16x16x32_bf16 v[144:147], v[60:63], a[16:19], v[144:147]\n"
    "v_mfma_f32_16x16x32_bf16 v[148:151], v[44:47], a[0:3], 0\n"
    "s_waitcnt lgkmcnt(4)\n"
    "buffer_load_dwordx2 a[188:189], v250, s[48:51], s64 offen sc1\n"
    "v_mfma_f32_16x16x32_bf16 v[148:151], v[64:67], a[16:19], v[148:151]\n"
    "ds_read_b128 a[68:71], v232 offset:25600\n"
    "v_mfma_f32_16x16x32_bf16 v[152:155], v[48:51], a[0:3], 0\n"
    "s_waitcnt lgkmcnt(4)\n"
    "buffer_load_dwordx2 a[190:191], v250, s[48:51], s64 offen offset:2048 sc1\n"
    "v_mfma_f32_16x16x32_bf16 v[152:155], v[68:71], a[16:19], v[152:155]\n"
    "ds_read_b128 a[160:163], v234 offset:8192\n"
    "v_mfma_f32_16x16x32_bf16 v[156:159], v[32:35], a[4:7], 0\n"
    "s_waitcnt vmcnt(23)\n"
    "buffer_load_dword v229, v240, s[48:51], s63 offen sc1\n"
    "v_mfma_f32_16x16x32_bf16 v[156:159], v[52:55], a[20:23], v[156:159]\n"
    "buffer_load_dwordx2 v[80:81], v251, s[48:51], s64 offen sc1\n"
    "v_mfma_f32_16x16x32_bf16 v[160:163], v[36:39], a[4:7], 0\n"
    "ds_read_b128 a[164:167], v234 offset:9216\n"
    "v_mfma_f32_16x16x32_bf16 v[160:163], v[56:59], a[20:23], v[160:163]\n"
    "buffer_load_dwordx2 v[82:83], v251, s[48:51], s64 offen offset:2048 sc1\n"
    "v_mfma_f32_16x16x32_bf16 v[164:167], v[40:43], a[4:7], 0\n"
    "ds_read_b128 a[168:171], v234 offset:24576\n"
    "v_mfma_f32_16x16x32_bf16 v[164:167], v[60:63], a[20:23], v[164:167]\n"
    "buffer_load_dwordx2 v[84:85], v252, s[48:51], s64 offen sc1\n"
    "v_mfma_f32_16x16x32_bf16 v[168:171], v[44:47], a[4:7], 0\n"
    "ds_read_b128 a[172:175], v234 offset:25600\n"
    "v_mfma_f32_16x16x32_bf16 v[168:171], v[64:67], a[20:23], v[168:171]\n"
    "buffer_load_dwordx2 v[86:87], v252, s[48:51], s64 offen offset:2048 sc1\n"
    "v_mfma_f32_16x16x32_bf16 v[172:175], v[48:51], a[4:7], 0\n"
    "v_mfma_f32_16x16x32_bf16 v[172:175], v[68:71], a[20:23], v[172:175]\n"
    "s_add_u32 s64, s64, 0x1000\n"
    "ds_read_b128 a[176:179], v233 offset:0\n"
    "v_mfma_f32_16x16x32_bf16 v[176:179], v[32:35], a[8:11], 0\n"
    "s_waitcnt vmcnt(26)\n"
    "s_cmp_ge_u32 s36, 2\n"
    "s_cselect_b32 s67, 0x40000000, s64\n"
    "buffer_load_dwordx2 v[88:89], v249, s[48:51], s67 offen sc1\n"
    "v_mfma_f32_16x16x32_bf16 v[176:179], v[52:55], a[24:27], v[176:179]\n"
    "ds_read_b128 a[180:183], v233 offset:1024\n"
    "v_mfma_f32_16x16x32_bf16 v[180:183], v[36:39], a[8:11], 0\n"
    "buffer_load_dwordx2 v[90:91], v249, s[48:51], s67 offen offset:2048 sc1\n"
    "v_mfma_f32_16x16x32_bf16 v[180:183], v[56:59], a[24:27], v[180:183]\n"
    "v_mfma_f32_16x16x32_bf16 v[184:187], v[40:43], a[8:11], 0\n"
    "buffer_load_dwordx2 v[92:93], v250, s[48:51], s67 offen sc1\n"
    "v_mfma_f32_16x16x32_bf16 v[184:187], v[60:63], a[24:27], v[184:187]\n"
    "v_mfma_f32_16x16x32_bf16 v[188:191], v[44:47], a[8:11], 0\n"
    "buffer_load_dwordx2 v[94:95], v250, s[48:51], s67 offen offset:2048 sc1\n"
    "v_mfma_f32_16x16x32_bf16 v[188:191], v[64:67], a[24:27], v[188:191]\n"
    "v_mfma_f32_16x16x32_bf16 v[192:195], v[48:51], a[8:11], 0\n"
    "v_mfma_f32_16x16x32_bf16 v[192:195], v[68:71], a[24:27], v[192:195]\n"
    "buffer_load_dwordx2 v[96:97], v251, s[48:51], s67 offen sc1\n"
    "v_mfma_f32_16x16x32_bf16 v[0:3], v[32:35], a[12:15], 0\n"
    "s_waitcnt vmcnt(28)\n"
    "v_mfma_f32_16x16x32_bf16 v[0:3], v[52:55], a[28:31], v[0:3]\n"
    "buffer_load_dwordx2 v[98:99], v251, s[48:51], s67 offen offset:2048 sc1\n"
    "v_mfma_f32_16x16x32_bf16 v[4:7], v[36:39], a[12:15], 0\n"
    "v_mfma_f32_16x16x32_bf16 v[4:7], v[56:59], a[28:31], v[4:7]\n"
    "buffer_load_dwordx2 v[100:101], v252, s[48:51], s67 offen sc1\n"
    "v_mfma_f32_16x16x32_bf16 v[8:11], v[40:43], a[12:15], 0\n"
    "v_mfma_f32_16x16x32_bf16 v[8:11], v[60:63], a[28:31], v[8:11]\n"
    "buffer_load_dwordx2 v[102:103], v252, s[48:51], s67 offen offset:2048 sc1\n"
    "v_mfma_f32_16x16x32_bf16 v[12:15], v[44:47], a[12:15], 0\n"
    "v_mfma_f32_16x16x32_bf16 v[12:15], v[64:67], a[28:31], v[12:15]\n"
    "s_add_u32 s64, s64, 0x1000\n"
    "v_mfma_f32_16x16x32_bf16 v[16:19], v[48:51], a[12:15], 0\n"
    "v_mfma_f32_16x16x32_bf16 v[16:19], v[68:71], a[28:31], v[16:19]\n"
    "s_waitcnt vmcnt(30) lgkmcnt(9)\n"
    "v_mfma_f32_16x16x32_bf16 v[136:139], v[72:75], a[32:35], v[136:139]\n"
    "s_waitcnt vmcnt(22) lgkmcnt(4)\n"
    "ds_read_b128 v[104:107], v233 offset:16384\n"
    "v_mfma_f32_16x16x32_bf16 v[136:139], a[164:167], a[48:51], v[136:139]\n"
    "v_mfma_f32_16x16x32_bf16 v[140:143], v[76:79], a[32:35], v[140:143]\n"
    "s_waitcnt vmcnt(12) lgkmcnt(4)\n"
    "v_sub_u32_e32 v229, v229, v230\n"
    "v_subrev_u32_e64 v229, 2, v229\n"
    "v_mfma_f32_16x16x32_bf16 v[140:143], a[168:171], a[48:51], v[140:143]\n"
    "s_waitcnt lgkmcnt(3)\n"
    "ds_read_b128 v[108:111], v233 offset:17408\n"
    "v_cmp_gt_u32_e32 vcc, 2, v229\n"
    "v_mfma_f32_16x16x32_bf16 v[144:147], a[64:67], a[32:35], v[144:147]\n"
    "ds_read_b128 v[112:115], v232 offset:0\n"
    "v_mfma_f32_16x16x32_bf16 v[144:147], a[172:175], a[48:51], v[144:147]\n"
    "s_cmp_eq_u64 vcc, -1\n"
    "s_cbranch_scc1 POK9_%=\n"
    "s_mov_b32 s61, 20000\n"
    "PSP9_%=:\n"
    "s_sleep 4\n"
    "buffer_load_dword v229, v240, s[48:51], s63 offen sc1\n"
    "s_waitcnt vmcnt(0)\n"
    "v_sub_u32_e32 v229, v229, v230\n"
    "v_subrev_u32_e32 v229, 2, v229\n"
    "v_cmp_gt_u32_e32 vcc, 2, v229\n"
    "s_nop 1\n"
    "s_cmp_eq_u64 vcc, -1\n"
    "s_cbranch_scc1 POK9_%=\n"
    "s_sub_u32 s61, s61, 1\n"
    "s_cmp_lg_u32 s61, 0\n"
    "s_cbranch_scc1 PSP9_%=\n"
    "POK9_%=:\n"
    "buffer_load_dwordx4 v[24:27], v244, s[48:51], s64 offen sc1\n"
    "v_mfma_f32_16x16x32_bf16 v[148:151], a[68:71], a[32:35], v[148:151]\n"
    "s_waitcnt lgkmcnt(4)\n"
    "buffer_load_dwordx4 v[28:31], v245, s[48:51], s64 offen sc1\n"
    "v_add_u32_e64 v231, 3, v230\n"
    "v_mfma_f32_16x16x32_bf16 v[148:151], a[176:179], a[48:51], v[148:151]\n"
    "s_waitcnt lgkmcnt(0)\n"
    "s_barrier\n"
    "ds_read_b128 v[116:119], v232 offset:1024\n"
    "v_mfma_f32_16x16x32_bf16 v[152:155], a[160:163], a[32:35], v[152:155]\n"
    "buffer_store_dword v231, v241, s[48:51], s93 offen sc1\n"
    "v_mfma_f32_16x16x32_bf16 v[152:155], a[180:183], a[48:51], v[152:155]\n"
    "ds_read_b128 v[120:123], v232 offset:49152\n"
    "v_mfma_f32_16x16x32_bf16 v[156:159], v[72:75], a[36:39], v[156:159]\n"
    "v_mfma_f32_16x16x32_bf16 v[156:159], a[164:167], a[52:55], v[156:159]\n"
    "buffer_load_dwordx4 v[32:35], v246, s[48:51], s64 offen sc1\n"
    "v_mfma_f32_16x16x32_bf16 v[160:163], v[76:79], a[36:39], v[160:163]\n"
    "ds_read_b128 v[124:127], v232 offset:50176\n"
    "v_mfma_f32_16x16x32_bf16 v[160:163], a[168:171], a[52:55], v[160:163]\n"
    "v_mfma_f32_16x16x32_bf16 v[164:167], a[64:67], a[36:39], v[164:167]\n"
    "ds_read_b128 v[128:131], v232 offset:57344\n"
    "v_mfma_f32_16x16x32_bf16 v[164:167], a[172:175], a[52:55], v[164:167]\n"
    "buffer_load_dwordx4 v[36:39], v247, s[48:51], s64 offen sc1\n"
    "v_mfma_f32_16x16x32_bf16 v[168:171], a[68:71], a[36:39], v[168:171]\n"
    "ds_read_b128 v[132:135], v232 offset:58368\n"
    "v_mfma_f32_16x16x32_bf16 v[168:171], a[176:179], a[52:55], v[168:171]\n"
    "v_mfma_f32_16x16x32_bf16 v[172:175], a[160:163], a[36:39], v[172:175]\n"
    "v_mfma_f32_16x16x32_bf16 v[172:175], a[180:183], a[52:55], v[172:175]\n"
    "s_add_u32 s64, s64, 0x1000\n"
    "ds_read_b128 v[196:199], v233 offset:8192\n"
    "v_mfma_f32_16x16x32_bf16 v[176:179], v[72:75], a[40:43], v[176:179]\n"
    "buffer_load_dwordx4 v[40:43], v244, s[48:51], s64 offen sc1\n"
    "v_mfma_f32_16x16x32_bf16 v[176:179], a[164:167], a[56:59], v[176:179]\n"
    "ds_read_b128 v[20:23], v233 offset:9216\n"
    "v_mfma_f32_16x16x32_bf16 v[180:183], v[76:79], a[40:43], v[180:183]\n"
    "v_mfma_f32_16x16x32_bf16 v[180:183], a[168:171], a[56:59], v[180:183]\n"
    "v_mfma_f32_16x16x32_bf16 v[184:187], a[64:67], a[40:43], v[184:187]\n"
    "buffer_load_dwordx4 v[44:47], v245, s[48:51], s64 offen sc1\n"
    "v_mfma_f32_16x16x32_bf16 v[184:187], a[172:175], a[56:59], v[184:187]\n"
    "v_mfma_f32_16x16x32_bf16 v[188:191], a[68:71], a[40:43], v[188:191]\n"
    "v_mfma_f32_16x16x32_bf16 v[188:191], a[176:179], a[56:59], v[188:191]\n"
    "v_mfma_f32_16x16x32_bf16 v[192:195], a[160:163], a[40:43], v[192:195]\n"
    "v_mfma_f32_16x16x32_bf16 v[192:195], a[180:183], a[56:59], v[192:195]\n"
    "buffer_load_dwordx4 v[48:51], v246, s[48:51], s64 offen sc1\n"
    "v_mfma_f32_16x16x32_bf16 v[0:3], v[72:75], a[44:47], v[0:3]\n"
    "v_mfma_f32_16x16x32_bf16 v[0:3], a[164:167], a[60:63], v[0:3]\n"
    "v_mfma_f32_16x16x32_bf16 v[4:7], v[76:79], a[44:47], v[4:7]\n"
    "v_mfma_f32_16x16x32_bf16 v[4:7], a[168:171], a[60:63], v[4:7]\n"
    "buffer_load_dwordx4 a[0:3], v247, s[48:51], s64 offen sc1\n"
    "v_mfma_f32_16x16x32_bf16 v[8:11], a[64:67], a[44:47], v[8:11]\n"
    "v_mfma_f32_16x16x32_bf16 v[8:11], a[172:175], a[60:63], v[8:11]\n"
    "v_mfma_f32_16x16x32_bf16 v[12:15], a[68:71], a[44:47], v[12:15]\n"
    "v_mfma_f32_16x16x32_bf16 v[12:15], a[176:179], a[60:63], v[12:15]\n"
    "s_add_u32 s64, s64, 0x1000\n"
    "v_mfma_f32_16x16x32_bf16 v[16:19], a[160:163], a[44:47], v[16:19]\n"
    "v_mfma_f32_16x16x32_bf16 v[16:19], a[180:183], a[60:63], v[16:19]\n"
    "v_mfma_f32_16x16x32_bf16 v[136:139], v[104:107], a[184:187], v[136:139]\n"
    "s_waitcnt vmcnt(15) lgkmcnt(4)\n"
    "ds_read_b128 a[4:7], v232 offset:16384\n"
    "v_mfma_f32_16x16x32_bf16 v[136:139], v[124:127], v[88:91], v[136:139]\n"
    "v_mfma_f32_16x16x32_bf16 v[140:143], v[108:111], a[184:187], v[140:143]\n"
    "s_waitcnt lgkmcnt(4)\n"
    "buffer_load_dwordx4 a[12:15], v244, s[48:51], s64 offen sc1\n"
    "v_mfma_f32_16x16x32_bf16 v[140:143], v[128:131], v[88:91], v[140:143]\n"
    "v_mfma_f32_16x16x32_bf16 v[144:147], v[112:115], a[184:187], v[144:147]\n"
    "s_waitcnt lgkmcnt(3)\n"
    "v_mfma_f32_16x16x32_bf16 v[144:147], v[132:135], v[88:91], v[144:147]\n"
    "v_mfma_f32_16x16x32_bf16 v[148:151], v[116:119], a[184:187], v[148:151]\n"
    "s_waitcnt lgkmcnt(2)\n"
    "buffer_load_dwordx4 v[52:55], v245, s[48:51], s64 offen sc1\n"
    "v_mfma_f32_16x16x32_bf16 v[148:151], v[196:199], v[88:91], v[148:151]\n"
    "v_mfma_f32_16x16x32_bf16 v[152:155], v[120:123], a[184:187], v[152:155]\n"
    "s_waitcnt lgkmcnt(1)\n"
    "v_mfma_f32_16x16x32_bf16 v[152:155], v[20:23], v[88:91], v[152:155]\n"
    "v_mfma_f32_16x16x32_bf16 v[156:159], v[104:107], a[188:191], v[156:159]\n"
    "s_waitcnt vmcnt(15)\n"
    "v_mfma_f32_16x16x32_bf16 v[156:159], v[124:127], v[92:95], v[156:159]\n"
    "buffer_load_dwordx4 v[56:59], v246, s[48:51], s64 offen sc1\n"
    "v_mfma_f32_16x16x32_bf16 v[160:163], v[108:111], a[188:191], v[160:163]\n"
    "ds_read_b128 a[8:11], v232 offset:17408\n"
    "v_mfma_f32_16x16x32_bf16 v[160:163], v[128:131], v[92:95], v[160:163]\n"
    "v_mfma_f32_16x16x32_bf16 v[164:167], v[112:115], a[188:191], v[164:167]\n"
    "v_mfma_f32_16x16x32_bf16 v[164:167], v[132:135], v[92:95], v[164:167]\n"
    "buffer_load_dwordx4 v[60:63], v247, s[48:51], s64 offen sc1\n"
    "v_mfma_f32_16x16x32_bf16 v[168:171], v[116:119], a[188:191], v[168:171]\n"
    "v_mfma_f32_16x16x32_bf16 v[168:171], v[196:199], v[92:95], v[168:171]\n"
    "v_mfma_f32_16x16x32_bf16 v[172:175], v[120:123], a[188:191], v[172:175]\n"
    "v_mfma_f32_16x16x32_bf16 v[172:175], v[20:23], v[92:95], v[172:175]\n"
    "s_add_u32 s64, s64, 0x1000\n"
    "v_mfma_f32_16x16x32_bf16 v[176:179], v[104:107], v[80:83], v[176:179]\n"
    "s_waitcnt vmcnt(15)\n"
    "buffer_load_dwordx4 v[64:67], v244, s[48:51], s64 offen sc1\n"
    "v_mfma_f32_16x16x32_bf16 v[176:179], v[124:127], v[96:99], v[176:179]\n"
    "v_mfma_f32_16x16x32_bf16 v[180:183], v[108:111], v[80:83], v[180:183]\n"
    "v_mfma_f32_16x16x32_bf16 v[180:183], v[128:131], v[96:99], v[180:183]\n"
    "v_mfma_f32_16x16x32_bf16 v[184:187], v[112:115], v[80:83], v[184:187]\n"
    "buffer_load_dwordx4 v[68:71], v245, s[48:51], s64 offen sc1\n"
    "v_mfma_f32_16x16x32_bf16 v[184:187], v[132:135], v[96:99], v[184:187]\n"
    "v_mfma_f32_16x16x32_bf16 v[188:191], v[116:119], v[80:83], v[188:191]\n"
    "v_mfma_f32_16x16x32_bf16 v[188:191], v[196:199], v[96:99], v[188:191]\n"
    "v_mfma_f32_16x16x32_bf16 v[192:195], v[120:123], v[80:83], v[192:195]\n"
    "v_mfma_f32_16x16x32_bf16 v[192:195], v[20:23], v[96:99], v[192:195]\n"
    "buffer_load_dwordx4 a[16:19], v246, s[48:51], s64 offen sc1\n"
    "v_mfma_f32_16x16x32_bf16 v[0:3], v[104:107], v[84:87], v[0:3]\n"
    "s_waitcnt vmcnt(16)\n"
    "v_mfma_f32_16x16x32_bf16 v[0:3], v[124:127], v[100:103], v[0:3]\n"
    "v_mfma_f32_16x16x32_bf16 v[4:7], v[108:111], v[84:87], v[4:7]\n"
    "v_mfma_f32_16x16x32_bf16 v[4:7], v[128:131], v[100:103], v[4:7]\n"
    "buffer_load_dwordx4 a[20:23], v247, s[48:51], s64 offen sc1\n"
    "v_mfma_f32_16x16x32_bf16 v[8:11], v[112:115], v[84:87], v[8:11]\n"
    "v_mfma_f32_16x16x32_bf16 v[8:11], v[132:135], v[100:103], v[8:11]\n"
    "v_mfma_f32_16x16x32_bf16 v[12:15], v[116:119], v[84:87], v[12:15]\n"
    "v_mfma_f32_16x16x32_bf16 v[12:15], v[196:199], v[100:103], v[12:15]\n"
    "s_add_u32 s64, s64, 0x1000\n"
    "v_mfma_f32_16x16x32_bf16 v[16:19], v[120:123], v[84:87], v[16:19]\n"
    "v_mfma_f32_16x16x32_bf16 v[16:19], v[20:23], v[100:103], v[16:19]\n"
    "s_waitcnt vmcnt(16) lgkmcnt(1)\n"
    "v_mfma_f32_16x16x32_bf16 v[136:139], a[4:7], v[24:27], v[136:139]\n"
    "s_waitcnt vmcnt(11)\n"
    "v_mfma_f32_16x16x32_bf16 v[136:139], a[204:207], v[40:43], v[136:139]\n"
    "s_waitcnt lgkmcnt(0)\n"
    "v_mfma_f32_16x16x32_bf16 v[140:143], a[8:11], v[24:27], v[140:143]\n"
    "buffer_load_dword v229, v240, s[48:51], s63 offen sc1\n"
    "v_mfma_f32_16x16x32_bf16 v[140:143], a[208:211], v[40:43], v[140:143]\n"
    "v_mfma_f32_16x16x32_bf16 v[144:147], a[192:195], v[24:27], v[144:147]\n"
    "v_mfma_f32_16x16x32_bf16 v[144:147], a[212:215], v[40:43], v[144:147]\n"
    "v_mfma_f32_16x16x32_bf16 v[148:151], a[196:199], v[24:27], v[148:151]\n"
    "v_mfma_f32_16x16x32_bf16 v[148:151], a[216:219], v[40:43], v[148:151]\n"
    "v_mfma_f32_16x16x32_bf16 v[152:155], a[200:203], v[24:27], v[152:155]\n"
    "v_mfma_f32_16x16x32_bf16 v[152:155], a[220:223], v[40:43], v[152:155]\n"
    "v_mfma_f32_16x16x32_bf16 v[156:159], a[4:7], v[28:31], v[156:159]\n"
    "s_waitcnt vmcnt(11)\n"
    "v_mfma_f32_16x16x32_bf16 v[156:159], a[204:207], v[44:47], v[156:159]\n"
    "v_mfma_f32_16x16x32_bf16 v[160:163], a[8:11], v[28:31], v[160:163]\n"
    "v_mfma_f32_16x16x32_bf16 v[160:163], a[208:211], v[44:47], v[160:163]\n"
    "v_mfma_f32_16x16x32_bf16 v[164:167], a[192:195], v[28:31], v[164:167]\n"
    "v_mfma_f32_16x16x32_bf16 v[164:167], a[212:215], v[44:47], v[164:167]\n"
    "v_mfma_f32_16x16x32_bf16 v[168:171], a[196:199], v[28:31], v[168:171]\n"
    "v_mfma_f32_16x16x32_bf16 v[168:171], a[216:219], v[44:47], v[168:171]\n"
    "v_mfma_f32_16x16x32_bf16 v[172:175], a[200:203], v[28:31], v[172:175]\n"
    "v_mfma_f32_16x16x32_bf16 v[172:175], a[220:223], v[44:47], v[172:175]\n"
    "v_mfma_f32_16x16x32_bf16 v[176:179], a[4:7], v[32:35], v[176:179]\n"
    "s_waitcnt vmcnt(10)\n"
    "v_mfma_f32_16x16x32_bf16 v[176:179], a[204:207], v[48:51], v[176:179]\n"
    "v_mfma_f32_16x16x32_bf16 v[180:183], a[8:11], v[32:35], v[180:183]\n"
    "v_mfma_f32_16x16x32_bf16 v[180:183], a[208:211], v[48:51], v[180:183]\n"
    "v_mfma_f32_16x16x32_bf16 v[184:187], a[192:195], v[32:35], v[184:187]\n"
    "v_mfma_f32_16x16x32_bf16 v[184:187], a[212:215], v[48:51], v[184:187]\n"
    "v_mfma_f32_16x16x32_bf16 v[188:191], a[196:199], v[32:35], v[188:191]\n"
    "v_mfma_f32_16x16x32_bf16 v[188:191], a[216:219], v[48:51], v[188:191]\n"
    "v_mfma_f32_16x16x32_bf16 v[192:195], a[200:203], v[32:35], v[192:195]\n"
    "v_mfma_f32_16x16x32_bf16 v[192:195], a[220:223], v[48:51], v[192:195]\n"
    "v_mfma_f32_16x16x32_bf16 v[0:3], a[4:7], v[36:39], v[0:3]\n"
    "s_waitcnt vmcnt(9)\n"
    "s_waitcnt lgkmcnt(0)\n"
    "s_barrier\n"
    "v_mfma_f32_16x16x32_bf16 v[0:3], a[204:207], a[0:3], v[0:3]\n"
    "v_mfma_f32_16x16x32_bf16 v[4:7], a[8:11], v[36:39], v[4:7]\n"
    "v_mfma_f32_16x16x32_bf16 v[4:7], a[208:211], a[0:3], v[4:7]\n"
    "v_mfma_f32_16x16x32_bf16 v[8:11], a[192:195], v[36:39], v[8:11]\n"
    "v_mfma_f32_16x16x32_bf16 v[8:11], a[212:215], a[0:3], v[8:11]\n"
    "v_mfma_f32_16x16x32_bf16 v[12:15], a[196:199], v[36:39], v[12:15]\n"
    "v_mfma_f32_16x16x32_bf16 v[12:15], a[216:219], a[0:3], v[12:15]\n"
    "v_mfma_f32_16x16x32_bf16 v[16:19], a[200:203], v[36:39], v[16:19]\n"
    "v_mfma_f32_16x16x32_bf16 v[16:19], a[220:223], a[0:3], v[16:19]\n"
    "s_waitcnt vmcnt(8)\n"
    "v_mfma_f32_16x16x32_bf16 v[136:139], a[224:227], a[12:15], v[136:139]\n"
    "s_waitcnt vmcnt(4)\n"
    "v_mfma_f32_16x16x32_bf16 v[136:139], a[244:247], v[64:67], v[136:139]\n"
    "v_mfma_f32_16x16x32_bf16 v[140:143], a[228:231], a[12:15], v[140:143]\n"
    "s_waitcnt vmcnt(0)\n"
    "s_add_u32 s65, s64, 0x1000\n"
    "v_mfma_f32_16x16x32_bf16 v[140:143], a[248:251], v[64:67], v[140:143]\n"
    "s_add_u32 s66, s64, 0x2000\n"
    "s_add_u32 s67, s64, 0x3000\n"
    "v_sub_u32_e32 v229, v229, v230\n"
    "v_mfma_f32_16x16x32_bf16 v[144:147], a[232:235], a[12:15], v[144:147]\n"
    "s_lshl_b32 s39, s36, 10\n"
    "v_subrev_u32_e32 v229, 3, v229\n"
    "v_cmp_gt_u32_e32 vcc, 1, v229\n"
    "v_mfma_f32_16x16x32_bf16 v[144:147], a[252:255], v[64:67], v[144:147]\n"
    "v_mfma_f32_16x16x32_bf16 v[148:151], a[236:239], a[12:15], v[148:151]\n"
    "s_cmp_eq_u64 vcc, -1\n"
    "s_cbranch_scc1 POK10_%=\n"
    "s_mov_b32 s61, 20000\n"
    "PSP10_%=:\n"
    "s_sleep 4\n"
    "buffer_load_dword v229, v240, s[48:51], s63 offen sc1\n"
    "s_waitcnt vmcnt(0)\n"
    "v_sub_u32_e32 v229, v229, v230\n"
    "v_subrev_u32_e32 v229, 3, v229\n"
    "v_cmp_gt_u32_e32 vcc, 1, v229\n"
    "s_nop 1\n"
    "s_cmp_eq_u64 vcc, -1\n"
    "s_cbranch_scc1 POK10_%=\n"
    "s_sub_u32 s61, s61, 1\n"
    "s_cmp_lg_u32 s61, 0\n"
    "s_cbranch_scc1 PSP10_%=\n"
    "POK10_%=:\n"
    "buffer_load_dwordx4 a[24:27], v244, s[48:51], s64 offen sc1\n"
    "buffer_load_dwordx4 a[28:31], v244, s[48:51], s65 offen sc1\n"
    "v_mfma_f32_16x16x32_bf16 v[148:151], a[72:75], v[64:67], v[148:151]\n"
    "v_add_u32_e64 v228, s39, v228\n"
    "v_mfma_f32_16x16x32_bf16 v[152:155], a[240:243], a[12:15], v[152:155]\n"
    "v_mfma_f32_16x16x32_bf16 v[152:155], a[76:79], v[64:67], v[152:155]\n"
    "buffer_load_dwordx4 v[72:75], v244, s[48:51], s66 offen sc1\n"
    "v_mfma_f32_16x16x32_bf16 v[156:159], a[224:227], v[52:55], v[156:159]\n"
    "ds_write_b128 v247, v[212:215] offset:61440\n"
    "v_mfma_f32_16x16x32_bf16 v[156:159], a[244:247], v[68:71], v[156:159]\n"
    "v_mfma_f32_16x16x32_bf16 v[160:163], a[228:231], v[52:55], v[160:163]\n"
    "buffer_load_dwordx4 v[76:79], v244, s[48:51], s67 offen sc1\n"
    "v_mfma_f32_16x16x32_bf16 v[160:163], a[248:251], v[68:71], v[160:163]\n"
    "ds_read_b128 v[104:107], v228 offset:61440\n"
    "v_mfma_f32_16x16x32_bf16 v[164:167], a[232:235], v[52:55], v[164:167]\n"
    "v_mfma_f32_16x16x32_bf16 v[164:167], a[252:255], v[68:71], v[164:167]\n"
    "v_mfma_f32_16x16x32_bf16 v[168:171], a[236:239], v[52:55], v[168:171]\n"
    "buffer_load_dwordx4 a[64:67], v245, s[48:51], s64 offen sc1\n"
    "v_mfma_f32_16x16x32_bf16 v[168:171], a[72:75], v[68:71], v[168:171]\n"
    "ds_read_b128 v[108:111], v228 offset:61504\n"
    "v_mfma_f32_16x16x32_bf16 v[172:175], a[240:243], v[52:55], v[172:175]\n"
    "v_mfma_f32_16x16x32_bf16 v[172:175], a[76:79], v[68:71], v[172:175]\n"
    "v_mfma_f32_16x16x32_bf16 v[176:179], a[224:227], v[56:59], v[176:179]\n"
    "buffer_load_dwordx4 a[68:71], v245, s[48:51], s65 offen sc1\n"
    "ds_read_b128 v[112:115], v228 offset:61568\n"
    "v_mfma_f32_16x16x32_bf16 v[176:179], a[244:247], a[16:19], v[176:179]\n"
    "v_mfma_f32_16x16x32_bf16 v[180:183], a[228:231], v[56:59], v[180:183]\n"
    "v_mfma_f32_16x16x32_bf16 v[180:183], a[248:251], a[16:19], v[180:183]\n"
    "ds_read_b128 v[116:119], v228 offset:61632\n"
    "buffer_load_dwordx4 a[160:163], v245, s[48:51], s66 offen sc1\n"
    "v_mfma_f32_16x16x32_bf16 v[184:187], a[232:235], v[56:59], v[184:187]\n"
    "v_mfma_f32_16x16x32_bf16 v[184:187], a[252:255], a[16:19], v[184:187]\n"
    "v_mfma_f32_16x16x32_bf16 v[188:191], a[236:239], v[56:59], v[188:191]\n"
    "ds_read_b128 v[120:123], v228 offset:61696\n"
    "v_mfma_f32_16x16x32_bf16 v[188:191], a[72:75], a[16:19], v[188:191]\n"
    "buffer_load_dwordx4 a[32:35], v245, s[48:51], s67 offen sc1\n"
    "v_mfma_f32_16x16x32_bf16 v[192:195], a[240:243], v[56:59], v[192:195]\n"
    "v_mfma_f32_16x16x32_bf16 v[192:195], a[76:79], a[16:19], v[192:195]\n"
    "v_mfma_f32_16x16x32_bf16 v[0:3], a[224:227], v[60:63], v[0:3]\n"
    "buffer_load_dwordx4 a[36:39], v246, s[48:51], s64 offen sc1\n"
    "v_mfma_f32_16x16x32_bf16 v[0:3], a[244:247], a[20:23], v[0:3]\n"
    "v_mfma_f32_16x16x32_bf16 v[4:7], a[228:231], v[60:63], v[4:7]\n"
    "v_mfma_f32_16x16x32_bf16 v[4:7], a[248:251], a[20:23], v[4:7]\n"
    "v_mfma_f32_16x16x32_bf16 v[8:11], a[232:235], v[60:63], v[8:11]\n"
    "buffer_load_dwordx4 a[40:43], v246, s[48:51], s65 offen sc1\n"
    "v_mfma_f32_16x16x32_bf16 v[8:11], a[252:255], a[20:23], v[8:11]\n"
    "v_mfma_f32_16x16x32_bf16 v[12:15], a[236:239], v[60:63], v[12:15]\n"
    "v_mfma_f32_16x16x32_bf16 v[12:15], a[72:75], a[20:23], v[12:15]\n"
    "v_mfma_f32_16x16x32_bf16 v[16:19], a[240:243], v[60:63], v[16:19]\n"
    "v_mfma_f32_16x16x32_bf16 v[16:19], a[76:79], a[20:23], v[16:19]\n"
    "s_waitcnt vmcnt(9)\n"
    "v_mfma_f32_16x16x32_bf16 v[136:139], a[80:83], a[24:27], v[136:139]\n"
    "s_waitcnt vmcnt(8)\n"
    "buffer_load_dwordx4 a[44:47], v246, s[48:51], s66 offen sc1\n"
    "v_mfma_f32_16x16x32_bf16 v[136:139], a[100:103], a[28:31], v[136:139]\n"
    "s_waitcnt vmcnt(8)\n"
    "v_mfma_f32_16x16x32_bf16 v[136:139], a[120:123], v[72:75], v[136:139]\n"
    "s_waitcnt vmcnt(7)\n"
    "v_mfma_f32_16x16x32_bf16 v[136:139], a[140:143], v[76:79], v[136:139]\n"
    "v_mfma_f32_16x16x32_bf16 v[140:143], a[84:87], a[24:27], v[140:143]\n"
    "v_mfma_f32_16x16x32_bf16 v[140:143], a[104:107], a[28:31], v[140:143]\n"
    "buffer_load_dwordx4 a[164:167], v246, s[48:51], s67 offen sc1\n"
    "v_mfma_f32_16x16x32_bf16 v[140:143], a[124:127], v[72:75], v[140:143]\n"
    "v_mfma_f32_16x16x32_bf16 v[140:143], a[144:147], v[76:79], v[140:143]\n"
    "v_mfma_f32_16x16x32_bf16 v[144:147], a[88:91], a[24:27], v[144:147]\n"
    "v_mfma_f32_16x16x32_bf16 v[144:147], a[108:111], a[28:31], v[144:147]\n"
    "s_nop 0\n"
    "ds_write_b128 v244, v[136:139] offset:0\n"
    "v_mfma_f32_16x16x32_bf16 v[144:147], a[128:131], v[72:75], v[144:147]\n"
    "buffer_load_dwordx4 a[168:171], v247, s[48:51], s64 offen sc1\n"
    "v_lshlrev_b32_e32 v80, 16, v216\n"
    "v_mfma_f32_16x16x32_bf16 v[144:147], a[148:151], v[76:79], v[144:147]\n"
    "v_and_b32_e32 v81, 0xffff0000, v216\n"
    "v_mfma_f32_16x16x32_bf16 v[148:151], a[92:95], a[24:27], v[148:151]\n"
    "v_mfma_f32_16x16x32_bf16 v[148:151], a[112:115], a[28:31], v[148:151]\n"
    "ds_write_b128 v244, v[140:143] offset:4096\n"
    "v_lshlrev_b32_e64 v82, 16, v217\n"
    "v_mfma_f32_16x16x32_bf16 v[148:151], a[132:135], v[72:75], v[148:151]\n"
    "v_mfma_f32_16x16x32_bf16 v[148:151], a[152:155], v[76:79], v[148:151]\n"
    "buffer_load_dwordx4 a[172:175], v247, s[48:51], s65 offen sc1\n"
    "v_and_b32_e32 v83, 0xffff0000, v217\n"
    "v_mfma_f32_16x16x32_bf16 v[152:155], a[96:99], a[24:27], v[152:155]\n"
    "v_mfma_f32_16x16x32_bf16 v[152:155], a[116:119], a[28:31], v[152:155]\n"
    "ds_write_b128 v244, v[144:147] offset:8192\n"
    "v_lshlrev_b32_e64 v84, 16, v218\n"
    "v_mfma_f32_16x16x32_bf16 v[152:155], a[136:139], v[72:75], v[152:155]\n"
    "v_mfma_f32_16x16x32_bf16 v[152:155], a[156:159], v[76:79], v[152:155]\n"
    "s_waitcnt vmcnt(9)\n"
    "v_and_b32_e32 v85, 0xffff0000, v218\n"
    "v_mfma_f32_16x16x32_bf16 v[156:159], a[80:83], a[64:67], v[156:159]\n"
    "s_waitcnt vmcnt(8)\n"
    "buffer_load_dwordx4 a[176:179], v247, s[48:51], s66 offen sc1\n"
    "v_mfma_f32_16x16x32_bf16 v[156:159], a[100:103], a[68:71], v[156:159]\n"
    "s_waitcnt vmcnt(8)\n"
    "ds_write_b128 v244, v[148:151] offset:12288\n"
    "v_lshlrev_b32_e32 v86, 16, v219\n"
    "v_mfma_f32_16x16x32_bf16 v[156:159], a[120:123], a[160:163], v[156:159]\n"
    "s_waitcnt vmcnt(7)\n"
    "v_mfma_f32_16x16x32_bf16 v[156:159], a[140:143], a[32:35], v[156:159]\n"
    "v_and_b32_e32 v87, 0xffff0000, v219\n"
    "v_mfma_f32_16x16x32_bf16 v[160:163], a[84:87], a[64:67], v[160:163]\n"
    "v_mfma_f32_16x16x32_bf16 v[160:163], a[104:107], a[68:71], v[160:163]\n"
    "ds_write_b128 v244, v[152:155] offset:16384\n"
    "buffer_load_dwordx4 a[180:183], v247, s[48:51], s67 offen sc1\n"
    "v_mfma_f32_16x16x32_bf16 v[160:163], a[124:127], a[160:163], v[160:163]\n"
    "v_lshlrev_b32_e32 v124, 16, v220\n"
    "v_mfma_f32_16x16x32_bf16 v[160:163], a[144:147], a[32:35], v[160:163]\n"
    "v_and_b32_e32 v125, 0xffff0000, v220\n"
    "v_mfma_f32_16x16x32_bf16 v[164:167], a[88:91], a[64:67], v[164:167]\n"
    "v_mfma_f32_16x16x32_bf16 v[164:167], a[108:111], a[68:71], v[164:167]\n"
    "ds_write_b128 v245, v[156:159] offset:20480\n"
    "v_lshlrev_b32_e64 v126, 16, v221\n"
    "v_mfma_f32_16x16x32_bf16 v[164:167], a[128:131], a[160:163], v[164:167]\n"
    "PG14_%=:\n"
    "s_cbranch_execz PG18_%=\n"
    "v_mfma_f32_16x16x32_bf16 v[164:167], a[148:151], a[32:35], v[164:167]\n"
    "v_and_b32_e32 v127, 0xffff0000, v221\n"
    "v_mfma_f32_16x16x32_bf16 v[168:171], a[92:95], a[64:67], v[168:171]\n"
    "v_mfma_f32_16x16x32_bf16 v[168:171], a[112:115], a[68:71], v[168:171]\n"
    "ds_write_b128 v245, v[160:163] offset:24576\n"
    "v_lshlrev_b32_e64 v128, 16, v222\n"
    "v_mfma_f32_16x16x32_bf16 v[168:171], a[132:135], a[160:163], v[168:171]\n"
    "v_mfma_f32_16x16x32_bf16 v[168:171], a[152:155], a[32:35], v[168:171]\n"
    "v_and_b32_e32 v129, 0xffff0000, v222\n"
    "v_mfma_f32_16x16x32_bf16 v[172:175], a[96:99], a[64:67], v[172:175]\n"
    "v_lshlrev_b32_e64 v130, 16, v223\n"
    "v_mfma_f32_16x16x32_bf16 v[172:175], a[116:119], a[68:71], v[172:175]\n"
    "ds_write_b128 v245, v[164:167] offset:28672\n"
    "v_mfma_f32_16x16x32_bf16 v[172:175], a[136:139], a[160:163], v[172:175]\n"
    "v_and_b32_e32 v131, 0xffff0000, v223\n"
    "v_mfma_f32_16x16x32_bf16 v[172:175], a[156:159], a[32:35], v[172:175]\n"
    "s_waitcnt vmcnt(7)\n"
    "v_mfma_f32_16x16x32_bf16 v[176:179], a[80:83], a[36:39], v[176:179]\n"
    "s_waitcnt vmcnt(6)\n"
    "v_lshlrev_b32_e64 v132, 16, v224\n"
    "v_mfma_f32_16x16x32_bf16 v[176:179], a[100:103], a[40:43], v[176:179]\n"
    "s_waitcnt vmcnt(5)\n"
    "ds_write_b128 v245, v[168:171] offset:32768\n"
    "v_mfma_f32_16x16x32_bf16 v[176:179], a[120:123], a[44:47], v[176:179]\n"
    "s_waitcnt vmcnt(4)\n"
    "v_and_b32_e32 v133, 0xffff0000, v224\n"
    "v_mfma_f32_16x16x32_bf16 v[176:179], a[140:143], a[164:167], v[176:179]\n"
    "v_mfma_f32_16x16x32_bf16 v[180:183], a[84:87], a[36:39], v[180:183]\n"
    "v_lshlrev_b32_e64 v134, 16, v225\n"
    "v_mfma_f32_16x16x32_bf16 v[180:183], a[104:107], a[40:43], v[180:183]\n"
    "ds_write_b128 v245, v[172:175] offset:36864\n"
    "v_mfma_f32_16x16x32_bf16 v[180:183], a[124:127], a[44:47], v[180:183]\n"
    "v_and_b32_e32 v135, 0xffff0000, v225\n"
    "v_mfma_f32_16x16x32_bf16 v[180:183], a[144:147], a[164:167], v[180:183]\n"
    "v_mfma_f32_16x16x32_bf16 v[184:187], a[88:91], a[36:39], v[184:187]\n"
    "s_mul_i32 s38, s38, 0x5000\n"
    "v_mfma_f32_16x16x32_bf16 v[184:187], a[108:111], a[40:43], v[184:187]\n"
    "ds_write_b128 v246, v[176:179] offset:40960\n"
    "v_mfma_f32_16x16x32_bf16 v[184:187], a[128:131], a[44:47], v[184:187]\n"
    "s_mul_i32 s39, s86, 0xa0\n"
    "v_mfma_f32_16x16x32_bf16 v[184:187], a[148:151], a[164:167], v[184:187]\n"
    "v_mfma_f32_16x16x32_bf16 v[188:191], a[92:95], a[36:39], v[188:191]\n"
    "s_add_u32 s38, s38, s39\n"
    "v_mfma_f32_16x16x32_bf16 v[188:191], a[112:115], a[40:43], v[188:191]\n"
    "ds_write_b128 v246, v[180:183] offset:45056\n"
    "v_mfma_f32_16x16x32_bf16 v[188:191], a[132:135], a[44:47], v[188:191]\n"
    "v_bfe_u32 v235, v255, 8, 1\n"
    "v_mfma_f32_16x16x32_bf16 v[188:191], a[152:155], a[164:167], v[188:191]\n"
    "v_mfma_f32_16x16x32_bf16 v[192:195], a[96:99], a[36:39], v[192:195]\n"
    "v_mul_u32_u24_e32 v235, 24, v235\n"
    "v_mfma_f32_16x16x32_bf16 v[192:195], a[116:119], a[40:43], v[192:195]\n"
    "ds_write_b128 v246, v[184:187] offset:49152\n"
    "v_mfma_f32_16x16x32_bf16 v[192:195], a[136:139], a[44:47], v[192:195]\n"
    "v_add_u32_e64 v235, v235, v227\n"
    "v_mfma_f32_16x16x32_bf16 v[192:195], a[156:159], a[164:167], v[192:195]\n"
    "s_waitcnt vmcnt(3)\n"
    "v_mfma_f32_16x16x32_bf16 v[0:3], a[80:83], a[168:171], v[0:3]\n"
    "s_waitcnt vmcnt(2)\n"
    "v_mfma_f32_16x16x32_bf16 v[0:3], a[100:103], a[172:175], v[0:3]\n"
    "s_waitcnt vmcnt(1)\n"
    "ds_write_b128 v246, v[188:191] offset:53248\n"
    "v_mfma_f32_16x16x32_bf16 v[0:3], a[120:123], a[176:179], v[0:3]\n"
    "s_waitcnt vmcnt(0)\n"
    "v_mfma_f32_16x16x32_bf16 v[0:3], a[140:143], a[180:183], v[0:3]\n"
    "v_mfma_f32_16x16x32_bf16 v[4:7], a[84:87], a[168:171], v[4:7]\n"
    "v_mfma_f32_16x16x32_bf16 v[4:7], a[104:107], a[172:175], v[4:7]\n"
    "ds_write_b128 v246, v[192:195] offset:57344\n"
    "v_mfma_f32_16x16x32_bf16 v[4:7], a[124:127], a[176:179], v[4:7]\n"
    "v_mfma_f32_16x16x32_bf16 v[4:7], a[144:147], a[180:183], v[4:7]\n"
    "v_mfma_f32_16x16x32_bf16 v[8:11], a[88:91], a[168:171], v[8:11]\n"
    "s_waitcnt lgkmcnt(0)\n"
    "s_barrier\n"
    "v_mfma_f32_16x16x32_bf16 v[8:11], a[108:111], a[172:175], v[8:11]\n"
    "ds_read_b128 v[196:199], v247 offset:0\n"
    "ds_read_b128 v[100:103], v247 offset:20480\n"
    "v_mfma_f32_16x16x32_bf16 v[8:11], a[128:131], a[176:179], v[8:11]\n"
    "ds_read_b128 v[40:43], v247 offset:40960\n"
    "ds_read_b128 v[20:23], v247 offset:4096\n"
    "v_mfma_f32_16x16x32_bf16 v[8:11], a[148:151], a[180:183], v[8:11]\n"
    "ds_read_b128 v[24:27], v247 offset:24576\n"
    "ds_read_b128 v[44:47], v247 offset:45056\n"
    "v_mfma_f32_16x16x32_bf16 v[12:15], a[92:95], a[168:171], v[12:15]\n"
    "ds_read_b128 v[88:91], v247 offset:8192\n"
    "ds_read_b128 v[28:31], v247 offset:28672\n"
    "v_mfma_f32_16x16x32_bf16 v[12:15], a[112:115], a[172:175], v[12:15]\n"
    "ds_read_b128 v[48:51], v247 offset:49152\n"
    "ds_read_b128 v[92:95], v247 offset:12288\n"
    "v_mfma_f32_16x16x32_bf16 v[12:15], a[132:135], a[176:179], v[12:15]\n"
    "ds_read_b128 v[32:35], v247 offset:32768\n"
    "ds_read_b128 v[52:55], v247 offset:53248\n"
    "v_mfma_f32_16x16x32_bf16 v[12:15], a[152:155], a[180:183], v[12:15]\n"
    "ds_read_b128 v[96:99], v247 offset:16384\n"
    "ds_read_b128 v[36:39], v247 offset:36864\n"
    "v_mfma_f32_16x16x32_bf16 v[16:19], a[96:99], a[168:171], v[16:19]\n"
    "ds_read_b128 v[56:59], v247 offset:57344\n"
    "v_mfma_f32_16x16x32_bf16 v[16:19], a[116:119], a[172:175], v[16:19]\n"
    "v_mfma_f32_16x16x32_bf16 v[16:19], a[136:139], a[176:179], v[16:19]\n"
    "v_mfma_f32_16x16x32_bf16 v[16:19], a[156:159], a[180:183], v[16:19]\n"
    "s_waitcnt lgkmcnt(13)\n"
    "v_pk_add_f32 v[196:197], v[196:197], v[100:101]\n"
    "s_waitcnt lgkmcnt(12)\n"
    "v_pk_add_f32 v[0:1], v[0:1], v[40:41]\n"
    "v_pk_add_f32 v[198:199], v[198:199], v[102:103]\n"
    "v_pk_add_f32 v[2:3], v[2:3], v[42:43]\n"
    "v_pk_add_f32 v[0:1], v[0:1], v[196:197]\n"
    "v_pk_add_f32 v[2:3], v[2:3], v[198:199]\n"
    "v_pk_fma_f32 v[80:81], v[104:105], v[0:1], v[80:81]\n"
    "v_pk_fma_f32 v[82:83], v[106:107], v[2:3], v[82:83]\n"
    "v_cvt_pk_bf16_f32 v80, v80, v81\n"
    "v_cvt_pk_bf16_f32 v81, v82, v83\n"
    "s_waitcnt lgkmcnt(10)\n"
    "v_pk_add_f32 v[20:21], v[20:21], v[24:25]\n"
    "s_waitcnt lgkmcnt(9)\n"
    "v_pk_add_f32 v[4:5], v[4:5], v[44:45]\n"
    "v_pk_add_f32 v[22:23], v[22:23], v[26:27]\n"
    "v_pk_add_f32 v[6:7], v[6:7], v[46:47]\n"
    "v_pk_add_f32 v[4:5], v[4:5], v[20:21]\n"
    "v_pk_add_f32 v[6:7], v[6:7], v[22:23]\n"
    "v_pk_fma_f32 v[84:85], v[108:109], v[4:5], v[84:85]\n"
    "v_pk_fma_f32 v[86:87], v[110:111], v[6:7], v[86:87]\n"
    "v_cvt_pk_bf16_f32 v82, v84, v85\n"
    "v_cvt_pk_bf16_f32 v83, v86, v87\n"
    "s_nop 0\n"
    "v_permlane16_swap_b32 v80, v82\n"
    "v_permlane16_swap_b32 v81, v83\n"
    "buffer_store_dwordx4 v[80:83], v235, s[56:59], s38 offen\n"
    "s_waitcnt lgkmcnt(7)\n"
    "v_pk_add_f32 v[88:89], v[88:89], v[28:29]\n"
    "s_waitcnt lgkmcnt(6)\n"
    "v_pk_add_f32 v[8:9], v[8:9], v[48:49]\n"
    "v_pk_add_f32 v[90:91], v[90:91], v[30:31]\n"
    "v_pk_add_f32 v[10:11], v[10:11], v[50:51]\n"
    "v_pk_add_f32 v[8:9], v[8:9], v[88:89]\n"
    "v_pk_add_f32 v[10:11], v[10:11], v[90:91]\n"
    "v_pk_fma_f32 v[124:125], v[112:113], v[8:9], v[124:125]\n"
    "v_pk_fma_f32 v[126:127], v[114:115], v[10:11], v[126:127]\n"
    "v_cvt_pk_bf16_f32 v124, v124, v125\n"
    "v_cvt_pk_bf16_f32 v125, v126, v127\n"
    "s_waitcnt lgkmcnt(4)\n"
    "v_pk_add_f32 v[92:93], v[92:93], v[32:33]\n"
    "s_waitcnt lgkmcnt(3)\n"
    "v_pk_add_f32 v[12:13], v[12:13], v[52:53]\n"
    "v_pk_add_f32 v[94:95], v[94:95], v[34:35]\n"
    "v_pk_add_f32 v[14:15], v[14:15], v[54:55]\n"
    "v_pk_add_f32 v[12:13], v[12:13], v[92:93]\n"
    "v_pk_add_f32 v[14:15], v[14:15], v[94:95]\n"
    "v_pk_fma_f32 v[128:129], v[116:117], v[12:13], v[128:129]\n"
    "v_pk_fma_f32 v[130:131], v[118:119], v[14:15], v[130:131]\n"
    "v_cvt_pk_bf16_f32 v126, v128, v129\n"
    "v_cvt_pk_bf16_f32 v127, v130, v131\n"
    "s_nop 0\n"
    "v_permlane16_swap_b32 v124, v126\n"
    "v_permlane16_swap_b32 v125, v127\n"
    "buffer_store_dwordx4 v[124:127], v235, s[56:59], s38 offen offset:64\n"
    "s_waitcnt lgkmcnt(1)\n"
    "v_pk_add_f32 v[96:97], v[96:97], v[36:37]\n"
    "s_waitcnt lgkmcnt(0)\n"
    "v_pk_add_f32 v[16:17], v[16:17], v[56:57]\n"
    "v_pk_add_f32 v[98:99], v[98:99], v[38:39]\n"
    "v_pk_add_f32 v[18:19], v[18:19], v[58:59]\n"
    "v_pk_add_f32 v[16:17], v[16:17], v[96:97]\n"
    "v_pk_add_f32 v[18:19], v[18:19], v[98:99]\n"
    "v_pk_fma_f32 v[132:133], v[120:121], v[16:17], v[132:133]\n"
    "v_pk_fma_f32 v[134:135], v[122:123], v[18:19], v[134:135]\n"
    "v_cvt_pk_bf16_f32 v132, v132, v133\n"
    "v_cvt_pk_bf16_f32 v133, v134, v135\n"
    "buffer_store_dwordx2 v[132:133], v227, s[56:59], s38 offen offset:128\n"
    "s_waitcnt vmcnt(0) lgkmcnt(0)\n"
    "s_waitcnt vmcnt(0) lgkmcnt(0)\n"
    "s_endpgm\n"
    "K_END%=:\n"
    "PG15_%=:\n"
    "PG16_%=:\n"
    "PG17_%=:\n"
    "PG18_%=:\n"
    "s_mov_b64 exec, -1\n"
    "s_branch PGRET%=\n"
    ".p2align 3\n"
    "SLOW%=:\n"
    "s_waitcnt vmcnt(0) lgkmcnt(0)\n"
    "s_nop 15\n"
    "v_bfe_u32 v108, v255, 4, 4\n"
    "v_lshrrev_b32_e32 v110, 8, v255\n"
    "v_lshlrev_b32_e32 v109, 1, v108\n"
    "v_lshl_add_u32 v109, v110, 10, v109\n"
    "v_lshlrev_b32_e64 v108, 8, v108\n"
    "v_lshl_add_u32 v108, v110, 3, v108\n"
    "s_mov_b32 s65, 0\n"
    "v_mov_b32_e32 v144, 0\n"
    "v_mov_b32_e32 v145, 0\n"
    "v_mov_b32_e32 v146, 0\n"
    "v_mov_b32_e32 v147, 0\n"
    "v_mov_b32_e32 v148, 0\n"
    "v_mov_b32_e32 v149, 0\n"
    "v_mov_b32_e32 v150, 0\n"
    "v_mov_b32_e32 v151, 0\n"
    "v_mov_b32_e32 v152, 0\n"
    "v_mov_b32_e32 v153, 0\n"
    "v_mov_b32_e32 v154, 0\n"
    "v_mov_b32_e32 v155, 0\n"
    "v_mov_b32_e32 v156, 0\n"
    "v_mov_b32_e32 v157, 0\n"
    "v_mov_b32_e32 v158, 0\n"
    "v_mov_b32_e32 v159, 0\n"
    "v_mov_b32_e32 v192, 0\n"
    "v_mov_b32_e32 v193, 0\n"
    "v_mov_b32_e32 v194, 0\n"
    "v_mov_b32_e32 v195, 0\n"
    "v_mov_b32_e32 v196, 0\n"
    "v_mov_b32_e32 v197, 0\n"
    "v_mov_b32_e32 v198, 0\n"
    "v_mov_b32_e32 v199, 0\n"
    "v_mov_b32_e32 v200, 0\n"
    "v_mov_b32_e32 v201, 0\n"
    "v_mov_b32_e32 v202, 0\n"
    "v_mov_b32_e32 v203, 0\n"
    "v_mov_b32_e32 v204, 0\n"
    "v_mov_b32_e32 v205, 0\n"
    "v_mov_b32_e32 v206, 0\n"
    "v_mov_b32_e64 v207, 0\n"
    "v_mov_b32_e32 v104, 0xff800000\n"
    "v_mov_b32_e32 v105, 0xff800000\n"
    "v_mov_b32_e32 v106, 0xff800000\n"
    "v_mov_b32_e32 v107, 0xff800000\n"
    "v_mfma_f32_16x16x32_bf16 v[208:211], v[144:147], v[144:147], 0\n"
    "v_mfma_f32_16x16x32_bf16 v[212:215], v[144:147], v[144:147], 0\n"
    "v_mfma_f32_16x16x32_bf16 v[216:219], v[144:147], v[144:147], 0\n"
    "v_mfma_f32_16x16x32_bf16 v[220:223], v[144:147], v[144:147], 0\n"
    "v_mfma_f32_16x16x32_bf16 a[0:3], v[144:147], v[144:147], 0\n"
    "v_mfma_f32_16x16x32_bf16 a[4:7], v[144:147], v[144:147], 0\n"
    "v_mfma_f32_16x16x32_bf16 a[8:11], v[144:147], v[144:147], 0\n"
    "v_mfma_f32_16x16x32_bf16 a[12:15], v[144:147], v[144:147], 0\n"
    "v_mfma_f32_16x16x32_bf16 a[16:19], v[144:147], v[144:147], 0\n"
    "v_mfma_f32_16x16x32_bf16 a[20:23], v[144:147], v[144:147], 0\n"
    "v_mfma_f32_16x16x32_bf16 a[24:27], v[144:147], v[144:147], 0\n"
    "v_mfma_f32_16x16x32_bf16 a[28:31], v[144:147], v[144:147], 0\n"
    "v_mfma_f32_16x16x32_bf16 a[32:35], v[144:147], v[144:147], 0\n"
    "v_mfma_f32_16x16x32_bf16 a[36:39], v[144:147], v[144:147], 0\n"
    "v_mfma_f32_16x16x32_bf16 a[40:43], v[144:147], v[144:147], 0\n"
    "v_mfma_f32_16x16x32_bf16 a[44:47], v[144:147], v[144:147], 0\n"
    "v_mfma_f32_16x16x32_bf16 a[48:51], v[144:147], v[144:147], 0\n"
    "v_mfma_f32_16x16x32_bf16 a[52:55], v[144:147], v[144:147], 0\n"
    "v_mfma_f32_16x16x32_bf16 a[56:59], v[144:147], v[144:147], 0\n"
    "v_mfma_f32_16x16x32_bf16 a[60:63], v[144:147], v[144:147], 0\n"
    "v_mfma_f32_16x16x32_bf16 a[64:67], v[144:147], v[144:147], 0\n"
    "v_mfma_f32_16x16x32_bf16 a[68:71], v[144:147], v[144:147], 0\n"
    "v_mfma_f32_16x16x32_bf16 a[72:75], v[144:147], v[144:147], 0\n"
    "v_mfma_f32_16x16x32_bf16 a[76:79], v[144:147], v[144:147], 0\n"
    "v_mfma_f32_16x16x32_bf16 a[80:83], v[144:147], v[144:147], 0\n"
    "v_mfma_f32_16x16x32_bf16 a[84:87], v[144:147], v[144:147], 0\n"
    "v_mfma_f32_16x16x32_bf16 a[88:91], v[144:147], v[144:147], 0\n"
    "v_mfma_f32_16x16x32_bf16 a[92:95], v[144:147], v[144:147], 0\n"
    "v_mfma_f32_16x16x32_bf16 a[96:99], v[144:147], v[144:147], 0\n"
    "v_mfma_f32_16x16x32_bf16 a[100:103], v[144:147], v[144:147], 0\n"
    "v_mfma_f32_16x16x32_bf16 a[104:107], v[144:147], v[144:147], 0\n"
    "v_mfma_f32_16x16x32_bf16 a[108:111], v[144:147], v[144:147], 0\n"
    "v_mfma_f32_16x16x32_bf16 a[112:115], v[144:147], v[144:147], 0\n"
    "v_mfma_f32_16x16x32_bf16 a[116:119], v[144:147], v[144:147], 0\n"
    "v_mfma_f32_16x16x32_bf16 a[120:123], v[144:147], v[144:147], 0\n"
    "v_mfma_f32_16x16x32_bf16 a[124:127], v[144:147], v[144:147], 0\n"
    "SLOWP%=:\n"
    "s_lshl_b32 s62, s73, 19\n"
    "s_add_u32 s62, s62, 0x60000\n"
    "s_mov_b32 s64, 16\n"
    "SLOWL%=:\n"
    "s_add_u32 s63, s62, 0x1000\n"
    "buffer_load_dwordx2 a[192:193], v108, s[40:43], s62 offen\n"
    "buffer_load_dwordx2 a[194:195], v108, s[40:43], s62 offen offset:32\n"
    "buffer_load_dwordx2 a[196:197], v108, s[40:43], s62 offen offset:64\n"
    "buffer_load_dwordx2 a[198:199], v108, s[40:43], s62 offen offset:96\n"
    "buffer_load_dwordx2 a[200:201], v108, s[40:43], s62 offen offset:128\n"
    "buffer_load_dwordx2 a[202:203], v108, s[40:43], s62 offen offset:160\n"
    "buffer_load_dwordx2 a[204:205], v108, s[40:43], s62 offen offset:192\n"
    "buffer_load_dwordx2 a[206:207], v108, s[40:43], s62 offen offset:224\n"
    "buffer_load_dwordx2 a[208:209], v108, s[40:43], s63 offen\n"
    "buffer_load_dwordx2 a[210:211], v108, s[40:43], s63 offen offset:32\n"
    "buffer_load_dwordx2 a[212:213], v108, s[40:43], s63 offen offset:64\n"
    "buffer_load_dwordx2 a[214:215], v108, s[40:43], s63 offen offset:96\n"
    "buffer_load_dwordx2 a[216:217], v108, s[40:43], s63 offen offset:128\n"
    "buffer_load_dwordx2 a[218:219], v108, s[40:43], s63 offen offset:160\n"
    "buffer_load_dwordx2 a[220:221], v108, s[40:43], s63 offen offset:192\n"
    "buffer_load_dwordx2 a[222:223], v108, s[40:43], s63 offen offset:224\n"
    "s_waitcnt vmcnt(14)\n"
    "v_mfma_f32_16x16x32_bf16 v[112:115], a[192:195], v[0:3], v[192:195]\n"
    "s_waitcnt vmcnt(12)\n"
    "v_mfma_f32_16x16x32_bf16 v[112:115], a[196:199], v[4:7], v[112:115]\n"
    "s_waitcnt vmcnt(10)\n"
    "v_mfma_f32_16x16x32_bf16 v[112:115], a[200:203], v[8:11], v[112:115]\n"
    "s_waitcnt vmcnt(8)\n"
    "v_mfma_f32_16x16x32_bf16 v[112:115], a[204:207], v[12:15], v[112:115]\n"
    "s_waitcnt vmcnt(6)\n"
    "v_mfma_f32_16x16x32_bf16 v[116:119], a[208:211], v[0:3], v[192:195]\n"
    "s_waitcnt vmcnt(4)\n"
    "v_mfma_f32_16x16x32_bf16 v[116:119], a[212:215], v[4:7], v[116:119]\n"
    "s_waitcnt vmcnt(2)\n"
    "v_mfma_f32_16x16x32_bf16 v[116:119], a[216:219], v[8:11], v[116:119]\n"
    "s_waitcnt vmcnt(0)\n"
    "v_mfma_f32_16x16x32_bf16 v[116:119], a[220:223], v[12:15], v[116:119]\n"
    "v_mfma_f32_16x16x32_bf16 v[120:123], a[192:195], v[16:19], v[196:199]\n"
    "v_mfma_f32_16x16x32_bf16 v[120:123], a[196:199], v[20:23], v[120:123]\n"
    "v_mfma_f32_16x16x32_bf16 v[120:123], a[200:203], v[24:27], v[120:123]\n"
    "v_mfma_f32_16x16x32_bf16 v[120:123], a[204:207], v[28:31], v[120:123]\n"
    "v_mfma_f32_16x16x32_bf16 v[124:127], a[208:211], v[16:19], v[196:199]\n"
    "v_mfma_f32_16x16x32_bf16 v[124:127], a[212:215], v[20:23], v[124:127]\n"
    "v_mfma_f32_16x16x32_bf16 v[124:127], a[216:219], v[24:27], v[124:127]\n"
    "v_mfma_f32_16x16x32_bf16 v[124:127], a[220:223], v[28:31], v[124:127]\n"
    "v_mfma_f32_16x16x32_bf16 v[128:131], a[192:195], v[32:35], v[200:203]\n"
    "v_mfma_f32_16x16x32_bf16 v[128:131], a[196:199], v[36:39], v[128:131]\n"
    "v_mfma_f32_16x16x32_bf16 v[128:131], a[200:203], v[40:43], v[128:131]\n"
    "v_mfma_f32_16x16x32_bf16 v[128:131], a[204:207], v[44:47], v[128:131]\n"
    "v_mfma_f32_16x16x32_bf16 v[132:135], a[208:211], v[32:35], v[200:203]\n"
    "v_mfma_f32_16x16x32_bf16 v[132:135], a[212:215], v[36:39], v[132:135]\n"
    "v_mfma_f32_16x16x32_bf16 v[132:135], a[216:219], v[40:43], v[132:135]\n"
    "v_mfma_f32_16x16x32_bf16 v[132:135], a[220:223], v[44:47], v[132:135]\n"
    "v_mfma_f32_16x16x32_bf16 v[136:139], a[192:195], v[48:51], v[204:207]\n"
    "v_mfma_f32_16x16x32_bf16 v[136:139], a[196:199], v[52:55], v[136:139]\n"
    "v_mfma_f32_16x16x32_bf16 v[136:139], a[200:203], v[56:59], v[136:139]\n"
    "v_mfma_f32_16x16x32_bf16 v[136:139], a[204:207], v[60:63], v[136:139]\n"
    "v_mfma_f32_16x16x32_bf16 v[140:143], a[208:211], v[48:51], v[204:207]\n"
    "v_mfma_f32_16x16x32_bf16 v[140:143], a[212:215], v[52:55], v[140:143]\n"
    "v_mfma_f32_16x16x32_bf16 v[140:143], a[216:219], v[56:59], v[140:143]\n"
    "v_mfma_f32_16x16x32_bf16 v[140:143], a[220:223], v[60:63], v[140:143]\n"
    "s_nop 9\n"
    "s_cmp_eq_u32 s65, 0\n"
    "s_cbranch_scc0 SLOW1%=\n"
    "v_max3_f32 v104, v104, v112, v113\n"
    "v_max3_f32 v104, v104, v114, v115\n"
    "v_max3_f32 v104, v104, v116, v117\n"
    "v_max3_f32 v104, v104, v118, v119\n"
    "v_max3_f32 v105, v105, v120, v121\n"
    "v_max3_f32 v105, v105, v122, v123\n"
    "v_max3_f32 v105, v105, v124, v125\n"
    "v_max3_f32 v105, v105, v126, v127\n"
    "v_max3_f32 v106, v106, v128, v129\n"
    "v_max3_f32 v106, v106, v130, v131\n"
    "v_max3_f32 v106, v106, v132, v133\n"
    "v_max3_f32 v106, v106, v134, v135\n"
    "v_max3_f32 v107, v107, v136, v137\n"
    "v_max3_f32 v107, v107, v138, v139\n"
    "v_max3_f32 v107, v107, v140, v141\n"
    "v_max3_f32 v107, v107, v142, v143\n"
    "s_branch SLOWN%=\n"
    "SLOW1%=:\n"
    "v_exp_f32_e32 v112, v112\n"
    "v_exp_f32_e32 v113, v113\n"
    "v_exp_f32_e32 v114, v114\n"
    "v_exp_f32_e32 v115, v115\n"
    "v_exp_f32_e32 v116, v116\n"
    "v_exp_f32_e32 v117, v117\n"
    "v_exp_f32_e32 v118, v118\n"
    "v_exp_f32_e32 v119, v119\n"
    "v_exp_f32_e32 v120, v120\n"
    "v_exp_f32_e32 v121, v121\n"
    "v_exp_f32_e32 v122, v122\n"
    "v_exp_f32_e32 v123, v123\n"
    "v_exp_f32_e32 v124, v124\n"
    "v_exp_f32_e32 v125, v125\n"
    "v_exp_f32_e32 v126, v126\n"
    "v_exp_f32_e32 v127, v127\n"
    "v_exp_f32_e32 v128, v128\n"
    "v_exp_f32_e32 v129, v129\n"
    "v_exp_f32_e32 v130, v130\n"
    "v_exp_f32_e32 v131, v131\n"
    "v_exp_f32_e32 v132, v132\n"
    "v_exp_f32_e32 v133, v133\n"
    "v_exp_f32_e32 v134, v134\n"
    "v_exp_f32_e32 v135, v135\n"
    "v_exp_f32_e32 v136, v136\n"
    "v_exp_f32_e32 v137, v137\n"
    "v_exp_f32_e32 v138, v138\n"
    "v_exp_f32_e32 v139, v139\n"
    "v_exp_f32_e32 v140, v140\n"
    "v_exp_f32_e32 v141, v141\n"
    "v_exp_f32_e32 v142, v142\n"
    "v_exp_f32_e32 v143, v143\n"
    "v_cvt_pk_bf16_f32 v144, v112, v113\n"
    "v_cvt_pk_bf16_f32 v145, v114, v115\n"
    "v_cvt_pk_bf16_f32 v146, v116, v117\n"
    "v_cvt_pk_bf16_f32 v147, v118, v119\n"
    "v_cvt_pk_bf16_f32 v148, v120, v121\n"
    "v_cvt_pk_bf16_f32 v149, v122, v123\n"
    "v_cvt_pk_bf16_f32 v150, v124, v125\n"
    "v_cvt_pk_bf16_f32 v151, v126, v127\n"
    "v_cvt_pk_bf16_f32 v152, v128, v129\n"
    "v_cvt_pk_bf16_f32 v153, v130, v131\n"
    "v_cvt_pk_bf16_f32 v154, v132, v133\n"
    "v_cvt_pk_bf16_f32 v155, v134, v135\n"
    "v_cvt_pk_bf16_f32 v156, v136, v137\n"
    "v_cvt_pk_bf16_f32 v157, v138, v139\n"
    "v_cvt_pk_bf16_f32 v158, v140, v141\n"
    "v_cvt_pk_bf16_f32 v159, v142, v143\n"
    "v_mfma_f32_16x16x32_bf16 v[208:211], v[224:227], v[144:147], v[208:211]\n"
    "v_mfma_f32_16x16x32_bf16 v[212:215], v[224:227], v[148:151], v[212:215]\n"
    "v_mfma_f32_16x16x32_bf16 v[216:219], v[224:227], v[152:155], v[216:219]\n"
    "v_mfma_f32_16x16x32_bf16 v[220:223], v[224:227], v[156:159], v[220:223]\n"
    "buffer_load_ushort v68, v109, s[44:47], s62 offen offset:0\n"
    "buffer_load_ushort v69, v109, s[44:47], s62 offen offset:256\n"
    "buffer_load_ushort v70, v109, s[44:47], s62 offen offset:512\n"
    "buffer_load_ushort v71, v109, s[44:47], s62 offen offset:768\n"
    "buffer_load_ushort v72, v109, s[44:47], s63 offen offset:0\n"
    "buffer_load_ushort v73, v109, s[44:47], s63 offen offset:256\n"
    "buffer_load_ushort v74, v109, s[44:47], s63 offen offset:512\n"
    "buffer_load_ushort v75, v109, s[44:47], s63 offen offset:768\n"
    "s_waitcnt vmcnt(6)\n"
    "v_lshl_or_b32 v64, v69, 16, v68\n"
    "s_waitcnt vmcnt(4)\n"
    "v_lshl_or_b32 v65, v71, 16, v70\n"
    "s_waitcnt vmcnt(2)\n"
    "v_lshl_or_b32 v66, v73, 16, v72\n"
    "s_waitcnt vmcnt(0)\n"
    "v_lshl_or_b32 v67, v75, 16, v74\n"
    "s_nop 1\n"
    "v_mfma_f32_16x16x32_bf16 a[0:3], v[64:67], v[144:147], a[0:3]\n"
    "v_mfma_f32_16x16x32_bf16 a[4:7], v[64:67], v[148:151], a[4:7]\n"
    "v_mfma_f32_16x16x32_bf16 a[8:11], v[64:67], v[152:155], a[8:11]\n"
    "v_mfma_f32_16x16x32_bf16 a[12:15], v[64:67], v[156:159], a[12:15]\n"
    "buffer_load_ushort v68, v109, s[44:47], s62 offen offset:32\n"
    "buffer_load_ushort v69, v109, s[44:47], s62 offen offset:288\n"
    "buffer_load_ushort v70, v109, s[44:47], s62 offen offset:544\n"
    "buffer_load_ushort v71, v109, s[44:47], s62 offen offset:800\n"
    "buffer_load_ushort v72, v109, s[44:47], s63 offen offset:32\n"
    "buffer_load_ushort v73, v109, s[44:47], s63 offen offset:288\n"
    "buffer_load_ushort v74, v109, s[44:47], s63 offen offset:544\n"
    "buffer_load_ushort v75, v109, s[44:47], s63 offen offset:800\n"
    "s_waitcnt vmcnt(6)\n"
    "v_lshl_or_b32 v64, v69, 16, v68\n"
    "s_waitcnt vmcnt(4)\n"
    "v_lshl_or_b32 v65, v71, 16, v70\n"
    "s_waitcnt vmcnt(2)\n"
    "v_lshl_or_b32 v66, v73, 16, v72\n"
    "s_waitcnt vmcnt(0)\n"
    "v_lshl_or_b32 v67, v75, 16, v74\n"
    "s_nop 1\n"
    "v_mfma_f32_16x16x32_bf16 a[16:19], v[64:67], v[144:147], a[16:19]\n"
    "v_mfma_f32_16x16x32_bf16 a[20:23], v[64:67], v[148:151], a[20:23]\n"
    "v_mfma_f32_16x16x32_bf16 a[24:27], v[64:67], v[152:155], a[24:27]\n"
    "v_mfma_f32_16x16x32_bf16 a[28:31], v[64:67], v[156:159], a[28:31]\n"
    "buffer_load_ushort v68, v109, s[44:47], s62 offen offset:64\n"
    "buffer_load_ushort v69, v109, s[44:47], s62 offen offset:320\n"
    "buffer_load_ushort v70, v109, s[44:47], s62 offen offset:576\n"
    "buffer_load_ushort v71, v109, s[44:47], s62 offen offset:832\n"
    "buffer_load_ushort v72, v109, s[44:47], s63 offen offset:64\n"
    "buffer_load_ushort v73, v109, s[44:47], s63 offen offset:320\n"
    "buffer_load_ushort v74, v109, s[44:47], s63 offen offset:576\n"
    "buffer_load_ushort v75, v109, s[44:47], s63 offen offset:832\n"
    "s_waitcnt vmcnt(6)\n"
    "v_lshl_or_b32 v64, v69, 16, v68\n"
    "s_waitcnt vmcnt(4)\n"
    "v_lshl_or_b32 v65, v71, 16, v70\n"
    "s_waitcnt vmcnt(2)\n"
    "v_lshl_or_b32 v66, v73, 16, v72\n"
    "s_waitcnt vmcnt(0)\n"
    "v_lshl_or_b32 v67, v75, 16, v74\n"
    "s_nop 1\n"
    "v_mfma_f32_16x16x32_bf16 a[32:35], v[64:67], v[144:147], a[32:35]\n"
    "v_mfma_f32_16x16x32_bf16 a[36:39], v[64:67], v[148:151], a[36:39]\n"
    "v_mfma_f32_16x16x32_bf16 a[40:43], v[64:67], v[152:155], a[40:43]\n"
    "v_mfma_f32_16x16x32_bf16 a[44:47], v[64:67], v[156:159], a[44:47]\n"
    "buffer_load_ushort v68, v109, s[44:47], s62 offen offset:96\n"
    "buffer_load_ushort v69, v109, s[44:47], s62 offen offset:352\n"
    "buffer_load_ushort v70, v109, s[44:47], s62 offen offset:608\n"
    "buffer_load_ushort v71, v109, s[44:47], s62 offen offset:864\n"
    "buffer_load_ushort v72, v109, s[44:47], s63 offen offset:96\n"
    "buffer_load_ushort v73, v109, s[44:47], s63 offen offset:352\n"
    "buffer_load_ushort v74, v109, s[44:47], s63 offen offset:608\n"
    "buffer_load_ushort v75, v109, s[44:47], s63 offen offset:864\n"
    "s_waitcnt vmcnt(6)\n"
    "v_lshl_or_b32 v64, v69, 16, v68\n"
    "s_waitcnt vmcnt(4)\n"
    "v_lshl_or_b32 v65, v71, 16, v70\n"
    "s_waitcnt vmcnt(2)\n"
    "v_lshl_or_b32 v66, v73, 16, v72\n"
    "s_waitcnt vmcnt(0)\n"
    "v_lshl_or_b32 v67, v75, 16, v74\n"
    "s_nop 1\n"
    "v_mfma_f32_16x16x32_bf16 a[48:51], v[64:67], v[144:147], a[48:51]\n"
    "v_mfma_f32_16x16x32_bf16 a[52:55], v[64:67], v[148:151], a[52:55]\n"
    "v_mfma_f32_16x16x32_bf16 a[56:59], v[64:67], v[152:155], a[56:59]\n"
    "v_mfma_f32_16x16x32_bf16 a[60:63], v[64:67], v[156:159], a[60:63]\n"
    "buffer_load_ushort v68, v109, s[44:47], s62 offen offset:128\n"
    "buffer_load_ushort v69, v109, s[44:47], s62 offen offset:384\n"
    "buffer_load_ushort v70, v109, s[44:47], s62 offen offset:640\n"
    "buffer_load_ushort v71, v109, s[44:47], s62 offen offset:896\n"
    "buffer_load_ushort v72, v109, s[44:47], s63 offen offset:128\n"
    "buffer_load_ushort v73, v109, s[44:47], s63 offen offset:384\n"
    "buffer_load_ushort v74, v109, s[44:47], s63 offen offset:640\n"
    "buffer_load_ushort v75, v109, s[44:47], s63 offen offset:896\n"
    "s_waitcnt vmcnt(6)\n"
    "v_lshl_or_b32 v64, v69, 16, v68\n"
    "s_waitcnt vmcnt(4)\n"
    "v_lshl_or_b32 v65, v71, 16, v70\n"
    "s_waitcnt vmcnt(2)\n"
    "v_lshl_or_b32 v66, v73, 16, v72\n"
    "s_waitcnt vmcnt(0)\n"
    "v_lshl_or_b32 v67, v75, 16, v74\n"
    "s_nop 1\n"
    "v_mfma_f32_16x16x32_bf16 a[64:67], v[64:67], v[144:147], a[64:67]\n"
    "v_mfma_f32_16x16x32_bf16 a[68:71], v[64:67], v[148:151], a[68:71]\n"
    "v_mfma_f32_16x16x32_bf16 a[72:75], v[64:67], v[152:155], a[72:75]\n"
    "v_mfma_f32_16x16x32_bf16 a[76:79], v[64:67], v[156:159], a[76:79]\n"
    "buffer_load_ushort v68, v109, s[44:47], s62 offen offset:160\n"
    "buffer_load_ushort v69, v109, s[44:47], s62 offen offset:416\n"
    "buffer_load_ushort v70, v109, s[44:47], s62 offen offset:672\n"
    "buffer_load_ushort v71, v109, s[44:47], s62 offen offset:928\n"
    "buffer_load_ushort v72, v109, s[44:47], s63 offen offset:160\n"
    "buffer_load_ushort v73, v109, s[44:47], s63 offen offset:416\n"
    "buffer_load_ushort v74, v109, s[44:47], s63 offen offset:672\n"
    "buffer_load_ushort v75, v109, s[44:47], s63 offen offset:928\n"
    "s_waitcnt vmcnt(6)\n"
    "v_lshl_or_b32 v64, v69, 16, v68\n"
    "s_waitcnt vmcnt(4)\n"
    "v_lshl_or_b32 v65, v71, 16, v70\n"
    "s_waitcnt vmcnt(2)\n"
    "v_lshl_or_b32 v66, v73, 16, v72\n"
    "s_waitcnt vmcnt(0)\n"
    "v_lshl_or_b32 v67, v75, 16, v74\n"
    "s_nop 1\n"
    "v_mfma_f32_16x16x32_bf16 a[80:83], v[64:67], v[144:147], a[80:83]\n"
    "v_mfma_f32_16x16x32_bf16 a[84:87], v[64:67], v[148:151], a[84:87]\n"
    "v_mfma_f32_16x16x32_bf16 a[88:91], v[64:67], v[152:155], a[88:91]\n"
    "v_mfma_f32_16x16x32_bf16 a[92:95], v[64:67], v[156:159], a[92:95]\n"
    "buffer_load_ushort v68, v109, s[44:47], s62 offen offset:192\n"
    "buffer_load_ushort v69, v109, s[44:47], s62 offen offset:448\n"
    "buffer_load_ushort v70, v109, s[44:47], s62 offen offset:704\n"
    "buffer_load_ushort v71, v109, s[44:47], s62 offen offset:960\n"
    "buffer_load_ushort v72, v109, s[44:47], s63 offen offset:192\n"
    "buffer_load_ushort v73, v109, s[44:47], s63 offen offset:448\n"
    "buffer_load_ushort v74, v109, s[44:47], s63 offen offset:704\n"
    "buffer_load_ushort v75, v109, s[44:47], s63 offen offset:960\n"
    "s_waitcnt vmcnt(6)\n"
    "v_lshl_or_b32 v64, v69, 16, v68\n"
    "s_waitcnt vmcnt(4)\n"
    "v_lshl_or_b32 v65, v71, 16, v70\n"
    "s_waitcnt vmcnt(2)\n"
    "v_lshl_or_b32 v66, v73, 16, v72\n"
    "s_waitcnt vmcnt(0)\n"
    "v_lshl_or_b32 v67, v75, 16, v74\n"
    "s_nop 1\n"
    "v_mfma_f32_16x16x32_bf16 a[96:99], v[64:67], v[144:147], a[96:99]\n"
    "v_mfma_f32_16x16x32_bf16 a[100:103], v[64:67], v[148:151], a[100:103]\n"
    "v_mfma_f32_16x16x32_bf16 a[104:107], v[64:67], v[152:155], a[104:107]\n"
    "v_mfma_f32_16x16x32_bf16 a[108:111], v[64:67], v[156:159], a[108:111]\n"
    "buffer_load_ushort v68, v109, s[44:47], s62 offen offset:224\n"
    "buffer_load_ushort v69, v109, s[44:47], s62 offen offset:480\n"
    "buffer_load_ushort v70, v109, s[44:47], s62 offen offset:736\n"
    "buffer_load_ushort v71, v109, s[44:47], s62 offen offset:992\n"
    "buffer_load_ushort v72, v109, s[44:47], s63 offen offset:224\n"
    "buffer_load_ushort v73, v109, s[44:47], s63 offen offset:480\n"
    "buffer_load_ushort v74, v109, s[44:47], s63 offen offset:736\n"
    "buffer_load_ushort v75, v109, s[44:47], s63 offen offset:992\n"
    "s_waitcnt vmcnt(6)\n"
    "v_lshl_or_b32 v64, v69, 16, v68\n"
    "s_waitcnt vmcnt(4)\n"
    "v_lshl_or_b32 v65, v71, 16, v70\n"
    "s_waitcnt vmcnt(2)\n"
    "v_lshl_or_b32 v66, v73, 16, v72\n"
    "s_waitcnt vmcnt(0)\n"
    "v_lshl_or_b32 v67, v75, 16, v74\n"
    "s_nop 1\n"
    "v_mfma_f32_16x16x32_bf16 a[112:115], v[64:67], v[144:147], a[112:115]\n"
    "v_mfma_f32_16x16x32_bf16 a[116:119], v[64:67], v[148:151], a[116:119]\n"
    "v_mfma_f32_16x16x32_bf16 a[120:123], v[64:67], v[152:155], a[120:123]\n"
    "v_mfma_f32_16x16x32_bf16 a[124:127], v[64:67], v[156:159], a[124:127]\n"
    "SLOWN%=:\n"
    "s_add_u32 s62, s62, 0x2000\n"
    "s_sub_u32 s64, s64, 1\n"
    "s_cmp_lg_u32 s64, 0\n"
    "s_cbranch_scc1 SLOWL%=\n"
    "s_nop 9\n"
    "s_cmp_eq_u32 s65, 0\n"
    "s_cbranch_scc0 SLOWE%=\n"
    "v_mov_b32_e32 v110, v104\n"
    "s_nop 1\n"
    "v_permlane32_swap_b32 v104, v110\n"
    "v_max_f32_e32 v104, v104, v110\n"
    "v_mov_b32_e32 v110, v104\n"
    "s_nop 1\n"
    "v_permlane16_swap_b32 v104, v110\n"
    "v_max_f32_e32 v104, v104, v110\n"
    "v_sub_f32_e32 v192, 0, v104\n"
    "v_sub_f32_e32 v193, 0, v104\n"
    "v_sub_f32_e32 v194, 0, v104\n"
    "v_sub_f32_e32 v195, 0, v104\n"
    "v_mov_b32_e32 v110, v105\n"
    "s_nop 1\n"
    "v_permlane32_swap_b32 v105, v110\n"
    "v_max_f32_e32 v105, v105, v110\n"
    "v_mov_b32_e32 v110, v105\n"
    "s_nop 1\n"
    "v_permlane16_swap_b32 v105, v110\n"
    "v_max_f32_e32 v105, v105, v110\n"
    "v_sub_f32_e32 v196, 0, v105\n"
    "v_sub_f32_e32 v197, 0, v105\n"
    "v_sub_f32_e32 v198, 0, v105\n"
    "v_sub_f32_e32 v199, 0, v105\n"
    "v_mov_b32_e32 v110, v106\n"
    "s_nop 1\n"
    "v_permlane32_swap_b32 v106, v110\n"
    "v_max_f32_e32 v106, v106, v110\n"
    "v_mov_b32_e32 v110, v106\n"
    "s_nop 1\n"
    "v_permlane16_swap_b32 v106, v110\n"
    "v_max_f32_e32 v106, v106, v110\n"
    "v_sub_f32_e32 v200, 0, v106\n"
    "v_sub_f32_e32 v201, 0, v106\n"
    "v_sub_f32_e32 v202, 0, v106\n"
    "v_sub_f32_e32 v203, 0, v106\n"
    "v_mov_b32_e32 v110, v107\n"
    "s_nop 1\n"
    "v_permlane32_swap_b32 v107, v110\n"
    "v_max_f32_e32 v107, v107, v110\n"
    "v_mov_b32_e32 v110, v107\n"
    "s_nop 1\n"
    "v_permlane16_swap_b32 v107, v110\n"
    "v_max_f32_e32 v107, v107, v110\n"
    "v_sub_f32_e32 v204, 0, v107\n"
    "v_sub_f32_e32 v205, 0, v107\n"
    "v_sub_f32_e32 v206, 0, v107\n"
    "v_sub_f32_e32 v207, 0, v107\n"
    "s_mov_b32 s65, 1\n"
    "s_branch SLOWP%=\n"
    "SLOWE%=:\n"
    "v_mov_b32_e32 v144, 0\n"
    "v_mov_b32_e32 v145, 0\n"
    "v_mov_b32_e32 v146, 0\n"
    "v_mov_b32_e32 v147, 0\n"
    "v_mov_b32_e32 v148, 0\n"
    "v_mov_b32_e32 v149, 0\n"
    "v_mov_b32_e32 v150, 0\n"
    "v_mov_b32_e32 v151, 0\n"
    "v_mov_b32_e32 v152, 0\n"
    "v_mov_b32_e32 v153, 0\n"
    "v_mov_b32_e32 v154, 0\n"
    "v_mov_b32_e32 v155, 0\n"
    "v_mov_b32_e32 v156, 0\n"
    "v_mov_b32_e32 v157, 0\n"
    "v_mov_b32_e32 v158, 0\n"
    "v_mov_b32_e32 v159, 0\n"
    "s_nop 15\n"
    "s_branch BACK%=\n"
    :: "s"(w), "s"(blockIdx.x), "s"(__builtin_amdgcn_kernarg_segment_ptr()), "s"(g_jobs.v) : "v0", "v1", "v2", "v3", "v4", "v5", "v6", "v7", "v8", "v9", "v10", "v11", "v12", "v13", "v14", "v15", "v16", "v17", "v18", "v19", "v20", "v21", "v22", "v23", "v24", "v25", "v26", "v27", "v28", "v29", "v30", "v31", "v32", "v33", "v34", "v35", "v36", "v37", "v38", "v39", "v40", "v41", "v42", "v43", "v44", "v45", "v46", "v47", "v48", "v49", "v50", "v51", "v52", "v53", "v54", "v55", "v56", "v57", "v58", "v59", "v60", "v61", "v62", "v63", "v64", "v65", "v66", "v67", "v68", "v69", "v70", "v71", "v72", "v73", "v74", "v75", "v76", "v77", "v78", "v79", "v80", "v81", "v82", "v83", "v84", "v85", "v86", "v87", "v88", "v89", "v90", "v91", "v92", "v93", "v94", "v95", "v96", "v97", "v98", "v99", "v100", "v101", "v102", "v103", "v104", "v105", "v106", "v107", "v108", "v109", "v110", "v111", "v112", "v113", "v114", "v115", "v116", "v117", "v118", "v119", "v120", "v121", "v122", "v123", "v124", "v125", "v126", "v127", "v128", "v129", "v130", "v131", "v132", "v133", "v134", "v135", "v136", "v137", "v138", "v139", "v140", "v141", "v142", "v143", "v144", "v145", "v146", "v147", "v148", "v149", "v150", "v151", "v152", "v153", "v154", "v155", "v156", "v157", "v158", "v159", "v160", "v161", "v162", "v163", "v164", "v165", "v166", "v167", "v168", "v169", "v170", "v171", "v172", "v173", "v174", "v175", "v176", "v177", "v178", "v179", "v180", "v181", "v182", "v183", "v184", "v185", "v186", "v187", "v188", "v189", "v190", "v191", "v192", "v193", "v194", "v195", "v196", "v197", "v198", "v199", "v200", "v201", "v202", "v203", "v204", "v205", "v206", "v207", "v208", "v209", "v210", "v211", "v212", "v213", "v214", "v215", "v216", "v217", "v218", "v219", "v220", "v221", "v222", "v223", "v224", "v225", "v226", "v227", "v228", "v229", "v230", "v231", "v232", "v233", "v234", "v235", "v236", "v237", "v238", "v239", "v240", "v241", "v242", "v243", "v244", "v245", "v246", "v247", "v248", "v249", "v250", "v251", "v252", "v253", "v254", "v255", "a0", "a1", "a2", "a3", "a4", "a5", "a6", "a7", "a8", "a9", "a10", "a11", "a12", "a13", "a14", "a15", "a16", "a17", "a18", "a19", "a20", "a21", "a22", "a23", "a24", "a25", "a26", "a27", "a28", "a29", "a30", "a31", "a32", "a33", "a34", "a35", "a36", "a37", "a38", "a39", "a40", "a41", "a42", "a43", "a44", "a45", "a46", "a47", "a48", "a49", "a50", "a51", "a52", "a53", "a54", "a55", "a56", "a57", "a58", "a59", "a60", "a61", "a62", "a63", "a64", "a65", "a66", "a67", "a68", "a69", "a70", "a71", "a72", "a73", "a74", "a75", "a76", "a77", "a78", "a79", "a80", "a81", "a82", "a83", "a84", "a85", "a86", "a87", "a88", "a89", "a90", "a91", "a92", "a93", "a94", "a95", "a96", "a97", "a98", "a99", "a100", "a101", "a102", "a103", "a104", "a105", "a106", "a107", "a108", "a109", "a110", "a111", "a112", "a113", "a114", "a115", "a116", "a117", "a118", "a119", "a120", "a121", "a122", "a123", "a124", "a125", "a126", "a127", "a128", "a129", "a130", "a131", "a132", "a133", "a134", "a135", "a136", "a137", "a138", "a139", "a140", "a141", "a142", "a143", "a144", "a145", "a146", "a147", "a148", "a149", "a150", "a151", "a152", "a153", "a154", "a155", "a156", "a157", "a158", "a159", "a160", "a161", "a162", "a163", "a164", "a165", "a166", "a167", "a168", "a169", "a170", "a171", "a172", "a173", "a174", "a175", "a176", "a177", "a178", "a179", "a180", "a181", "a182", "a183", "a184", "a185", "a186", "a187", "a188", "a189", "a190", "a191", "a192", "a193", "a194", "a195", "a196", "a197", "a198", "a199", "a200", "a201", "a202", "a203", "a204", "a205", "a206", "a207", "a208", "a209", "a210", "a211", "a212", "a213", "a214", "a215", "a216", "a217", "a218", "a219", "a220", "a221", "a222", "a223", "a224", "a225", "a226", "a227", "a228", "a229", "a230", "a231", "a232", "a233", "a234", "a235", "a236", "a237", "a238", "a239", "a240", "a241", "a242", "a243", "a244", "a245", "a246", "a247", "a248", "a249", "a250", "a251", "a252", "a253", "a254", "a255", "s8", "s9", "s10", "s11", "s12", "s13", "s14", "s15", "s16", "s17", "s18", "s19", "s20", "s21", "s22", "s23", "s24", "s25", "s26", "s27", "s28", "s29", "s30", "s31", "s34", "s35", "s36", "s37", "s38", "s39", "s40", "s41", "s42", "s43", "s44", "s45", "s46", "s47", "s48", "s49", "s50", "s51", "s52", "s53", "s54", "s55", "s56", "s57", "s58", "s59", "s60", "s61", "s62", "s63", "s64", "s65", "s66", "s67", "s68", "s69", "s70", "s71", "s72", "s73", "s74", "s75", "s76", "s77", "s78", "s79", "s80", "s81", "s82", "s83", "s84", "s85", "s86", "s87", "s88", "s89", "s90", "s91", "s92", "s93", "s94", "s95", "s96", "s97", "s98", "s99", "vcc", "scc", "memory");
}

extern "C" void kernel_launch(void* const* d_in, const int* in_sizes, int n_in, void* d_out, int out_size, void* d_ws,
                              size_t ws_size, hipStream_t stream) {
  k_block<<<256, 256, 0, stream>>>((const u16*)d_in[0], (const u16*)d_in[1], (const u16*)d_in[2], (const u16*)d_in[3],
                                   (const u16*)d_in[4], (const u16*)d_in[6], (const u16*)d_in[7], (const u16*)d_in[8],
                                   (const u16*)d_in[9], (const u16*)d_in[10], (char*)d_ws, (u16*)d_out);
}
```
